```python
import torch

def _softmax(x, axis=-1):
    return torch.softmax(x, dim=axis)
def _gelu(x):
    return torch.nn.functional.gelu(x, approximate="tanh")
def _silu(x):
    return torch.nn.functional.silu(x)
def _sigmoid(x):
    return torch.sigmoid(x)
def _layernorm(x, axis=-1, eps=1e-5):
    m = x.mean(dim=axis, keepdim=True)
    v = x.var(dim=axis, keepdim=True, unbiased=False)
    return (x - m) / torch.sqrt(v + eps)
def _scatter_add(tgt, idx, src, axis=0):
    return tgt.clone().index_add_(axis, idx, src)
def _segment_sum(src, seg, n):
    out = torch.zeros((n,) + src.shape[1:], dtype=src.dtype)
    return out.index_add_(0, seg, src)
def _pad(x, pw):
    flat = tuple(v for pair in reversed(pw) for v in pair)
    return torch.nn.functional.pad(x, flat)

kernel_name = 'recurrent_scan__sliding_local_dilated__swiglu_ffn__prompt'

def setup_inputs():

    _w = (lambda a: (lambda t: t.to(torch.bfloat16) if t.is_floating_point() else t)(torch.as_tensor(a)))
    def _A_setup():
        B, S, D = 1, 2048, 576
        return {
            "x": _w((lambda *s: torch.randn(*s, dtype=torch.bfloat16) * 0.05)(B, S, D)),
            "wqkv": _w((lambda *s: torch.randn(*s, dtype=torch.bfloat16) * 0.05)(D, 3 * D)),
            "wo": _w((lambda *s: torch.randn(*s, dtype=torch.bfloat16) * 0.05)(D, D)),
        }
    _A_in = _A_setup()

    def _B_setup():
        B, S, D, F = 1, 2048, 576, 4608
        return {
            "x": _w((lambda *s: torch.randn(*s, dtype=torch.bfloat16) * 0.05)(B, S, D)),
            "w_gate": _w((lambda *s: torch.randn(*s, dtype=torch.bfloat16) * 0.05)(D, F)),
            "w_up": _w((lambda *s: torch.randn(*s, dtype=torch.bfloat16) * 0.05)(D, F)),
            "w_down": _w((lambda *s: torch.randn(*s, dtype=torch.bfloat16) * 0.05)(F, D)),
        }
    _B_in = _B_setup()

    return {
        "A_x": _A_in["x"],
        "A_wqkv": _A_in["wqkv"],
        "A_wo": _A_in["wo"],
        "B_x": _B_in["x"],
        "B_w_gate": _B_in["w_gate"],
        "B_w_up": _B_in["w_up"],
        "B_w_down": _B_in["w_down"],
    }

def reference(A_x, A_wqkv, A_wo, B_x, B_w_gate, B_w_up, B_w_down):
    def _A_ref(x, wqkv, wo):
        B, S, D = x.shape
        H = 32; Dh = D // H
        W, dil = 8, 2
        qkv = (lambda x, *d: x.permute(*d))((x @ wqkv).reshape(B, S, 3, H, Dh), 2, 0, 3, 1, 4)
        q, k, v = qkv[0], qkv[1], qkv[2]
        scale = Dh ** -0.5
        pos = torch.arange(S)
        off = torch.arange(W) * dil
        idx = pos[:, None] - off[None, :]
        mask = idx >= 0
        idx_c = torch.where(mask, idx, 0).reshape(-1)
        kg = k[:, :, idx_c, :].reshape(B, H, S, W, Dh)
        vg = v[:, :, idx_c, :].reshape(B, H, S, W, Dh)
        logits = torch.einsum("bhsd,bhswd->bhsw", q, kg) * scale
        logits = torch.where(mask[None, None, :, :], logits, -1e9)
        attn = _softmax(logits, axis=-1)
        o = torch.einsum("bhsw,bhswd->bhsd", attn, vg)
        return (lambda x, *d: x.permute(*d))(o, 0, 2, 1, 3).reshape(B, S, D) @ wo

    def _B_ref(x, w_gate, w_up, w_down):
        g = _silu(x @ w_gate)
        u = x @ w_up
        return (g * u) @ w_down

    x = A_x
    xn = _layernorm(x, axis=-1)
    s = _A_ref(xn, A_wqkv, A_wo)
    g = _silu(_B_ref(xn, B_w_gate, B_w_up, B_w_down))
    return x + s * g
```

```cpp
#include <hip/hip_runtime.h>
#include <hip/hip_bf16.h>
typedef unsigned short u16;
__device__ __forceinline__ float bf2f(u16 v){ return __uint_as_float(((unsigned)v)<<16); }
__device__ __forceinline__ u16 f2bf(float f){ __bf16 b=(__bf16)f; return __builtin_bit_cast(u16,b); }
constexpr int S=2048,D=576,F=4608,H=32,DH=18;

__global__ void ln_k(const u16* x,u16* xn){
  int r=blockIdx.x; int t=threadIdx.x;
  float v[9]; float s=0;
  for(int i=0;i<9;i++){ v[i]=bf2f(x[r*D+t+64*i]); s+=v[i]; }
  for(int o=32;o;o>>=1) s+=__shfl_xor(s,o);
  float m=s/D; float q=0;
  for(int i=0;i<9;i++){ float d=v[i]-m; q+=d*d; }
  for(int o=32;o;o>>=1) q+=__shfl_xor(q,o);
  float rs=rsqrtf(q/D+1e-5f);
  for(int i=0;i<9;i++) xn[r*D+t+64*i]=f2bf((v[i]-m)*rs);
}
__global__ void gemm_k(const u16* A,const u16* B,float* C,int M,int N,int K){
  __shared__ float As[16][17], Bs[16][17];
  int tx=threadIdx.x&15, ty=threadIdx.x>>4;
  int row=blockIdx.y*16+ty, col=blockIdx.x*16+tx;
  float acc=0;
  for(int k0=0;k0<K;k0+=16){
    As[ty][tx]=bf2f(A[row*K+k0+tx]);
    Bs[ty][tx]=bf2f(B[(k0+ty)*N+col]);
    __syncthreads();
    for(int k=0;k<16;k++) acc+=As[ty][k]*Bs[k][tx];
    __syncthreads();
  }
  C[row*N+col]=acc;
}
__global__ void attn_k(const float* qkv,u16* o){
  int idx=blockIdx.x*blockDim.x+threadIdx.x;
  int s=idx/H,h=idx%H;
  const float* q=qkv+s*3*D+h*DH;
  float lg[8]; float mx=-1e30f;
  for(int w=0;w<8;w++){
    int p=s-2*w;
    if(p>=0){ const float* k=qkv+p*3*D+D+h*DH; float a=0; for(int d=0;d<DH;d++) a+=q[d]*k[d]; lg[w]=a*0.23570226039551584f; }
    else lg[w]=-1e9f;
    mx=fmaxf(mx,lg[w]);
  }
  float sum=0; for(int w=0;w<8;w++){ lg[w]=__expf(lg[w]-mx); sum+=lg[w]; }
  float inv=1.f/sum;
  for(int d=0;d<DH;d++){ float a=0; for(int w=0;w<8;w++){ int p=s-2*w; if(p<0)p=0; a+=lg[w]*qkv[p*3*D+2*D+h*DH+d]; } o[s*D+h*DH+d]=f2bf(a*inv); }
}
__global__ void swiglu_k(const float* g,const float* u,u16* h,int n){
  int i=blockIdx.x*blockDim.x+threadIdx.x; if(i<n){ float a=g[i]; h[i]=f2bf(a/(1.f+__expf(-a))*u[i]); }
}
__global__ void final_k(const u16* x,const float* s,const float* d,u16* out,int n){
  int i=blockIdx.x*blockDim.x+threadIdx.x; if(i<n){ float a=d[i]; out[i]=f2bf(bf2f(x[i])+s[i]*(a/(1.f+__expf(-a)))); }
}
extern "C" void kernel_launch(void* const* d_in,const int* in_sizes,int n_in,void* d_out,int out_size,void* d_ws,size_t ws_size,hipStream_t stream){
  const u16* x=(const u16*)d_in[0]; const u16* wqkv=(const u16*)d_in[1]; const u16* wo=(const u16*)d_in[2];
  const u16* wg=(const u16*)d_in[4]; const u16* wu=(const u16*)d_in[5]; const u16* wd=(const u16*)d_in[6];
  char* ws=(char*)d_ws;
  u16* xn=(u16*)ws; ws+=S*D*2;
  u16* o=(u16*)ws; ws+=S*D*2;
  u16* h=(u16*)ws; ws+=(size_t)S*F*2;
  float* qkv=(float*)ws; ws+=(size_t)S*3*D*4;
  float* g=(float*)ws; ws+=(size_t)S*F*4;
  float* u=(float*)ws; ws+=(size_t)S*F*4;
  float* sb=(float*)ws; ws+=(size_t)S*D*4;
  float* db=(float*)ws; ws+=(size_t)S*D*4;
  ln_k<<<S,64,0,stream>>>(x,xn);
  gemm_k<<<dim3(3*D/16,S/16),256,0,stream>>>(xn,wqkv,qkv,S,3*D,D);
  attn_k<<<S*H/256,256,0,stream>>>(qkv,o);
  gemm_k<<<dim3(D/16,S/16),256,0,stream>>>(o,wo,sb,S,D,D);
  gemm_k<<<dim3(F/16,S/16),256,0,stream>>>(xn,wg,g,S,F,D);
  gemm_k<<<dim3(F/16,S/16),256,0,stream>>>(xn,wu,u,S,F,D);
  swiglu_k<<<S*F/256,256,0,stream>>>(g,u,h,S*F);
  gemm_k<<<dim3(D/16,S/16),256,0,stream>>>(h,wd,db,S,D,F);
  final_k<<<S*D/256,256,0,stream>>>(x,sb,db,(u16*)d_out,S*D);
}
```

```cpp
#include <hip/hip_runtime.h>
#include <utility>
typedef unsigned short u16; typedef unsigned long long u64;
typedef __attribute__((ext_vector_type(4))) unsigned u32x4;
typedef __attribute__((ext_vector_type(2))) unsigned u32x2;
typedef __attribute__((ext_vector_type(4))) int i32x4;
typedef __attribute__((ext_vector_type(4))) short s16x4;
typedef __attribute__((ext_vector_type(8))) __bf16 bf16x8;
typedef __attribute__((ext_vector_type(2))) __bf16 bf16x2;
typedef __attribute__((ext_vector_type(4))) float f32x4;
typedef __attribute__((address_space(3))) s16x4* lds_s16x4_p;
typedef __attribute__((address_space(3))) void* lds_vp;
#define DI __device__ __forceinline__
#define SB() __builtin_amdgcn_sched_barrier(0)
#define AI __attribute__((always_inline))
#define IC(x) std::integral_constant<int,(x)>{}
template<class Fn,int... I> DI void sfor_impl(Fn&& f, std::integer_sequence<int,I...>){ (f(std::integral_constant<int,I>{}),...); }
template<int N,class Fn> DI void sfor(Fn&& f){ sfor_impl(f,std::make_integer_sequence<int,N>{}); }

constexpr int S=2048,D=576,F=4608;
constexpr float LOG2E=1.4426950408889634f;
constexpr unsigned WS_HF=0;
constexpr unsigned WS_PF=32768;
constexpr unsigned WS_SF=49152;
constexpr unsigned WS_OF=53248;
constexpr unsigned WS_KF=57344;
constexpr unsigned WS_FLAG_BYTES=131072;
constexpr unsigned WS_XN=1u<<20;
constexpr unsigned WS_O =4u<<20;
constexpr unsigned WS_OL=7u<<20;
constexpr unsigned WS_KV=11u<<20;
constexpr unsigned WS_SB=8u<<20;
constexpr unsigned WS_H =16u<<20;
constexpr unsigned WS_P =40u<<20;
constexpr unsigned WS_BYTES=64u<<20;
constexpr int SLOT=24576;
constexpr unsigned TAG=1;

DI s16x4 trr(unsigned a){ return __builtin_amdgcn_ds_read_tr16_b64_v4i16((lds_s16x4_p)(size_t)a); }
DI bf16x8 cat(s16x4 a,s16x4 b){ return __builtin_bit_cast(bf16x8,__builtin_shufflevector(a,b,0,1,2,3,4,5,6,7)); }
DI unsigned pack2(float a,float b){ bf16x2 v={(__bf16)a,(__bf16)b}; return __builtin_bit_cast(unsigned,v); }
DI float bflo(unsigned v){ return __uint_as_float(v<<16); }
DI float bfhi(unsigned v){ return __uint_as_float(v&0xffff0000u); }
DI i32x4 mk_rsrc(const void* p, unsigned bytes){
  unsigned long long a=(unsigned long long)p; i32x4 r; r[0]=(int)a; r[1]=(int)(a>>32); r[2]=(int)bytes; r[3]=0x00020000; return r;
}
template<unsigned SO,unsigned LO> DI void glds16(i32x4 rsrc, unsigned voff, unsigned sbase, unsigned lbase){
  unsigned t;
  asm volatile("s_add_u32 m0, %4, %6\n\ts_add_u32 %0, %3, %5\n\tbuffer_load_dwordx4 %1, %2, %0 offen lds":"=&s"(t):"v"(voff),"s"(rsrc),"s"(sbase),"s"(lbase),"i"(SO),"i"(LO):"memory","scc");
}
template<bool AG,bool SC1,int OFF> DI void ldfrag(bf16x8& d,i32x4 rsrc,unsigned voff,unsigned soff){
  if constexpr(AG){ if constexpr(SC1) asm volatile("buffer_load_dwordx4 %0, %1, %2, %3 offen offset:%4 sc1":"=a"(d):"v"(voff),"s"(rsrc),"s"(soff),"i"(OFF):"memory");
                    else asm volatile("buffer_load_dwordx4 %0, %1, %2, %3 offen offset:%4":"=a"(d):"v"(voff),"s"(rsrc),"s"(soff),"i"(OFF):"memory"); }
  else { if constexpr(SC1) asm volatile("buffer_load_dwordx4 %0, %1, %2, %3 offen offset:%4 sc1":"=v"(d):"v"(voff),"s"(rsrc),"s"(soff),"i"(OFF):"memory");
         else asm volatile("buffer_load_dwordx4 %0, %1, %2, %3 offen offset:%4":"=v"(d):"v"(voff),"s"(rsrc),"s"(soff),"i"(OFF):"memory"); }
}
template<int OFF> DI void st16_sc1(u32x4 v,i32x4 rsrc,unsigned voff,unsigned soff){
  asm volatile("buffer_store_dwordx4 %0, %1, %2, %3 offen offset:%4 sc1\n\ts_nop 1"::"v"(v),"v"(voff),"s"(rsrc),"s"(soff),"i"(OFF):"memory");
}
DI void st4_sc1(unsigned v,i32x4 rsrc,unsigned voff,unsigned soff){
  asm volatile("buffer_store_dword %0, %1, %2, %3 offen sc1"::"v"(v),"v"(voff),"s"(rsrc),"s"(soff):"memory");
}
DI void ld4_sc1(unsigned& d,i32x4 rsrc,unsigned voff,unsigned soff){
  asm volatile("buffer_load_dword %0, %1, %2, %3 offen sc1":"=v"(d):"v"(voff),"s"(rsrc),"s"(soff):"memory");
}
template<int OFF> DI void ld16_sc1(u32x4& d,i32x4 rsrc,unsigned voff,unsigned soff){
  asm volatile("buffer_load_dwordx4 %0, %1, %2, %3 offen offset:%4 sc1":"=v"(d):"v"(voff),"s"(rsrc),"s"(soff),"i"(OFF):"memory");
}
template<class T> DI void pin(T& v){ asm volatile("":"+v"(v)); }
template<int N> DI void waitvm(){ asm volatile("s_waitcnt vmcnt(%0)"::"i"(N>63?63:N):"memory"); }
template<int N> DI void poll_check(unsigned& v,i32x4 rsrc,unsigned voff,unsigned soff){
  unsigned cnt;
  asm volatile("s_waitcnt vmcnt(%5)\n\t"
               "s_mov_b32 %1, 0x4000\n"
               "1:\n\t"
               "v_cmp_ne_u32 vcc, 1, %0\n\t"
               "s_nop 0\n\t"
               "s_cbranch_vccz 2f\n\t"
               "s_sleep 1\n\t"
               "buffer_load_dword %0, %2, %3, %4 offen sc1\n\t"
               "s_sub_u32 %1, %1, 1\n\t"
               "s_waitcnt vmcnt(0)\n\t"
               "s_cbranch_scc0 1b\n"
               "2:"
               :"+v"(v),"=&s"(cnt):"v"(voff),"s"(rsrc),"s"(soff),"i"(N>63?63:N):"memory","vcc","scc");
}
template<bool FIRST,bool AG> DI void mma(f32x4& acc,const bf16x8& a,const bf16x8& b){
  if constexpr(FIRST){ if constexpr(AG) asm volatile("v_mfma_f32_16x16x32_bf16 %0, %1, %2, 0":"=&v"(acc):"v"(a),"a"(b)); else asm volatile("v_mfma_f32_16x16x32_bf16 %0, %1, %2, 0":"=&v"(acc):"v"(a),"v"(b)); }
  else { if constexpr(AG) asm volatile("v_mfma_f32_16x16x32_bf16 %0, %1, %2, %0":"+v"(acc):"v"(a),"a"(b)); else asm volatile("v_mfma_f32_16x16x32_bf16 %0, %1, %2, %0":"+v"(acc):"v"(a),"v"(b)); }
}

__global__ __launch_bounds__(128) void k0_ln(const u16* __restrict__ x,char* __restrict__ ws){
  const int tid=threadIdx.x, lane=tid&63;
  if(tid>=64){ if(lane<32) *(u32x4*)(ws+blockIdx.x*512+lane*16)=u32x4{0,0,0,0}; return; }
  const int p=lane&7, gw=blockIdx.x>>3, rb=(blockIdx.x>>1)&3, m=(blockIdx.x&1)*8+(lane>>3);
  const int row=gw*64+4*(m&~1)+2*rb+(m&1);
  u32x4 v[9];
  #pragma unroll
  for(int i=0;i<9;i++) v[i]=*(const u32x4*)(x+row*D+i*64+p*8);
  float s=0;
  #pragma unroll
  for(int i=0;i<9;i++)
    #pragma unroll
    for(int j=0;j<4;j++) s+=bflo(v[i][j])+bfhi(v[i][j]);
  s+=__shfl_xor(s,1); s+=__shfl_xor(s,2); s+=__shfl_xor(s,4);
  const float mean=s*(1.0f/D);
  float q=0;
  #pragma unroll
  for(int i=0;i<9;i++)
    #pragma unroll
    for(int j=0;j<4;j++){ float a=bflo(v[i][j])-mean,b=bfhi(v[i][j])-mean; q=fmaf(a,a,q); q=fmaf(b,b,q); }
  q+=__shfl_xor(q,1); q+=__shfl_xor(q,2); q+=__shfl_xor(q,4);
  const float sc=LOG2E*rsqrtf(q*(1.0f/D)+1e-5f), of=-mean*sc;
  char* dst=ws+WS_XN+(size_t)gw*18*4096+rb*1024+(p>>2)*4096+((p&3)*16+m)*16;
  #pragma unroll
  for(int i=0;i<9;i++){
    u32x4 o;
    #pragma unroll
    for(int j=0;j<4;j++) o[j]=pack2(fmaf(bflo(v[i][j]),sc,of),fmaf(bfhi(v[i][j]),sc,of));
    *(u32x4*)(dst+i*8192)=o;
  }
}

constexpr int NBS=162;
constexpr int PDF=3, NBUF=4;
constexpr int BG_T[14]={1,1,1,1,1,1,2,2,1,1,1,2,2,2};
constexpr int BG_I[14]={0,1,2,3,4,5,0,1,6,7,8,2,3,4};
struct Frag{ int ph; int ks; int c; bool first; int win; int base; int off; int d1; };
template<int ROLE> constexpr int na(){ return ROLE==0?432:360; }
template<int ROLE> constexpr Frag frag(int n){
  Frag f{};
  if(n<na<ROLE>()){
    if(ROLE==0){
      int pr=n/144,m=n%144,ks=m/4,c=m%4;
      if(m<12&&pr>0){ ks=(m%6)/2; c=(m/6)*2+(m&1); }
      if(m>=132){ ks=33+((m-132)%6)/2; c=((m-132)/6)*2+(m&1); }
      f.ph=ks/18; f.ks=ks%18; f.c=c; f.first=(ks%18==0); f.win=ks/12; f.base=c; f.off=(ks%12)*4096; f.d1=2048;
    } else {
      int kk=n/5,c=n%5,st=kk/4,sl=st%6;
      f.ph=3+kk/18; f.ks=kk%18; f.c=c; f.first=(f.ks==0); f.win=sl/2; f.base=(f.ph==6); f.off=(sl%2)*SLOT+(kk%4)*5120+c*(f.ph==6?32:8); f.d1=2560;
    }
  } else {
    int m=n-na<ROLE>(); if(m>=NBS) m=NBS-1;
    int gs=m/12,i=m%12,t=BG_T[gs],gi=BG_I[gs];
    int pos=(gs*12)%144;
    f.ph=2;
    if(t==1){ f.ks=2*gi+i/6; f.c=i%6; pos+=(i/6)*6; f.base=f.c&1; f.off=(f.c&~1)*32; f.d1=3072; }
    else { f.ks=4*gi+i/3; f.c=6+i%3; pos+=(i/3)*3; f.base=2; f.off=(i%3)*32; f.d1=1536; }
    f.first=(f.ks==0); f.win=pos/48; f.off+=(pos%48)*1024;
  }
  return f;
}
template<int ROLE> constexpr int stage_start(int s){ return ROLE==0? 24*s : (s<=18? 20*s : 360+24*(s-18)); }
template<int ROLE> constexpr int stage_n(int s){ return (ROLE==1&&s<18)?5:6; }
constexpr int LAST_STAGE=24;
template<int ROLE> constexpr int glds_step(int st,int i){
  if(st<=1) return -1;
  if(st==2) return i;
  if(ROLE==0){
    if(st==3) return 30+i;
    if(st==4) return 36+i;
    if(st==5) return 42+2*i;
    if(st==6) return 54+2*i;
    return stage_start<ROLE>(st-4)-3+4*i;
  } else {
    if(st==3) return 30+i;
    if(st==4) return 35+i;
    if(st==5) return 40+i;
    if(st==6) return 46+2*i;
    return stage_start<ROLE>(st-4)-3+3*i;
  }
}
struct Ev{ int gst,gi; int xl; int hl; int hfl; int poll; int pchk; int pst; int pfl; int kvst; int kvld; int ol; int sst; };
constexpr int HFD=28;
template<int ROLE> constexpr int hl_step(int i){
  int kk=i/4,r=i%4;
  if(kk>=12) return na<ROLE>()+63+(i-48);
  if(ROLE==0) return 366+6*kk+r+(r>1);
  return 296+6*kk+r+(r>1);
}
template<int ROLE> constexpr int p1_issue(){ return ROLE==0?340:280; }
template<int ROLE> constexpr int p1_check(){ return ROLE==0?364:295; }
template<int ROLE> constexpr int p2_issue(){ return na<ROLE>()+46; }
template<int ROLE> constexpr int p2_check(){ return na<ROLE>()+62; }
constexpr int PST0=133, PFL0=158;
constexpr int KVST0=182, KVFL=226, KVP_ISSUE=240, KVP_CHECK=256, KVLD0=257;
constexpr int OBLK=48;
constexpr int SST0=362, SFL=398;
template<int ROLE> constexpr int hst_count(int n){
  if(ROLE!=0) return 0;
  int rel=-1;
  if(n<432 && n%144>=138) rel=n%144-138;
  if(n>=144 && n<438 && n%144<6) rel=n%144;
  if(rel<0) return 0;
  int c=0; for(int r=0;r<4;r++){ int i=rel*4+r-1; if(i>=0&&i%6==4) c++; } return c;
}
constexpr int NMAX=640;
struct Tab{ Ev e[NMAX]; int pre[NMAX+1]; int bar[NMAX]; };
template<int ROLE> constexpr Tab make_tab(){
  Tab t{};
  for(int n=0;n<NMAX;n++){ t.e[n]=Ev{-1,-1,-1,-1,-1,-1,-1,-1,-1,-1,-1,-1,-1}; t.bar[n]=-1; }
  for(int st=0;st<=LAST_STAGE;st++){
    for(int i=0;i<stage_n<ROLE>(st);i++){ int n=glds_step<ROLE>(st,i); if(n>=0){ t.e[n].gst=st; t.e[n].gi=i; } }
    if(st>0) t.bar[stage_start<ROLE>(st)-PDF-1]=st;
  }
  for(int i=0;i<24;i++) t.e[6+i].xl=48+i;
  for(int i=0;i<72;i++) t.e[hl_step<ROLE>(i)].hl=i;
  t.e[p1_issue<ROLE>()].poll=0; t.e[p2_issue<ROLE>()].poll=1; t.e[p1_check<ROLE>()].pchk=0; t.e[p2_check<ROLE>()].pchk=1;
  if(ROLE==0){
    for(int p=0;p<3;p++) t.e[144*(p+1)+5+HFD].hfl=p;
  } else {
    for(int i=0;i<18;i++) t.e[KVST0+i].kvst=i;
    t.e[KVFL].hfl=3; t.e[KVP_ISSUE].poll=2; t.e[KVP_CHECK].pchk=2;
    for(int i=0;i<5;i++) t.e[KVLD0+i].kvld=i;
    for(int i=0;i<24;i++) t.e[270+i].ol=40+i;
    for(int i=0;i<10;i++) t.e[SST0+i].sst=i;
    t.e[SFL].hfl=4;
  }
  for(int i=0;i<12;i++) t.e[na<ROLE>()+PST0+i].pst=i;
  t.e[na<ROLE>()+PFL0].pfl=0;
  t.pre[0]=0;
  for(int n=0;n<NMAX;n++){ const Ev& e=t.e[n];
    t.pre[n+1]=t.pre[n]+(e.gst>=0)+(e.xl>=0)+(e.hl>=0)+(e.hfl>=0)+(e.poll>=0)+(e.pst>=0)+(e.pfl>=0)+(e.kvst>=0)+(e.kvld>=0)+(e.ol>=0)+(e.sst>=0)+hst_count<ROLE>(n)
              +((ROLE==1&&n==269)?OBLK:0); }
  return t;
}
template<int ROLE> inline constexpr Tab TAB=make_tab<ROLE>();
template<int ROLE> constexpr Ev ev(int n){ return TAB<ROLE>.e[n]; }
template<int ROLE> constexpr int vm_between(int a,int b){ return b>a+1? TAB<ROLE>.pre[b]-TAB<ROLE>.pre[a+1] : 0; }

DI float dot2(unsigned a,unsigned b,float c){ return __builtin_amdgcn_fdot2_f32_bf16(__builtin_bit_cast(bf16x2,a),__builtin_bit_cast(bf16x2,b),c,false); }
template<int CTRL> DI unsigned dppmov(unsigned old,unsigned src){ return (unsigned)__builtin_amdgcn_update_dpp((int)old,(int)src,CTRL,0xf,0xf,false); }
template<int DL,int BK> DI unsigned shiftc(unsigned cur,unsigned h){
  constexpr int sh=4*BK+(DL==1?2:0);
  unsigned t; if constexpr(sh==0) t=h; else t=dppmov<0x100+sh>(h,h);
  return dppmov<0x110+2*DL>(t,cur);
}

template<int ROLE>
DI void body(const u16* __restrict__ x,const u16* __restrict__ w0,const u16* __restrict__ w1,const u16* __restrict__ wd,u16* __restrict__ out,char* __restrict__ ws,
             unsigned lds0,int mt,int xs,int j,u64* dbg){
  const int tid=threadIdx.x, lane=tid&63, w=__builtin_amdgcn_readfirstlane(tid>>6);
  const i32x4 r0=mk_rsrc(w0,ROLE==0?D*F*2:D*3*D*2), r1=mk_rsrc(w1,ROLE==0?D*F*2:D*D*2), rd=mk_rsrc(wd,D*F*2), rw=mk_rsrc(ws,WS_BYTES);
  const unsigned l16=lane*16;
  const int g=lane>>4, q=(lane&15)>>2, p=lane&3;
  const int gw=mt*4+w;
  const int cg=xs*3+j;
  constexpr int NA=na<ROLE>(), NSTEP=NA+NBS;
  #define STS(i)
  STS(0);
  unsigned voA[5];
  unsigned soA;
  if constexpr(ROLE==0){
    voA[0]=(8*((lane>>5)&1)+((lane>>3)&3))*(F*2)+(((lane&7)^(((lane>>4)&3)<<1))*16);
    soA=(16*(w&1)+4*(w>>1))*(F*2)+cg*3*128;
  } else {
    #pragma unroll
    for(int i=0;i<5;i++){ int idx=64*i+lane,rho=idx/10,pc=idx%10; int k=8*((rho>>2)&3)+4*(rho>>4)+(rho&3); voA[i]=k*(3*D*2)+(pc>8?8:pc)*16; }
    soA=xs*144;
  }
  auto wo_pat=[&](int i,int kind) AI -> unsigned { unsigned ln=lane; pin(ln);
    int idx=64*i+ln,rho=idx/10,pc=idx-rho*10; int gg=(rho>>2)&3,jj=4*(rho>>4)+(rho&3);
    int slot=kind? 16+(jj&1) : 8*(w&1)+jj; int odd=gg&1; int feat=odd?(slot<2?16+slot:slot-2):slot;
    int row=18*gg+feat+(kind?72*(jj>>1):0); return row*(D*2)+pc*16; };
  const unsigned soW=(9*(xs>>1)+4*(xs&1))*32;
  unsigned vo1[3],vo2[3];
  auto set_voB=[&]() AI { unsigned ln=lane; pin(ln);
    #pragma unroll
    for(int t=0;t<3;t++){ int idx=64*t+ln; int row=idx/12,pc=idx-row*12; vo1[t]=row*(D*2)+((pc^(((row>>2)&1)<<1))*16);
                          int row2=idx/6,pc2=idx-row2*6; vo2[t]=row2*(D*2)+192+pc2*16; } };
  const unsigned soB=(xs*576)*(D*2)+j*288;
  const unsigned lA=lds0+w*(ROLE==0?1024:5120), lB=lds0+w*6144;
  const unsigned w8=8*w;
  auto do_glds=[&](auto stc,auto ic) AI {
    constexpr int st=decltype(stc)::value,i=decltype(ic)::value;
    if constexpr(st<18 && ROLE==0){
      constexpr int pr=st/6,mat=(st%6)/3,kp=st%3;
      glds16<pr*128+(kp*192+i*32)*(F*2),(st%6)*SLOT+i*4096>(mat?r1:r0,voA[0],soA,lA);
    } else if constexpr(st<18){
      constexpr auto qoff=[](int kk){ int ps=kk/18,ks=kk%18; int sec=ps==0?1:ps==1?2:0; return ks*3+sec; };
      constexpr auto woff=[](int kk){ int ks=kk-54; return ks<16? (ks>>1) : (ks-16)*4; };
      if constexpr(st<13){
        constexpr unsigned KP=qoff(4*st)|(qoff(4*st+1)<<8)|(qoff(4*st+2)<<16)|(qoff(4*st+3)<<24);
        const unsigned u=(KP>>w8)&255u; const unsigned ks=(u*171u)>>9, sec=u-3*ks;
        glds16<0,(st%6)*SLOT+i*1024>(r0,voA[i],soA+(ks*96+sec)*1152,lA);
      } else if constexpr(st==13){
        const unsigned msk=(w<2)?0u:~0u;
        i32x4 rs; rs[0]=r0[0]^((r0[0]^r1[0])&msk); rs[1]=r0[1]^((r0[1]^r1[1])&msk); rs[2]=r0[2]^((r0[2]^r1[2])&msk); rs[3]=r0[3];
        const unsigned sq=soA+((16+(w&1))*96)*1152;
        const unsigned so=sq^((sq^soW)&msk);
        unsigned pw=wo_pat(i,0); unsigned vo=(w<2)?voA[i]:pw; voA[i]=pw;
        glds16<0,(st%6)*SLOT+i*1024>(rs,vo,so,lA);
      } else {
        constexpr unsigned KP=woff(4*st)|(woff(4*st+1)<<8)|(woff(4*st+2)<<16)|(woff(4*st+3)<<24);
        const unsigned u=(KP>>w8)&255u;
        unsigned vo=voA[i];
        if constexpr(st==17){ unsigned pl=wo_pat(i,1); vo=(w<2)?vo:pl; }
        glds16<0,(st%6)*SLOT+i*1024>(r1,vo,soW+u*(72*D*2),lA);
      }
    } else {
      constexpr int g0=(st-18)*2,g1=g0+1;
      constexpr int t0=BG_T[g0],t1=BG_T[g1],i0=BG_I[g0],i1=BG_I[g1];
      constexpr auto lo_=[](int l){ return 3*(l%3)+l/3; };
      constexpr auto rowb=[lo_](int t,int gi,int half){ return (t==1? lo_(gi)*64+half*32+(i/3)*16 : lo_((2*gi+half)>8?8:(2*gi+half))*64+(i/3)*32)/16; };
      constexpr unsigned KP=rowb(t0,i0,0)|(rowb(t0,i0,1)<<8)|(rowb(t1,i1,0)<<16)|(rowb(t1,i1,1)<<24);
      const unsigned rsel=((KP>>w8)&255u)*16;
      unsigned vo;
      if constexpr(t0==t1) vo=(t0==1)?vo1[i%3]:vo2[i%3];
      else vo=(w<2)?((t0==1)?vo1[i%3]:vo2[i%3]):((t1==1)?vo1[i%3]:vo2[i%3]);
      glds16<0,(st%6)*SLOT+i*1024>(rd,vo,soB+rsel*(D*2),lB);
    }
  };
  bf16x8 xr[18][4];
  auto ld_x=[&](auto ic) AI { constexpr int i=decltype(ic)::value,ks=i/4,r=i%4; ldfrag<(ks<16),false,r*1024>(xr[ks][r],rw,l16,WS_XN+(gw*18+ks)*4096); };
  auto ld_o=[&](auto ic) AI { constexpr int i=decltype(ic)::value,ks=i/4,r=i%4; ldfrag<true,true,r*1024>(xr[ks][r],rw,l16,WS_O+(gw*18+ks)*4096); };
  auto ld_h=[&](auto ic) AI { constexpr int i=decltype(ic)::value,kk=i/4,r=i%4; constexpr int l=kk/2;
    ldfrag<(kk<16),true,r*1024>(xr[kk][r],rw,l16,WS_H+(gw*144+xs*18+(3*(l%3)+l/3)*2+(kk&1))*4096); };
  const unsigned hf_line=WS_HF+((mt*8+xs)*4+w)*128;
  const unsigned vlane0=(lane==0)?0u:0x80000000u;
  unsigned pollv=0;
  constexpr int G0=stage_n<ROLE>(0);
  sfor<G0>([&](auto i) AI { do_glds(IC(0),i); });
  sfor<24>([&](auto i) AI { ld_x(i); });
  sfor<G0>([&](auto i) AI { do_glds(IC(1),i); });
  sfor<24>([&](auto i) AI { ld_x(IC(24+decltype(i)::value)); });
  unsigned bw[3][4];
  if constexpr(ROLE==0){ const int row=4*g+q; unsigned b0=lds0+row*128+32*((row>>1)&3)+8*p;
    #pragma unroll
    for(int i=0;i<3;i++)
      #pragma unroll
      for(int c=0;c<4;c++){ bw[i][c]=(b0^(c*32))+i*49152; pin(bw[i][c]); } }
  else {
    #pragma unroll
    for(int i=0;i<3;i++){ bw[i][0]=lds0+i*49152+(4*g+q)*160+36*p-4*(p&1); bw[i][1]=lds0+i*49152+(4*g+q)*160+8*p; asm volatile("":"+v"(bw[i][0]),"+v"(bw[i][1])); } }
  unsigned bb[3][3];
  auto set_bases_B=[&](auto ic) AI { constexpr int i=decltype(ic)::value; unsigned ln=lane; pin(ln); const int g2=ln>>4,row=4*g2+((ln&15)>>2),p2=ln&3;
    bb[i][0]=lds0+i*49152+row*192+8*p2+(g2&1)*32; bb[i][1]=lds0+i*49152+row*192+8*p2+((g2&1)^1)*32; bb[i][2]=lds0+i*49152+row*96+8*p2;
    asm volatile("":"+v"(bb[i][0]),"+v"(bb[i][1]),"+v"(bb[i][2])); };
  float c2v=LOG2E*LOG2E; pin(c2v);
  waitvm<48+G0>();
  __builtin_amdgcn_s_barrier();
  STS(1);
  f32x4 ga[5][4],ua[4][4],da[9][4];
  bf16x8 a[NBUF];
  auto faddr=[&](auto nc) AI -> unsigned { constexpr Frag f=frag<ROLE>(decltype(nc)::value); if constexpr(f.ph!=2) return bw[f.win][f.base]+f.off; else return bb[f.win][f.base]+f.off; };
  sfor<PDF>([&](auto ic) AI { constexpr int i=decltype(ic)::value; constexpr Frag f=frag<ROLE>(i); unsigned ad=faddr(ic); a[i]=cat(trr(ad),trr(ad+f.d1)); });
  float e0=0,e1=0,e2=0; u32x4 hv;
  SB();
  auto fin=[&](auto tc,auto icn,auto prc) AI {
    constexpr int t=decltype(tc)::value, i=decltype(icn)::value, pr=decltype(prc)::value; constexpr int rb=i/6, jj=i%6;
    if constexpr(jj<4){ constexpr int cc=2*t+(jj>>1), e=(jj&1)*2; unsigned v=pack2(ga[cc][rb][e]*ua[cc][rb][e],ga[cc][rb][e+1]*ua[cc][rb][e+1]); pin(v); hv[jj]=v; }
    else if constexpr(jj==4) st16_sc1<rb*1024>(hv,rw,l16,WS_H+(gw*144+(cg*3+pr)*2+t)*4096);
  };
  auto pk8=[&](const f32x4& u0,const f32x4& u1) AI -> u32x4 { u32x4 v; v[0]=pack2(u0[0],u0[1]); v[1]=pack2(u0[2],u0[3]); v[2]=pack2(u1[0],u1[1]); v[3]=pack2(u1[2],u1[3]); return v; };
  const unsigned pbase=WS_P+((xs*36+j*9)*64+gw*2)*1024;
  auto pstore=[&](auto tc,auto hc) AI { constexpr int t=decltype(tc)::value,hh=decltype(hc)::value; st16_sc1<hh*1024>(pk8(da[t][2*hh],da[t][2*hh+1]),rw,l16,pbase+t*65536); };
  const unsigned pf_w=WS_PF+((mt*4+j)*4+w)*128+xs*4;
  u32x4 kq[9],vq[9],hk[5],OL[8];
  const bool odd=g&1;
  const unsigned kf_own=WS_KF+(xs*32+gw)*128, kf_prev=WS_KF+(xs*32+(gw>0?gw-1:0))*128;
  const unsigned kv_prev=gw>0?WS_KV+(xs*32+gw-1)*5120:0x80000000u;

  sfor<NSTEP>([&](auto nc) AI {
    constexpr int n=decltype(nc)::value;
    constexpr Frag f=frag<ROLE>(n);
    constexpr Ev e=ev<ROLE>(n);
    if constexpr(ROLE==1 && n==90){
      asm volatile("s_nop 7"); SB();
      #pragma unroll
      for(int r=0;r<4;r++){
        #pragma unroll
        for(int pi=0;pi<9;pi++) kq[pi][r]=pack2(ga[pi/2][r][(pi&1)*2],ga[pi/2][r][(pi&1)*2+1]);
        unsigned t9=pack2(ga[4][r][2],ga[4][r][3]); kq[0][r]=odd?t9:kq[0][r];
      }
      #pragma unroll
      for(int pi=0;pi<9;pi++) pin(kq[pi]);
      SB();
    }
    if constexpr(ROLE==1 && n==180){
      asm volatile("s_nop 7"); SB();
      #pragma unroll
      for(int f2=0;f2<9;f2++)
        #pragma unroll
        for(int e2_=0;e2_<2;e2_++){ const int pos=2*f2+e2_;
          unsigned A=pack2(ga[pos/4][2][pos%4],ga[pos/4][3][pos%4]), B=pack2(ga[pos/4][0][pos%4],ga[pos/4][1][pos%4]);
          if(f2==0){ unsigned A9=pack2(ga[4][2][2+e2_],ga[4][3][2+e2_]), B9=pack2(ga[4][0][2+e2_],ga[4][1][2+e2_]); A=odd?A9:A; B=odd?B9:B; }
          vq[f2][2*e2_]=A; vq[f2][2*e2_+1]=B; }
      #pragma unroll
      for(int pi=0;pi<9;pi++) pin(vq[pi]);
      SB();
    }
    if constexpr(ROLE==1 && n==270){
      STS(2);
      waitvm<vm_between<ROLE>(KVLD0+4,269)>();
      asm volatile("s_nop 7"); pin(hk[0]); pin(hk[1]); pin(hk[2]); pin(hk[3]); pin(hk[4]); SB();
      unsigned qp[4][9];
      #pragma unroll
      for(int r=0;r<4;r++){
        #pragma unroll
        for(int pi=0;pi<9;pi++) qp[r][pi]=pack2(ga[pi/2][r][(pi&1)*2],ga[pi/2][r][(pi&1)*2+1]);
        unsigned t9=pack2(ga[4][r][2],ga[4][r][3]); qp[r][0]=odd?t9:qp[r][0];
      }
      const int am=(lane&15)>>1;
      const float b1=(gw==0&&am<1)?-1e30f:0.f, b2=(gw==0&&am<2)?-1e30f:0.f;
      float lg[4][8];
      #pragma unroll
      for(int r=0;r<4;r++)
        #pragma unroll
        for(int ww=0;ww<8;ww++) lg[r][ww]= ww<=r?0.f : (ww-r<=4? b1:b2);
      sfor<9>([&](auto pc) AI { constexpr int pi=decltype(pc)::value;
        unsigned ke[12];
        #pragma unroll
        for(int r=0;r<4;r++){ unsigned c=kq[pi][r], h=hk[pi/4][r]; ke[8+r]=c; ke[4+r]=shiftc<1,pi%4>(c,h); if(r>0) ke[r]=shiftc<2,pi%4>(c,h); }
        #pragma unroll
        for(int r=0;r<4;r++)
          #pragma unroll
          for(int ww=0;ww<8;ww++) lg[r][ww]=dot2(ke[8+r-ww],qp[r][pi],lg[r][ww]);
      });
      const float csc=0.23570226039551584f/LOG2E;
      unsigned pp[4][5]; float rinv[4];
      #pragma unroll
      for(int r=0;r<4;r++){
        float mx=fmaxf(fmaxf(fmaxf(lg[r][0],lg[r][1]),fmaxf(lg[r][2],lg[r][3])),fmaxf(fmaxf(lg[r][4],lg[r][5]),fmaxf(lg[r][6],lg[r][7])));
        const float mo=-mx*csc; float sum=0;
        #pragma unroll
        for(int ww=0;ww<8;ww++){ lg[r][ww]=__builtin_amdgcn_exp2f(fmaf(lg[r][ww],csc,mo)); sum+=lg[r][ww]; }
        rinv[r]=__builtin_amdgcn_rcpf(sum*LOG2E);
        if(r&1){
          #pragma unroll
          for(int t=0;t<4;t++) pp[r][t]=pack2(lg[r][2*t+1],lg[r][2*t]);
        } else {
          pp[r][0]=pack2(lg[r][0],0.f);
          #pragma unroll
          for(int t=1;t<4;t++) pp[r][t]=pack2(lg[r][2*t],lg[r][2*t-1]);
          pp[r][4]=pack2(0.f,lg[r][7]);
        }
      }
      unsigned op[4][9];
      sfor<9>([&](auto fc) AI { constexpr int f2=decltype(fc)::value; constexpr int si=9+f2;
        float o2[4][2];
        #pragma unroll
        for(int e2_=0;e2_<2;e2_++){
          unsigned A=vq[f2][2*e2_],B=vq[f2][2*e2_+1],hA=hk[si/4][2*e2_],hB=hk[si/4][2*e2_+1];
          unsigned pr[6]={A,B,shiftc<1,si%4>(A,hA),shiftc<1,si%4>(B,hB),shiftc<2,si%4>(A,hA),shiftc<2,si%4>(B,hB)};
          #pragma unroll
          for(int r=0;r<4;r++){ const int s0=(r>=2)?0:1; const int nt=(r&1)?4:5; float acc=0;
            #pragma unroll
            for(int t=0;t<nt;t++) acc=dot2(pr[s0+t],pp[r][t],acc);
            o2[r][e2_]=acc*rinv[r]; }
        }
        #pragma unroll
        for(int r=0;r<4;r++) op[r][f2]=pack2(o2[r][0],o2[r][1]);
      });
      sfor<8>([&](auto ic) AI { constexpr int r=decltype(ic)::value/2,s2=decltype(ic)::value%2;
        u32x4 v={op[r][4*s2],op[r][4*s2+1],op[r][4*s2+2],op[r][4*s2+3]}; st16_sc1<r*1024>(v,rw,l16,WS_O+(gw*18+2*xs+s2)*4096); });
      { u32x4 v={op[0][8],op[1][8],op[2][8],op[3][8]}; st16_sc1<0>(v,rw,l16,WS_OL+(gw*8+xs)*1024); }
      STS(3);
      waitvm<0>();
      const unsigned of_line=WS_OF+gw*128;
      st4_sc1(TAG,rw,vlane0,of_line+xs*4);
      const unsigned vp8=(lane&7)*4;
      ld4_sc1(pollv,rw,vp8,of_line); poll_check<0>(pollv,rw,vp8,of_line);
      sfor<40>([&](auto i) AI { ld_o(i); });
      sfor<8>([&](auto i) AI { constexpr int hh=decltype(i)::value;
        ld16_sc1<(hh&3)*1024>(OL[hh],rw,l16,WS_OL+(gw*8+(hh&4))*1024); });
      SB();
    }
    if constexpr(TAB<ROLE>.bar[n]>=0){
      constexpr int bs=TAB<ROLE>.bar[n]; constexpr int last=glds_step<ROLE>(bs,stage_n<ROLE>(bs)-1);
      if constexpr(last>=0) waitvm<vm_between<ROLE>(last,n)>();
      else waitvm<24+vm_between<ROLE>(-1,n)>();
      __builtin_amdgcn_s_barrier(); SB();
    }
    if constexpr(n<(ROLE==0?72:90) && f.c==0){ constexpr int ks=f.ks;
      if constexpr(ks<6) waitvm<(5-ks)*4+24+G0+vm_between<ROLE>(-1,n)>();
      else if constexpr(ks<12) waitvm<(11-ks)*4+vm_between<ROLE>(-1,n)>();
      else waitvm<vm_between<ROLE>(6+(ks-12)*4+3,n)>();
      SB(); }
    if constexpr(f.ph==6 && f.c==0){ constexpr int ks=f.ks;
      if constexpr(ks<10) waitvm<(9-ks)*4+8+vm_between<ROLE>(269,n)>();
      else if constexpr(ks<16) waitvm<vm_between<ROLE>(270+(ks-10)*4+3,n)>();
      SB();
      if constexpr(ks==12){
        #pragma unroll
        for(int hh=0;hh<8;hh++) pin(OL[hh]);
        #pragma unroll
        for(int aa=0;aa<2;aa++)
          #pragma unroll
          for(int r=0;r<4;r++){ u32x4 v={OL[4*aa][r],OL[4*aa+1][r],OL[4*aa+2][r],OL[4*aa+3][r]}; pin(v); xr[16+aa][r]=__builtin_bit_cast(bf16x8,v); }
        SB();
      }
    }
    if constexpr(f.ph==2 && f.c==0){ waitvm<vm_between<ROLE>(hl_step<ROLE>(f.ks*4+3),n)>(); SB(); }
    if constexpr(e.pchk==0){ unsigned vp=((lane%6)/2*3+(lane%6)%2)*4; poll_check<vm_between<ROLE>(p1_issue<ROLE>(),n)>(pollv,rw,vp,hf_line); SB(); }
    if constexpr(e.pchk==1){ unsigned vp=((lane%3)*3+2)*4; poll_check<vm_between<ROLE>(p2_issue<ROLE>(),n)>(pollv,rw,vp,hf_line); SB(); }
    if constexpr(e.pchk==2){ poll_check<vm_between<ROLE>(KVP_ISSUE,n)>(pollv,rw,0u,kf_prev); SB(); }
    if constexpr(n==glds_step<ROLE>(18,0)-2){ set_voB(); SB(); }
    if constexpr(n==NA-24){ set_bases_B(IC(0)); SB(); }
    if constexpr(n==NA+8){ set_bases_B(IC(1)); SB(); }
    if constexpr(n==NA+12){ set_bases_B(IC(2)); SB(); }
    constexpr int nn=n+PDF; constexpr Frag fn=frag<ROLE>(nn);
    s16x4 lo;
    sfor<4>([&](auto rc) AI {
      constexpr int r=decltype(rc)::value;
      if constexpr(f.ph==1) mma<f.first,(f.ks<16)>(ua[f.c][r],a[n%NBUF],xr[f.ks][r]);
      else if constexpr(f.ph==2) mma<f.first,(f.ks<16)>(da[f.c][r],a[n%NBUF],xr[f.ks][r]);
      else mma<f.first,(f.ks<16)>(ga[f.c][r],a[n%NBUF],xr[f.ks][r]);
      SB();
      if constexpr(nn<NSTEP){
        if constexpr(r==0){ lo=trr(faddr(IC(nn))); SB(); }
        if constexpr(r==1){ a[nn%NBUF]=cat(lo,trr(faddr(IC(nn))+fn.d1)); SB(); }
      }
      if constexpr(e.gst>=0){ if constexpr(r==3){ do_glds(IC(e.gst),IC(e.gi)); SB(); } }
      if constexpr(e.xl>=0){ if constexpr(r==2){ ld_x(IC(e.xl)); SB(); } }
      if constexpr(e.hl>=0){ if constexpr(r==2){ ld_h(IC(e.hl)); SB(); } }
      if constexpr(e.ol>=0){ if constexpr(r==2){ ld_o(IC(e.ol)); SB(); } }
      if constexpr(e.pst>=0){ if constexpr(r==2){ pstore(IC(e.pst/2),IC(e.pst%2)); SB(); } }
      if constexpr(e.sst>=0){ if constexpr(r==2){ constexpr int t=e.sst/2,hh=e.sst%2; st16_sc1<hh*1024>(pk8(ga[t][2*hh],ga[t][2*hh+1]),rw,l16,WS_SB+((9*(xs>>1)+4*(xs&1)+t)*64+gw*2)*1024); SB(); } }
      if constexpr(e.kvst>=0){ if constexpr(r==2){ constexpr int i=e.kvst; const unsigned vk=((lane&15)>=12)?(g*4+(lane&3))*16u:0x80000000u;
          st16_sc1<(i%16)*256>(i<9?kq[i%9]:vq[i%9],rw,vk,WS_KV+(xs*32+gw)*5120+(i/16)*4096); SB(); } }
      if constexpr(e.kvld>=0){ if constexpr(r==2){ constexpr int i=e.kvld; const unsigned vk=q*256+(g*4+p)*16;
          ld16_sc1<0>(hk[i],rw,vk,kv_prev+i*1024); SB(); } }
      if constexpr(r==2){
        if constexpr(e.poll==0){ unsigned vp=((lane%6)/2*3+(lane%6)%2)*4; ld4_sc1(pollv,rw,vp,hf_line); SB(); }
        if constexpr(e.poll==1){ unsigned vp=((lane%3)*3+2)*4; ld4_sc1(pollv,rw,vp,hf_line); SB(); }
        if constexpr(e.poll==2){ ld4_sc1(pollv,rw,0u,kf_prev); SB(); }
      }
      if constexpr(r==0){
        if constexpr(e.hfl>=0&&e.hfl<3){ waitvm<vm_between<ROLE>(144*(e.hfl+1)+5,n)>(); st4_sc1(TAG,rw,vlane0,hf_line+(j*3+e.hfl)*4); SB(); }
        if constexpr(e.hfl==3){ waitvm<vm_between<ROLE>(KVST0+17,n)>(); st4_sc1(TAG,rw,vlane0,kf_own); SB(); }
        if constexpr(e.hfl==4){ waitvm<vm_between<ROLE>(SST0+9,n)>(); st4_sc1(TAG,rw,vlane0,WS_SF+gw*128+xs*4); SB(); }
        if constexpr(e.pfl==0){ waitvm<vm_between<ROLE>(NA+PST0+11,n)>(); st4_sc1(TAG,rw,vlane0,pf_w); SB(); }
      }
      if constexpr(ROLE==0){
        constexpr int m=n%144;
        if constexpr(n<NA){
          constexpr int E0=73;
          if constexpr(r==2 && m>=E0 && m<E0+64){ constexpr int k=m-E0; e0=__builtin_amdgcn_exp2f(-ga[k/16][(k/4)%4][k%4]); pin(e0); SB(); }
          if constexpr(r==0 && m>=E0+1 && m<E0+65){ e1=fmaf(e0,c2v,c2v); pin(e1); SB(); }
          if constexpr(r==1 && m>=E0+2 && m<E0+66){ constexpr int k=m-E0-2; float gv=ga[k/16][(k/4)%4][k%4]*e2; pin(gv); ga[k/16][(k/4)%4][k%4]=gv; SB(); }
          if constexpr(r==3 && m>=E0+1 && m<E0+65){ e2=__builtin_amdgcn_rcpf(e1); pin(e2); SB(); }
          if constexpr(m>=138){ constexpr int i=(m-138)*4+r; if constexpr(i>=1){ fin(IC(0),IC(i-1),IC(n/144)); SB(); } }
        }
        if constexpr(n>=144 && n<NA+6 && m<6){ constexpr int i=m*4+r; if constexpr(i>=1){ fin(IC(1),IC(i-1),IC(n/144-1)); SB(); } }
      }
    });
  });
  STS(4);
  const int gc=mt*4+(xs>>1), rp=mt*8+xs;
  const unsigned pf_r=WS_PF+((mt*4+j)*4+(xs>>1))*128;
  const int tl[3]={w, (int)((0x03020504u>>w8)&255u), (int)((0x08070606u>>w8)&255u)};
  asm volatile("s_nop 7"); SB();
  sfor<6>([&](auto i) AI { pstore(IC(6+decltype(i)::value/2),IC(decltype(i)::value%2)); });
  const unsigned vrow=(4*(lane&14)+(lane&1))*(D*2)+g*8;
  const unsigned srow=(gc*64+4*(xs&1))*(D*2)+j*288;
  u32x2 xv[3][2];
  {
    const i32x4 rx=mk_rsrc(x,S*D*2);
    #pragma unroll
    for(int i=0;i<3;i++)
      #pragma unroll
      for(int h2=0;h2<2;h2++)
        asm volatile("buffer_load_dwordx2 %0, %1, %2, %3 offen":"=v"(xv[i][h2]):"v"(vrow),"s"(rx),"s"(srow+h2*2*(D*2)+tl[i]*32):"memory");
  }
  const unsigned vpe=(lane&8)? (WS_SF+gc*128+(2*j+(lane&1))*4) : (pf_r+(lane&7)*4);
  ld4_sc1(pollv,rw,vpe,0u);
  waitvm<7>();
  st4_sc1(TAG,rw,vlane0,pf_w+32);
  poll_check<1>(pollv,rw,vpe,0u);
  asm volatile("":"+v"(xv[0][0]),"+v"(xv[0][1]),"+v"(xv[1][0]),"+v"(xv[1][1]),"+v"(xv[2][0]),"+v"(xv[2][1]));
  STS(5);
  auto rsw=__builtin_amdgcn_make_buffer_rsrc((void*)ws,0,WS_BYTES,0x00020000);
  auto rso=__builtin_amdgcn_make_buffer_rsrc((void*)out,0,S*D*2,0x00020000);
  auto ld_tile=[&](int t,u32x4* pv) AI {
    #pragma unroll
    for(int k=0;k<8;k++) pv[k]=__builtin_amdgcn_raw_buffer_load_b128(rsw,l16,WS_P+((k*36+j*9+t)*64+rp)*1024,16); };
  auto fin_tile=[&](int t,u32x4* pv,u32x4 sv,u32x2* xx) AI {
    float acc[8];
    #pragma unroll
    for(int e=0;e<4;e++){ acc[2*e]=bflo(pv[0][e]); acc[2*e+1]=bfhi(pv[0][e]); }
    #pragma unroll
    for(int k=1;k<8;k++)
      #pragma unroll
      for(int e=0;e<4;e++){ acc[2*e]+=bflo(pv[k][e]); acc[2*e+1]+=bfhi(pv[k][e]); }
    #pragma unroll
    for(int h2=0;h2<2;h2++){
      u32x2 o;
      #pragma unroll
      for(int e=0;e<2;e++){
        float d0=acc[h2*4+2*e],d1=acc[h2*4+2*e+1];
        float g0=d0*__builtin_amdgcn_rcpf(1.0f+__builtin_amdgcn_exp2f(-LOG2E*d0)), g1=d1*__builtin_amdgcn_rcpf(1.0f+__builtin_amdgcn_exp2f(-LOG2E*d1));
        unsigned s2=sv[h2*2+e], x2=xx[h2][e];
        o[e]=pack2(fmaf(bflo(s2),g0,bflo(x2)),fmaf(bfhi(s2),g1,bfhi(x2)));
      }
      __builtin_amdgcn_raw_buffer_store_b64(o,rso,vrow,srow+h2*2*(D*2)+t*32,0);
    }
  };
  u32x4 pv0[8],pv1[8],sv[3];
  ld_tile(tl[0],pv0); ld_tile(tl[1],pv1);
  #pragma unroll
  for(int i=0;i<3;i++) sv[i]=__builtin_amdgcn_raw_buffer_load_b128(rsw,l16,WS_SB+((j*9+tl[i])*64+rp)*1024,16);
  SB();
  fin_tile(tl[0],pv0,sv[0],xv[0]); fin_tile(tl[1],pv1,sv[1],xv[1]);
  SB();
  const unsigned vp8=(lane&7)*4;
  ld4_sc1(pollv,rw,vp8,pf_r+32); poll_check<0>(pollv,rw,vp8,pf_r+32);
  STS(6);
  ld_tile(tl[2],pv0);
  fin_tile(tl[2],pv0,sv[2],xv[2]);
}

__global__ __launch_bounds__(256,1) void k1_main(const u16* __restrict__ x,const u16* __restrict__ wqkv,const u16* __restrict__ wo,const u16* __restrict__ wg,const u16* __restrict__ wu,const u16* __restrict__ wd,u16* __restrict__ out,char* __restrict__ ws
  ){
  u64* dbg=nullptr;
  extern __shared__ __attribute__((aligned(16))) char smem[];
  const unsigned lds0=(unsigned)(size_t)(lds_vp)smem;
  const int xs=blockIdx.x&7, idx=blockIdx.x>>3, mt=idx&7, j=idx>>3;
  if(j<3) body<0>(x,wg,wu,wd,out,ws,lds0,mt,xs,j,dbg);
  else body<1>(x,wqkv,wo,wd,out,ws,lds0,mt,xs,j,dbg);
}

extern "C" void kernel_launch(void* const* d_in,const int* in_sizes,int n_in,void* d_out,int out_size,void* d_ws,size_t ws_size,hipStream_t stream){
  const u16* x=(const u16*)d_in[0]; const u16* wqkv=(const u16*)d_in[1]; const u16* wo=(const u16*)d_in[2];
  const u16* wg=(const u16*)d_in[4]; const u16* wu=(const u16*)d_in[5]; const u16* wd=(const u16*)d_in[6];
  char* ws=(char*)d_ws;
  (void)hipFuncSetAttribute((const void*)k1_main,hipFuncAttributeMaxDynamicSharedMemorySize,6*SLOT);
  k0_ln<<<256,128,0,stream>>>(x,ws);
  k1_main<<<256,256,6*SLOT,stream>>>(x,wqkv,wo,wg,wu,wd,(u16*)d_out,ws
  );
}
```

```cpp
#include <hip/hip_runtime.h>
#include <utility>
typedef unsigned short u16; typedef unsigned long long u64;
typedef __attribute__((ext_vector_type(4))) unsigned u32x4;
typedef __attribute__((ext_vector_type(2))) unsigned u32x2;
typedef __attribute__((ext_vector_type(4))) int i32x4;
typedef __attribute__((ext_vector_type(4))) short s16x4;
typedef __attribute__((ext_vector_type(8))) __bf16 bf16x8;
typedef __attribute__((ext_vector_type(2))) __bf16 bf16x2;
typedef __attribute__((ext_vector_type(4))) float f32x4;
typedef __attribute__((address_space(3))) s16x4* lds_s16x4_p;
typedef __attribute__((address_space(3))) void* lds_vp;
#define DI __device__ __forceinline__
#define SB() __builtin_amdgcn_sched_barrier(0)
#define AI __attribute__((always_inline))
#define IC(x) std::integral_constant<int,(x)>{}
template<class Fn,int... I> DI void sfor_impl(Fn&& f, std::integer_sequence<int,I...>){ (f(std::integral_constant<int,I>{}),...); }
template<int N,class Fn> DI void sfor(Fn&& f){ sfor_impl(f,std::make_integer_sequence<int,N>{}); }

constexpr int S=2048,D=576,F=4608;
constexpr float LOG2E=1.4426950408889634f;
constexpr unsigned WS_HF=0;
constexpr unsigned WS_PF=32768;
constexpr unsigned WS_SF=49152;
constexpr unsigned WS_OF=53248;
constexpr unsigned WS_KF=57344;
constexpr unsigned WS_FLAG_BYTES=131072;
constexpr unsigned WS_XN=1u<<20;
constexpr unsigned WS_O =4u<<20;
constexpr unsigned WS_OL=7u<<20;
constexpr unsigned WS_KV=11u<<20;
constexpr unsigned WS_SB=8u<<20;
constexpr unsigned WS_H =16u<<20;
constexpr unsigned WS_P =40u<<20;
constexpr unsigned WS_BYTES=64u<<20;
constexpr int SLOT=24576;
constexpr unsigned TAG=1;
#define K0LINES 2

DI s16x4 trr(unsigned a){ return __builtin_amdgcn_ds_read_tr16_b64_v4i16((lds_s16x4_p)(size_t)a); }
DI bf16x8 cat(s16x4 a,s16x4 b){ return __builtin_bit_cast(bf16x8,__builtin_shufflevector(a,b,0,1,2,3,4,5,6,7)); }
DI unsigned pack2(float a,float b){ bf16x2 v={(__bf16)a,(__bf16)b}; return __builtin_bit_cast(unsigned,v); }
DI float bflo(unsigned v){ return __uint_as_float(v<<16); }
DI float bfhi(unsigned v){ return __uint_as_float(v&0xffff0000u); }
DI i32x4 mk_rsrc(const void* p, unsigned bytes){
  unsigned long long a=(unsigned long long)p; i32x4 r; r[0]=(int)a; r[1]=(int)(a>>32); r[2]=(int)bytes; r[3]=0x00020000; return r;
}
template<unsigned SO,unsigned LO> DI void glds16(i32x4 rsrc, unsigned voff, unsigned sbase, unsigned lbase){
  unsigned t;
  asm volatile("s_add_u32 m0, %4, %6\n\ts_add_u32 %0, %3, %5\n\tbuffer_load_dwordx4 %1, %2, %0 offen lds":"=&s"(t):"v"(voff),"s"(rsrc),"s"(sbase),"s"(lbase),"i"(SO),"i"(LO):"memory","scc");
}
DI void touch(i32x4 rsrc, unsigned voff, unsigned soff, unsigned ldummy){
  asm volatile("s_mov_b32 m0, %3\n\ts_nop 0\n\tbuffer_load_dword %0, %1, %2 offen lds"::"v"(voff),"s"(rsrc),"s"(soff),"s"(ldummy):"memory");
}
template<bool AG,bool SC1,int OFF> DI void ldfrag(bf16x8& d,i32x4 rsrc,unsigned voff,unsigned soff){
  if constexpr(AG){ if constexpr(SC1) asm volatile("buffer_load_dwordx4 %0, %1, %2, %3 offen offset:%4 sc1":"=a"(d):"v"(voff),"s"(rsrc),"s"(soff),"i"(OFF):"memory");
                    else asm volatile("buffer_load_dwordx4 %0, %1, %2, %3 offen offset:%4":"=a"(d):"v"(voff),"s"(rsrc),"s"(soff),"i"(OFF):"memory"); }
  else { if constexpr(SC1) asm volatile("buffer_load_dwordx4 %0, %1, %2, %3 offen offset:%4 sc1":"=v"(d):"v"(voff),"s"(rsrc),"s"(soff),"i"(OFF):"memory");
         else asm volatile("buffer_load_dwordx4 %0, %1, %2, %3 offen offset:%4":"=v"(d):"v"(voff),"s"(rsrc),"s"(soff),"i"(OFF):"memory"); }
}
template<int OFF> DI void st16_sc1(u32x4 v,i32x4 rsrc,unsigned voff,unsigned soff){
  asm volatile("buffer_store_dwordx4 %0, %1, %2, %3 offen offset:%4 sc1\n\ts_nop 1"::"v"(v),"v"(voff),"s"(rsrc),"s"(soff),"i"(OFF):"memory");
}
DI void st4_sc1(unsigned v,i32x4 rsrc,unsigned voff,unsigned soff){
  asm volatile("buffer_store_dword %0, %1, %2, %3 offen sc1"::"v"(v),"v"(voff),"s"(rsrc),"s"(soff):"memory");
}
DI void ld4_sc1(unsigned& d,i32x4 rsrc,unsigned voff,unsigned soff){
  asm volatile("buffer_load_dword %0, %1, %2, %3 offen sc1":"=v"(d):"v"(voff),"s"(rsrc),"s"(soff):"memory");
}
template<int OFF> DI void ld16_sc1(u32x4& d,i32x4 rsrc,unsigned voff,unsigned soff){
  asm volatile("buffer_load_dwordx4 %0, %1, %2, %3 offen offset:%4 sc1":"=v"(d):"v"(voff),"s"(rsrc),"s"(soff),"i"(OFF):"memory");
}
template<class T> DI void pin(T& v){ asm volatile("":"+v"(v)); }
template<int N> DI void waitvm(){ asm volatile("s_waitcnt vmcnt(%0)"::"i"(N>63?63:N):"memory"); }
template<int N> DI void poll_check(unsigned& v,i32x4 rsrc,unsigned voff,unsigned soff){
  unsigned cnt;
  asm volatile("s_waitcnt vmcnt(%5)\n\t"
               "s_mov_b32 %1, 0x4000\n"
               "1:\n\t"
               "v_cmp_ne_u32 vcc, 1, %0\n\t"
               "s_nop 0\n\t"
               "s_cbranch_vccz 2f\n\t"
               "s_sleep 1\n\t"
               "buffer_load_dword %0, %2, %3, %4 offen sc1\n\t"
               "s_sub_u32 %1, %1, 1\n\t"
               "s_waitcnt vmcnt(0)\n\t"
               "s_cbranch_scc0 1b\n"
               "2:"
               :"+v"(v),"=&s"(cnt):"v"(voff),"s"(rsrc),"s"(soff),"i"(N>63?63:N):"memory","vcc","scc");
}
template<bool FIRST,bool AG> DI void mma(f32x4& acc,const bf16x8& a,const bf16x8& b){
  if constexpr(FIRST){ if constexpr(AG) asm volatile("v_mfma_f32_16x16x32_bf16 %0, %1, %2, 0":"=&v"(acc):"v"(a),"a"(b)); else asm volatile("v_mfma_f32_16x16x32_bf16 %0, %1, %2, 0":"=&v"(acc):"v"(a),"v"(b)); }
  else { if constexpr(AG) asm volatile("v_mfma_f32_16x16x32_bf16 %0, %1, %2, %0":"+v"(acc):"v"(a),"a"(b)); else asm volatile("v_mfma_f32_16x16x32_bf16 %0, %1, %2, %0":"+v"(acc):"v"(a),"v"(b)); }
}

__global__ __launch_bounds__(256) void k0_ln(const u16* __restrict__ x,char* __restrict__ ws,const u16* __restrict__ wqkv,const u16* __restrict__ wg,const u16* __restrict__ wu
  ){
  const int tid=threadIdx.x, lane=tid&63;
  if(tid>=64){
    if(tid<96) *(u32x4*)(ws+blockIdx.x*512+lane*16)=u32x4{0,0,0,0};
    {
      unsigned long long pc; int rel;
      asm volatile("s_getpc_b64 %0\n\ts_mov_b32 %1, k1_main@rel32@lo+4":"=s"(pc),"=s"(rel));
      pc+=(long long)rel;
      const int hw=(tid>>6)-1, xs=blockIdx.x&7, idx=blockIdx.x>>3, mt=idx&7, j=idx>>3;
      const u32x4* cp=(const u32x4*)(pc+(idx*3+hw)*1024+lane*16);
      const int t=hw*64+lane;
      const char *p0,*p1;
      if(j<3){
        const int tt=t<72*K0LINES?t:0; const int row=mt*72+tt/K0LINES, ln=tt%K0LINES;
        const size_t o=(size_t)row*(F*2)+((xs*3+j)*3+ln)*128; p0=(const char*)wg+o; p1=(const char*)wu+o;
      } else {
        const int tt=t<72*K0LINES?t:0; const int row=mt*72+tt/K0LINES, sec=(1+tt%K0LINES)%3;
        p0=(const char*)wqkv+(size_t)row*(3*D*2)+sec*1152+xs*144; p1=p0+140;
      }
      u32x4 t0; unsigned t1,t2;
      asm volatile("global_load_dwordx4 %0, %3, off\n\tglobal_load_dword %1, %4, off\n\tglobal_load_dword %2, %5, off\n\ts_waitcnt vmcnt(0)":"=&v"(t0),"=&v"(t1),"=&v"(t2):"v"(cp),"v"(p0),"v"(p1):"memory");
    }
    return; }
  const int p=lane&7, gw=blockIdx.x>>3, rb=(blockIdx.x>>1)&3, m=(blockIdx.x&1)*8+(lane>>3);
  const int row=gw*64+4*(m&~1)+2*rb+(m&1);
  u32x4 v[9];
  #pragma unroll
  for(int i=0;i<9;i++) v[i]=*(const u32x4*)(x+row*D+i*64+p*8);
  float s=0;
  #pragma unroll
  for(int i=0;i<9;i++)
    #pragma unroll
    for(int j=0;j<4;j++) s+=bflo(v[i][j])+bfhi(v[i][j]);
  s+=__shfl_xor(s,1); s+=__shfl_xor(s,2); s+=__shfl_xor(s,4);
  const float mean=s*(1.0f/D);
  float q=0;
  #pragma unroll
  for(int i=0;i<9;i++)
    #pragma unroll
    for(int j=0;j<4;j++){ float a=bflo(v[i][j])-mean,b=bfhi(v[i][j])-mean; q=fmaf(a,a,q); q=fmaf(b,b,q); }
  q+=__shfl_xor(q,1); q+=__shfl_xor(q,2); q+=__shfl_xor(q,4);
  const float sc=LOG2E*rsqrtf(q*(1.0f/D)+1e-5f), of=-mean*sc;
  char* dst=ws+WS_XN+(size_t)gw*18*4096+rb*1024+(p>>2)*4096+((p&3)*16+m)*16;
  #pragma unroll
  for(int i=0;i<9;i++){
    u32x4 o;
    #pragma unroll
    for(int j=0;j<4;j++) o[j]=pack2(fmaf(bflo(v[i][j]),sc,of),fmaf(bfhi(v[i][j]),sc,of));
    *(u32x4*)(dst+i*8192)=o;
  }
}

constexpr int NBS=162;
constexpr int PDF=3, NBUF=4;
constexpr int BG_T[14]={1,1,1,1,1,1,2,2,1,1,1,2,2,2};
constexpr int BG_I[14]={0,1,2,3,4,5,0,1,6,7,8,2,3,4};
struct Frag{ int ph; int ks; int c; bool first; int win; int base; int off; int d1; };
template<int ROLE> constexpr int na(){ return ROLE==0?432:360; }
template<int ROLE> constexpr Frag frag(int n){
  Frag f{};
  if(n<na<ROLE>()){
    if(ROLE==0){
      int pr=n/144,m=n%144,ks=m/4,c=m%4;
      if(m<12&&pr>0){ ks=(m%6)/2; c=(m/6)*2+(m&1); }
      if(m>=132){ ks=33+((m-132)%6)/2; c=((m-132)/6)*2+(m&1); }
      f.ph=ks/18; f.ks=ks%18; f.c=c; f.first=(ks%18==0); f.win=ks/12; f.base=c; f.off=(ks%12)*4096; f.d1=2048;
    } else {
      int kk=n/5,c=n%5,st=kk/4,sl=st%6;
      f.ph=3+kk/18; f.ks=kk%18; f.c=c; f.first=(f.ks==0); f.win=sl/2; f.base=(f.ph==6); f.off=(sl%2)*SLOT+(kk%4)*5120+c*(f.ph==6?32:8); f.d1=2560;
    }
  } else {
    int m=n-na<ROLE>(); if(m>=NBS) m=NBS-1;
    int gs=m/12,i=m%12,t=BG_T[gs],gi=BG_I[gs];
    int pos=(gs*12)%144;
    f.ph=2;
    if(t==1){ f.ks=2*gi+i/6; f.c=i%6; pos+=(i/6)*6; f.base=f.c&1; f.off=(f.c&~1)*32; f.d1=3072; }
    else { f.ks=4*gi+i/3; f.c=6+i%3; pos+=(i/3)*3; f.base=2; f.off=(i%3)*32; f.d1=1536; }
    f.first=(f.ks==0); f.win=pos/48; f.off+=(pos%48)*1024;
  }
  return f;
}
template<int ROLE> constexpr int stage_start(int s){ return ROLE==0? 24*s : (s<=18? 20*s : 360+24*(s-18)); }
template<int ROLE> constexpr int stage_n(int s){ return (ROLE==1&&s<18)?5:6; }
constexpr int LAST_STAGE=24;
template<int ROLE> constexpr int glds_step(int st,int i){
  if(st<=1) return -1;
  if(st==2) return i;
  if(ROLE==0){
    if(st==3) return 30+i;
    if(st==4) return 36+i;
    if(st==5) return 42+2*i;
    if(st==6) return 54+2*i;
    return stage_start<ROLE>(st-4)-3+4*i;
  } else {
    if(st==3) return 30+i;
    if(st==4) return 35+i;
    if(st==5) return 40+i;
    if(st==6) return 46+2*i;
    return stage_start<ROLE>(st-4)-3+3*i;
  }
}
struct Ev{ int gst,gi; int xl; int hl; int hfl; int poll; int pchk; int pst; int pfl; int kvst; int kvld; int ol; int sst; int pf; };
constexpr int HFD[3]={60,40,28};
template<int ROLE> constexpr int hl_step(int i){
  int kk=i/4,r=i%4;
  if(kk>=12) return na<ROLE>()+63+(i-48);
  if(ROLE==0) return 366+6*kk+r+(r>1);
  return 296+6*kk+r+(r>1);
}
template<int ROLE> constexpr int p1_issue(){ return ROLE==0?348:280; }
template<int ROLE> constexpr int p1_check(){ return ROLE==0?364:295; }
template<int ROLE> constexpr int p2_issue(){ return na<ROLE>()+46; }
template<int ROLE> constexpr int p2_check(){ return na<ROLE>()+62; }
constexpr int PST0=133, PFL0=158;
constexpr int KVST0=182, KVFL=226, KVP_ISSUE=240, KVP_CHECK=256, KVLD0=257;
constexpr int OBLK=48;
constexpr int SST0=362, SFL=398;
constexpr int PFA=31, PFW=160, PFD=200;
template<int ROLE> constexpr int hst_count(int n){
  if(ROLE!=0) return 0;
  int rel=-1;
  if(n<432 && n%144>=138) rel=n%144-138;
  if(n>=144 && n<438 && n%144<6) rel=n%144;
  if(rel<0) return 0;
  int c=0; for(int r=0;r<4;r++){ int i=rel*4+r-1; if(i>=0&&i%6==4) c++; } return c;
}
constexpr int NMAX=640;
struct Tab{ Ev e[NMAX]; int pre[NMAX+1]; int bar[NMAX]; };
template<int ROLE> constexpr Tab make_tab(){
  Tab t{};
  for(int n=0;n<NMAX;n++){ t.e[n]=Ev{-1,-1,-1,-1,-1,-1,-1,-1,-1,-1,-1,-1,-1,-1}; t.bar[n]=-1; }
  for(int st=0;st<=LAST_STAGE;st++){
    for(int i=0;i<stage_n<ROLE>(st);i++){ int n=glds_step<ROLE>(st,i); if(n>=0){ t.e[n].gst=st; t.e[n].gi=i; } }
    if(st>0) t.bar[stage_start<ROLE>(st)-PDF-1]=st;
  }
  for(int i=0;i<24;i++) t.e[6+i].xl=48+i;
  t.e[PFA].pf=0; t.e[PFA+1].pf=1;
  for(int i=0;i<72;i++) t.e[hl_step<ROLE>(i)].hl=i;
  t.e[p1_issue<ROLE>()].poll=0; t.e[p2_issue<ROLE>()].poll=1; t.e[p1_check<ROLE>()].pchk=0; t.e[p2_check<ROLE>()].pchk=1;
  if(ROLE==0){
    for(int p=0;p<3;p++){ t.e[144*(p+1)+5+HFD[p]].hfl=p; t.e[144*(p+1)+5+HFD[p]].pf=4+p; }
    t.e[PFW].pf=2; t.e[PFD].pf=3;
  } else {
    for(int i=0;i<18;i++) t.e[KVST0+i].kvst=i;
    t.e[KVFL].hfl=3; t.e[KVP_ISSUE].poll=2; t.e[KVP_CHECK].pchk=2;
    for(int i=0;i<5;i++) t.e[KVLD0+i].kvld=i;
    for(int i=0;i<24;i++) t.e[270+i].ol=40+i;
    for(int i=0;i<10;i++) t.e[SST0+i].sst=i;
    t.e[SFL].hfl=4;
  }
  for(int i=0;i<12;i++) t.e[na<ROLE>()+PST0+i].pst=i;
  t.e[na<ROLE>()+PFL0].pfl=0;
  t.pre[0]=0;
  for(int n=0;n<NMAX;n++){ const Ev& e=t.e[n];
    t.pre[n+1]=t.pre[n]+(e.gst>=0)+(e.xl>=0)+(e.hl>=0)+(e.hfl>=0)+(e.poll>=0)+(e.pst>=0)+(e.pfl>=0)+(e.kvst>=0)+(e.kvld>=0)+(e.ol>=0)+(e.sst>=0)+(e.pf>=0)+hst_count<ROLE>(n)
              +((ROLE==1&&n==269)?OBLK:0); }
  return t;
}
template<int ROLE> inline constexpr Tab TAB=make_tab<ROLE>();
template<int ROLE> constexpr Ev ev(int n){ return TAB<ROLE>.e[n]; }
template<int ROLE> constexpr int vm_between(int a,int b){ return b>a+1? TAB<ROLE>.pre[b]-TAB<ROLE>.pre[a+1] : 0; }

DI float dot2(unsigned a,unsigned b,float c){ return __builtin_amdgcn_fdot2_f32_bf16(__builtin_bit_cast(bf16x2,a),__builtin_bit_cast(bf16x2,b),c,false); }
template<int CTRL> DI unsigned dppmov(unsigned old,unsigned src){ return (unsigned)__builtin_amdgcn_update_dpp((int)old,(int)src,CTRL,0xf,0xf,false); }
template<int DL,int BK> DI unsigned shiftc(unsigned cur,unsigned h){
  constexpr int sh=4*BK+(DL==1?2:0);
  unsigned t; if constexpr(sh==0) t=h; else t=dppmov<0x100+sh>(h,h);
  return dppmov<0x110+2*DL>(t,cur);
}

template<int ROLE>
DI void body(const u16* __restrict__ x,const u16* __restrict__ w0,const u16* __restrict__ w1,const u16* __restrict__ wd,const u16* __restrict__ wo,u16* __restrict__ out,char* __restrict__ ws,
             unsigned lds0,int mt,int xs,int j,u64* dbg){
  const int tid=threadIdx.x, lane=tid&63, w=__builtin_amdgcn_readfirstlane(tid>>6);
  const i32x4 r0=mk_rsrc(w0,ROLE==0?D*F*2:D*3*D*2), r1=mk_rsrc(w1,ROLE==0?D*F*2:D*D*2), rd=mk_rsrc(wd,D*F*2), rw=mk_rsrc(ws,WS_BYTES);
  const unsigned l16=lane*16;
  const int g=lane>>4, q=(lane&15)>>2, p=lane&3;
  const int gw=mt*4+w;
  const int cg=xs*3+j;
  constexpr int NA=na<ROLE>(), NSTEP=NA+NBS;
  #define STS(i)
  STS(0);
  unsigned voA[5];
  unsigned soA;
  if constexpr(ROLE==0){
    voA[0]=(8*((lane>>5)&1)+((lane>>3)&3))*(F*2)+(((lane&7)^(((lane>>4)&3)<<1))*16);
    soA=(16*(w&1)+4*(w>>1))*(F*2)+cg*3*128;
  } else {
    #pragma unroll
    for(int i=0;i<5;i++){ int idx=64*i+lane,rho=idx/10,pc=idx%10; int k=8*((rho>>2)&3)+4*(rho>>4)+(rho&3); voA[i]=k*(3*D*2)+(pc>8?8:pc)*16; }
    soA=xs*144;
  }
  auto wo_pat=[&](int i,int kind) AI -> unsigned { unsigned ln=lane; pin(ln);
    int idx=64*i+ln,rho=idx/10,pc=idx-rho*10; int gg=(rho>>2)&3,jj=4*(rho>>4)+(rho&3);
    int slot=kind? 16+(jj&1) : 8*(w&1)+jj; int odd=gg&1; int feat=odd?(slot<2?16+slot:slot-2):slot;
    int row=18*gg+feat+(kind?72*(jj>>1):0); return row*(D*2)+pc*16; };
  const int gW=xs*4+w, tW=9*(mt>>1)+4*(mt&1);
  const unsigned soW=tW*32;
  unsigned vo1[3],vo2[3];
  auto set_voB=[&]() AI { unsigned ln=lane; pin(ln);
    #pragma unroll
    for(int t=0;t<3;t++){ int idx=64*t+ln; int row=idx/12,pc=idx-row*12; vo1[t]=row*(D*2)+((pc^(((row>>2)&1)<<1))*16);
                          int row2=idx/6,pc2=idx-row2*6; vo2[t]=row2*(D*2)+192+pc2*16; } };
  const unsigned soB=(xs*576)*(D*2)+j*288;
  const unsigned lA=lds0+w*(ROLE==0?1024:5120), lB=lds0+w*6144;
  const unsigned w8=8*w;
  auto do_glds=[&](auto stc,auto ic) AI {
    constexpr int st=decltype(stc)::value,i=decltype(ic)::value;
    if constexpr(st<18 && ROLE==0){
      constexpr int pr=st/6,mat=(st%6)/3,kp=st%3;
      glds16<pr*128+(kp*192+i*32)*(F*2),(st%6)*SLOT+i*4096>(mat?r1:r0,voA[0],soA,lA);
    } else if constexpr(st<18){
      constexpr auto qoff=[](int kk){ int ps=kk/18,ks=kk%18; int sec=ps==0?1:ps==1?2:0; return ks*3+sec; };
      constexpr auto woff=[](int kk){ int ks=kk-54; return ks<16? (ks>>1) : (ks-16)*4; };
      if constexpr(st<13){
        constexpr unsigned KP=qoff(4*st)|(qoff(4*st+1)<<8)|(qoff(4*st+2)<<16)|(qoff(4*st+3)<<24);
        const unsigned u=(KP>>w8)&255u; const unsigned ks=(u*171u)>>9, sec=u-3*ks;
        glds16<0,(st%6)*SLOT+i*1024>(r0,voA[i],soA+(ks*96+sec)*1152,lA);
      } else if constexpr(st==13){
        const unsigned msk=(w<2)?0u:~0u;
        i32x4 rs; rs[0]=r0[0]^((r0[0]^r1[0])&msk); rs[1]=r0[1]^((r0[1]^r1[1])&msk); rs[2]=r0[2]^((r0[2]^r1[2])&msk); rs[3]=r0[3];
        const unsigned sq=soA+((16+(w&1))*96)*1152;
        const unsigned sw=soW+mt*(72*D*2);
        const unsigned so=sq^((sq^sw)&msk);
        unsigned pw=wo_pat(i,0); unsigned vo=(w<2)?voA[i]:pw; voA[i]=pw;
        glds16<0,(st%6)*SLOT+i*1024>(rs,vo,so,lA);
      } else {
        constexpr unsigned KP=woff(4*st)|(woff(4*st+1)<<8)|(woff(4*st+2)<<16)|(woff(4*st+3)<<24);
        unsigned u=(KP>>w8)&255u;
        if constexpr(st==17){ const unsigned msk=(w<2)?~0u:0u; u=(u+(mt&msk))&7u; } else u=(u+mt)&7u;
        unsigned vo=voA[i];
        if constexpr(st==17){ unsigned pl=wo_pat(i,1); vo=(w<2)?vo:pl; }
        glds16<0,(st%6)*SLOT+i*1024>(r1,vo,soW+u*(72*D*2),lA);
      }
    } else {
      constexpr int g0=(st-18)*2,g1=g0+1;
      constexpr int t0=BG_T[g0],t1=BG_T[g1],i0=BG_I[g0],i1=BG_I[g1];
      constexpr auto lo_=[](int l){ return 3*(l%3)+l/3; };
      constexpr auto rowb=[lo_](int t,int gi,int half){ return (t==1? lo_(gi)*64+half*32+(i/3)*16 : lo_((2*gi+half)>8?8:(2*gi+half))*64+(i/3)*32)/16; };
      constexpr unsigned KP=rowb(t0,i0,0)|(rowb(t0,i0,1)<<8)|(rowb(t1,i1,0)<<16)|(rowb(t1,i1,1)<<24);
      const unsigned rsel=((KP>>w8)&255u)*16;
      unsigned vo;
      if constexpr(t0==t1) vo=(t0==1)?vo1[i%3]:vo2[i%3];
      else vo=(w<2)?((t0==1)?vo1[i%3]:vo2[i%3]):((t1==1)?vo1[i%3]:vo2[i%3]);
      glds16<0,(st%6)*SLOT+i*1024>(rd,vo,soB+rsel*(D*2),lB);
    }
  };
  bf16x8 xr[18][4];
  auto ld_x=[&](auto ic) AI { constexpr int i=decltype(ic)::value,ks=i/4,r=i%4; ldfrag<(ks<16),false,r*1024>(xr[ks][r],rw,l16,WS_XN+(gw*18+ks)*4096); };
  auto ld_o=[&](auto ic) AI { constexpr int i=decltype(ic)::value,ks=i/4,r=i%4; ldfrag<true,true,r*1024>(xr[ks][r],rw,l16,WS_O+(gW*18+((ks+2*mt)&15))*4096); };
  auto ld_h=[&](auto ic) AI { constexpr int i=decltype(ic)::value,kk=i/4,r=i%4; constexpr int l=kk/2;
    ldfrag<(kk<16),true,r*1024>(xr[kk][r],rw,l16,WS_H+(gw*144+xs*18+(3*(l%3)+l/3)*2+(kk&1))*4096); };
  const unsigned hf_line=WS_HF+((mt*8+xs)*4+w)*128;
  const unsigned vlane0=(lane==0)?0u:0x80000000u;
  unsigned pollv=0;
  constexpr int G0=stage_n<ROLE>(0);
  sfor<G0>([&](auto i) AI { do_glds(IC(0),i); });
  sfor<24>([&](auto i) AI { ld_x(i); });
  sfor<G0>([&](auto i) AI { do_glds(IC(1),i); });
  sfor<24>([&](auto i) AI { ld_x(IC(24+decltype(i)::value)); });
  unsigned bw[3][4];
  if constexpr(ROLE==0){ const int row=4*g+q; unsigned b0=lds0+row*128+32*((row>>1)&3)+8*p;
    #pragma unroll
    for(int i=0;i<3;i++)
      #pragma unroll
      for(int c=0;c<4;c++){ bw[i][c]=(b0^(c*32))+i*49152; pin(bw[i][c]); } }
  else {
    #pragma unroll
    for(int i=0;i<3;i++){ bw[i][0]=lds0+i*49152+(4*g+q)*160+36*p-4*(p&1); bw[i][1]=lds0+i*49152+(4*g+q)*160+8*p; asm volatile("":"+v"(bw[i][0]),"+v"(bw[i][1])); } }
  unsigned bb[3][3];
  auto set_bases_B=[&](auto ic) AI { constexpr int i=decltype(ic)::value; unsigned ln=lane; pin(ln); const int g2=ln>>4,row=4*g2+((ln&15)>>2),p2=ln&3;
    bb[i][0]=lds0+i*49152+row*192+8*p2+(g2&1)*32; bb[i][1]=lds0+i*49152+row*192+8*p2+((g2&1)^1)*32; bb[i][2]=lds0+i*49152+row*96+8*p2;
    asm volatile("":"+v"(bb[i][0]),"+v"(bb[i][1]),"+v"(bb[i][2])); };
  float c2v=LOG2E*LOG2E; pin(c2v);
  waitvm<48+G0>();
  __builtin_amdgcn_s_barrier();
  STS(1);
  f32x4 ga[5][4],ua[4][4],da[9][4];
  bf16x8 a[NBUF];
  auto faddr=[&](auto nc) AI -> unsigned { constexpr Frag f=frag<ROLE>(decltype(nc)::value); if constexpr(f.ph!=2) return bw[f.win][f.base]+f.off; else return bb[f.win][f.base]+f.off; };
  sfor<PDF>([&](auto ic) AI { constexpr int i=decltype(ic)::value; constexpr Frag f=frag<ROLE>(i); unsigned ad=faddr(ic); a[i]=cat(trr(ad),trr(ad+f.d1)); });
  float e0=0,e1=0,e2=0; u32x4 hv;
  SB();
  auto fin=[&](auto tc,auto icn,auto prc) AI {
    constexpr int t=decltype(tc)::value, i=decltype(icn)::value, pr=decltype(prc)::value; constexpr int rb=i/6, jj=i%6;
    if constexpr(jj<4){ constexpr int cc=2*t+(jj>>1), e=(jj&1)*2; unsigned v=pack2(ga[cc][rb][e]*ua[cc][rb][e],ga[cc][rb][e+1]*ua[cc][rb][e+1]); pin(v); hv[jj]=v; }
    else if constexpr(jj==4) st16_sc1<rb*1024>(hv,rw,l16,WS_H+(gw*144+(cg*3+pr)*2+t)*4096);
  };
  auto pk8=[&](const f32x4& u0,const f32x4& u1) AI -> u32x4 { u32x4 v; v[0]=pack2(u0[0],u0[1]); v[1]=pack2(u0[2],u0[3]); v[2]=pack2(u1[0],u1[1]); v[3]=pack2(u1[2],u1[3]); return v; };
  const unsigned pbase=WS_P+((xs*36+j*9)*64+gw*2)*1024;
  auto pstore=[&](auto tc,auto hc) AI { constexpr int t=decltype(tc)::value,hh=decltype(hc)::value; st16_sc1<hh*1024>(pk8(da[t][2*hh],da[t][2*hh+1]),rw,l16,pbase+t*65536); };
  const unsigned pf_w=WS_PF+((mt*4+j)*4+w)*128+xs*4;
  u32x4 kq[9],vq[9],hk[5],OL[8];
  const bool odd=g&1;
  const unsigned kf_own=WS_KF+(xs*32+gw)*128, kf_prev=WS_KF+(xs*32+(gw>0?gw-1:0))*128;
  const unsigned kv_prev=gw>0?WS_KV+(xs*32+gw-1)*5120:0x80000000u;

  sfor<NSTEP>([&](auto nc) AI {
    constexpr int n=decltype(nc)::value;
    constexpr Frag f=frag<ROLE>(n);
    constexpr Ev e=ev<ROLE>(n);
    if constexpr(ROLE==1 && n==90){
      asm volatile("s_nop 7"); SB();
      #pragma unroll
      for(int r=0;r<4;r++){
        #pragma unroll
        for(int pi=0;pi<9;pi++) kq[pi][r]=pack2(ga[pi/2][r][(pi&1)*2],ga[pi/2][r][(pi&1)*2+1]);
        unsigned t9=pack2(ga[4][r][2],ga[4][r][3]); kq[0][r]=odd?t9:kq[0][r];
      }
      #pragma unroll
      for(int pi=0;pi<9;pi++) pin(kq[pi]);
      SB();
    }
    if constexpr(ROLE==1 && n==180){
      asm volatile("s_nop 7"); SB();
      #pragma unroll
      for(int f2=0;f2<9;f2++)
        #pragma unroll
        for(int e2_=0;e2_<2;e2_++){ const int pos=2*f2+e2_;
          unsigned A=pack2(ga[pos/4][2][pos%4],ga[pos/4][3][pos%4]), B=pack2(ga[pos/4][0][pos%4],ga[pos/4][1][pos%4]);
          if(f2==0){ unsigned A9=pack2(ga[4][2][2+e2_],ga[4][3][2+e2_]), B9=pack2(ga[4][0][2+e2_],ga[4][1][2+e2_]); A=odd?A9:A; B=odd?B9:B; }
          vq[f2][2*e2_]=A; vq[f2][2*e2_+1]=B; }
      #pragma unroll
      for(int pi=0;pi<9;pi++) pin(vq[pi]);
      SB();
    }
    if constexpr(ROLE==1 && n==270){
      STS(2);
      waitvm<vm_between<ROLE>(KVLD0+4,269)>();
      asm volatile("s_nop 7"); pin(hk[0]); pin(hk[1]); pin(hk[2]); pin(hk[3]); pin(hk[4]); SB();
      unsigned qp[4][9];
      #pragma unroll
      for(int r=0;r<4;r++){
        #pragma unroll
        for(int pi=0;pi<9;pi++) qp[r][pi]=pack2(ga[pi/2][r][(pi&1)*2],ga[pi/2][r][(pi&1)*2+1]);
        unsigned t9=pack2(ga[4][r][2],ga[4][r][3]); qp[r][0]=odd?t9:qp[r][0];
      }
      const int am=(lane&15)>>1;
      const float b1=(gw==0&&am<1)?-1e30f:0.f, b2=(gw==0&&am<2)?-1e30f:0.f;
      float lg[4][8];
      #pragma unroll
      for(int r=0;r<4;r++)
        #pragma unroll
        for(int ww=0;ww<8;ww++) lg[r][ww]= ww<=r?0.f : (ww-r<=4? b1:b2);
      sfor<9>([&](auto pc) AI { constexpr int pi=decltype(pc)::value;
        unsigned ke[12];
        #pragma unroll
        for(int r=0;r<4;r++){ unsigned c=kq[pi][r], h=hk[pi/4][r]; ke[8+r]=c; ke[4+r]=shiftc<1,pi%4>(c,h); if(r>0) ke[r]=shiftc<2,pi%4>(c,h); }
        #pragma unroll
        for(int r=0;r<4;r++)
          #pragma unroll
          for(int ww=0;ww<8;ww++) lg[r][ww]=dot2(ke[8+r-ww],qp[r][pi],lg[r][ww]);
      });
      const float csc=0.23570226039551584f/LOG2E;
      unsigned pp[4][5]; float rinv[4];
      #pragma unroll
      for(int r=0;r<4;r++){
        float mx=fmaxf(fmaxf(fmaxf(lg[r][0],lg[r][1]),fmaxf(lg[r][2],lg[r][3])),fmaxf(fmaxf(lg[r][4],lg[r][5]),fmaxf(lg[r][6],lg[r][7])));
        const float mo=-mx*csc; float sum=0;
        #pragma unroll
        for(int ww=0;ww<8;ww++){ lg[r][ww]=__builtin_amdgcn_exp2f(fmaf(lg[r][ww],csc,mo)); sum+=lg[r][ww]; }
        rinv[r]=__builtin_amdgcn_rcpf(sum*LOG2E);
        if(r&1){
          #pragma unroll
          for(int t=0;t<4;t++) pp[r][t]=pack2(lg[r][2*t+1],lg[r][2*t]);
        } else {
          pp[r][0]=pack2(lg[r][0],0.f);
          #pragma unroll
          for(int t=1;t<4;t++) pp[r][t]=pack2(lg[r][2*t],lg[r][2*t-1]);
          pp[r][4]=pack2(0.f,lg[r][7]);
        }
      }
      unsigned op[4][9];
      sfor<9>([&](auto fc) AI { constexpr int f2=decltype(fc)::value; constexpr int si=9+f2;
        float o2[4][2];
        #pragma unroll
        for(int e2_=0;e2_<2;e2_++){
          unsigned A=vq[f2][2*e2_],B=vq[f2][2*e2_+1],hA=hk[si/4][2*e2_],hB=hk[si/4][2*e2_+1];
          unsigned pr[6]={A,B,shiftc<1,si%4>(A,hA),shiftc<1,si%4>(B,hB),shiftc<2,si%4>(A,hA),shiftc<2,si%4>(B,hB)};
          #pragma unroll
          for(int r=0;r<4;r++){ const int s0=(r>=2)?0:1; const int nt=(r&1)?4:5; float acc=0;
            #pragma unroll
            for(int t=0;t<nt;t++) acc=dot2(pr[s0+t],pp[r][t],acc);
            o2[r][e2_]=acc*rinv[r]; }
        }
        #pragma unroll
        for(int r=0;r<4;r++) op[r][f2]=pack2(o2[r][0],o2[r][1]);
      });
      sfor<8>([&](auto ic) AI { constexpr int r=decltype(ic)::value/2,s2=decltype(ic)::value%2;
        u32x4 v={op[r][4*s2],op[r][4*s2+1],op[r][4*s2+2],op[r][4*s2+3]}; st16_sc1<r*1024>(v,rw,l16,WS_O+(gw*18+2*xs+s2)*4096); });
      { u32x4 v={op[0][8],op[1][8],op[2][8],op[3][8]}; st16_sc1<0>(v,rw,l16,WS_OL+(gw*8+xs)*1024); }
      STS(3);
      waitvm<0>();
      st4_sc1(TAG,rw,vlane0,WS_OF+gw*128+xs*4);
      const unsigned of_line=WS_OF+gW*128;
      const unsigned vp8=(lane&7)*4;
      ld4_sc1(pollv,rw,vp8,of_line); poll_check<0>(pollv,rw,vp8,of_line);
      sfor<40>([&](auto i) AI { ld_o(i); });
      sfor<8>([&](auto i) AI { constexpr int hh=decltype(i)::value;
        ld16_sc1<(hh&3)*1024>(OL[hh],rw,l16,WS_OL+(gW*8+(hh&4))*1024); });
      SB();
    }
    if constexpr(TAB<ROLE>.bar[n]>=0){
      constexpr int bs=TAB<ROLE>.bar[n]; constexpr int last=glds_step<ROLE>(bs,stage_n<ROLE>(bs)-1);
      if constexpr(last>=0) waitvm<vm_between<ROLE>(last,n)>();
      else waitvm<24+vm_between<ROLE>(-1,n)>();
      __builtin_amdgcn_s_barrier(); SB();
    }
    if constexpr(n<(ROLE==0?72:90) && f.c==0){ constexpr int ks=f.ks;
      if constexpr(ks<6) waitvm<(5-ks)*4+24+G0+vm_between<ROLE>(-1,n)>();
      else if constexpr(ks<12) waitvm<(11-ks)*4+vm_between<ROLE>(-1,n)>();
      else waitvm<vm_between<ROLE>(6+(ks-12)*4+3,n)>();
      SB(); }
    if constexpr(f.ph==6 && f.c==0){ constexpr int ks=f.ks;
      if constexpr(ks<10) waitvm<(9-ks)*4+8+vm_between<ROLE>(269,n)>();
      else if constexpr(ks<16) waitvm<vm_between<ROLE>(270+(ks-10)*4+3,n)>();
      SB();
      if constexpr(ks==12){
        #pragma unroll
        for(int hh=0;hh<8;hh++) pin(OL[hh]);
        #pragma unroll
        for(int aa=0;aa<2;aa++)
          #pragma unroll
          for(int r=0;r<4;r++){ u32x4 v={OL[4*aa][r],OL[4*aa+1][r],OL[4*aa+2][r],OL[4*aa+3][r]}; pin(v); xr[16+aa][r]=__builtin_bit_cast(bf16x8,v); }
        SB();
      }
    }
    if constexpr(f.ph==2 && f.c==0){ waitvm<vm_between<ROLE>(hl_step<ROLE>(f.ks*4+3),n)>(); SB(); }
    if constexpr(e.pchk==0){ unsigned vp=((lane%6)/2*3+(lane%6)%2)*4; poll_check<vm_between<ROLE>(p1_issue<ROLE>(),n)>(pollv,rw,vp,hf_line); SB(); }
    if constexpr(e.pchk==1){ unsigned vp=((lane%3)*3+2)*4; poll_check<vm_between<ROLE>(p2_issue<ROLE>(),n)>(pollv,rw,vp,hf_line); SB(); }
    if constexpr(e.pchk==2){ poll_check<vm_between<ROLE>(KVP_ISSUE,n)>(pollv,rw,0u,kf_prev); SB(); }
    if constexpr(n==glds_step<ROLE>(18,0)-2){ set_voB(); SB(); }
    if constexpr(n==NA-24){ set_bases_B(IC(0)); SB(); }
    if constexpr(n==NA+8){ set_bases_B(IC(1)); SB(); }
    if constexpr(n==NA+12){ set_bases_B(IC(2)); SB(); }
    constexpr int nn=n+PDF; constexpr Frag fn=frag<ROLE>(nn);
    s16x4 lo;
    sfor<4>([&](auto rc) AI {
      constexpr int r=decltype(rc)::value;
      if constexpr(f.ph==1) mma<f.first,(f.ks<16)>(ua[f.c][r],a[n%NBUF],xr[f.ks][r]);
      else if constexpr(f.ph==2) mma<f.first,(f.ks<16)>(da[f.c][r],a[n%NBUF],xr[f.ks][r]);
      else mma<f.first,(f.ks<16)>(ga[f.c][r],a[n%NBUF],xr[f.ks][r]);
      SB();
      if constexpr(nn<NSTEP){
        if constexpr(r==0){ lo=trr(faddr(IC(nn))); SB(); }
        if constexpr(r==1){ a[nn%NBUF]=cat(lo,trr(faddr(IC(nn))+fn.d1)); SB(); }
      }
      if constexpr(e.gst>=0){ if constexpr(r==3){ do_glds(IC(e.gst),IC(e.gi)); SB(); } }
      if constexpr(e.xl>=0){ if constexpr(r==2){ ld_x(IC(e.xl)); SB(); } }
      if constexpr(e.hl>=0){ if constexpr(r==2){ ld_h(IC(e.hl)); SB(); } }
      if constexpr(e.ol>=0){ if constexpr(r==2){ ld_o(IC(e.ol)); SB(); } }
      if constexpr(e.pst>=0){ if constexpr(r==2){ pstore(IC(e.pst/2),IC(e.pst%2)); SB(); } }
      if constexpr(e.sst>=0){ if constexpr(r==2){ constexpr int t=e.sst/2,hh=e.sst%2; st16_sc1<hh*1024>(pk8(ga[t][2*hh],ga[t][2*hh+1]),rw,l16,WS_SB+((tW+t)*64+gW*2)*1024); SB(); } }
      if constexpr(e.kvst>=0){ if constexpr(r==2){ constexpr int i=e.kvst; const unsigned vk=((lane&15)>=12)?(g*4+(lane&3))*16u:0x80000000u;
          st16_sc1<(i%16)*256>(i<9?kq[i%9]:vq[i%9],rw,vk,WS_KV+(xs*32+gw)*5120+(i/16)*4096); SB(); } }
      if constexpr(e.kvld>=0){ if constexpr(r==2){ constexpr int i=e.kvld; const unsigned vk=q*256+(g*4+p)*16;
          ld16_sc1<0>(hk[i],rw,vk,kv_prev+i*1024); SB(); } }
      if constexpr(e.pf>=0){ if constexpr(r==1){
          unsigned ln=lane; pin(ln); const unsigned ldm=lds0+6*SLOT;
          if constexpr(e.pf<2 && ROLE==0) touch(e.pf?r1:r0,ln<36?(ln>>1)*(F*2)+(ln&1)*128:0x80000000u,gw*18*(F*2)+(cg*3+1)*128,ldm);
          else if constexpr(e.pf<2) touch(r0,ln<36?(ln>>1)*(3*D*2)+(ln&1)*140:0x80000000u,gw*18*(3*D*2)+(e.pf?0:2)*1152+xs*144,ldm);
          else if constexpr(e.pf==2) touch(mk_rsrc(wo,D*D*2),ln<54?ln*128:0x80000000u,((j*8+mt)*4+w)*6912,ldm);
          else if constexpr(e.pf==3) touch(rd,ln<54?ln*128:0x80000000u,xs*576*(D*2)+((j*8+mt)*4+w)*6912,ldm);
          else touch(rw,ln*128,WS_H+(gw*144+(cg*3+(e.pf-4))*2)*4096,ldm);
          SB(); } }
      if constexpr(r==2){
        if constexpr(e.poll==0){ unsigned vp=((lane%6)/2*3+(lane%6)%2)*4; ld4_sc1(pollv,rw,vp,hf_line); SB(); }
        if constexpr(e.poll==1){ unsigned vp=((lane%3)*3+2)*4; ld4_sc1(pollv,rw,vp,hf_line); SB(); }
        if constexpr(e.poll==2){ ld4_sc1(pollv,rw,0u,kf_prev); SB(); }
      }
      if constexpr(r==0){
        if constexpr(e.hfl>=0&&e.hfl<3){ waitvm<vm_between<ROLE>(144*(e.hfl+1)+5,n)>(); st4_sc1(TAG,rw,vlane0,hf_line+(j*3+e.hfl)*4); SB(); }
        if constexpr(e.hfl==3){ waitvm<vm_between<ROLE>(KVST0+17,n)>(); st4_sc1(TAG,rw,vlane0,kf_own); SB(); }
        if constexpr(e.hfl==4){ waitvm<vm_between<ROLE>(SST0+9,n)>(); st4_sc1(TAG,rw,vlane0,WS_SF+gW*128+mt*4); SB(); }
        if constexpr(e.pfl==0){ waitvm<vm_between<ROLE>(NA+PST0+11,n)>(); st4_sc1(TAG,rw,vlane0,pf_w); SB(); }
      }
      if constexpr(ROLE==0){
        constexpr int m=n%144;
        if constexpr(n<NA){
          constexpr int E0=73;
          if constexpr(r==2 && m>=E0 && m<E0+64){ constexpr int k=m-E0; e0=__builtin_amdgcn_exp2f(-ga[k/16][(k/4)%4][k%4]); pin(e0); SB(); }
          if constexpr(r==0 && m>=E0+1 && m<E0+65){ e1=fmaf(e0,c2v,c2v); pin(e1); SB(); }
          if constexpr(r==1 && m>=E0+2 && m<E0+66){ constexpr int k=m-E0-2; float gv=ga[k/16][(k/4)%4][k%4]*e2; pin(gv); ga[k/16][(k/4)%4][k%4]=gv; SB(); }
          if constexpr(r==3 && m>=E0+1 && m<E0+65){ e2=__builtin_amdgcn_rcpf(e1); pin(e2); SB(); }
          if constexpr(m>=138){ constexpr int i=(m-138)*4+r; if constexpr(i>=1){ fin(IC(0),IC(i-1),IC(n/144)); SB(); } }
        }
        if constexpr(n>=144 && n<NA+6 && m<6){ constexpr int i=m*4+r; if constexpr(i>=1){ fin(IC(1),IC(i-1),IC(n/144-1)); SB(); } }
      }
    });
  });
  STS(4);
  const int gc=mt*4+(xs>>1), rp=mt*8+xs;
  const unsigned pf_r=WS_PF+((mt*4+j)*4+(xs>>1))*128;
  const int tl[3]={w, (int)((0x03020504u>>w8)&255u), (int)((0x08070606u>>w8)&255u)};
  asm volatile("s_nop 7"); SB();
  sfor<6>([&](auto i) AI { pstore(IC(6+decltype(i)::value/2),IC(decltype(i)::value%2)); });
  const unsigned vrow=(4*(lane&14)+(lane&1))*(D*2)+g*8;
  const unsigned srow=(gc*64+4*(xs&1))*(D*2)+j*288;
  u32x2 xv[3][2];
  {
    const i32x4 rx=mk_rsrc(x,S*D*2);
    #pragma unroll
    for(int i=0;i<3;i++)
      #pragma unroll
      for(int h2=0;h2<2;h2++)
        asm volatile("buffer_load_dwordx2 %0, %1, %2, %3 offen":"=v"(xv[i][h2]):"v"(vrow),"s"(rx),"s"(srow+h2*2*(D*2)+tl[i]*32):"memory");
  }
  const unsigned vpe=(lane&8)? (WS_SF+gc*128+(2*j+(lane&1))*4) : (pf_r+(lane&7)*4);
  ld4_sc1(pollv,rw,vpe,0u);
  waitvm<7>();
  st4_sc1(TAG,rw,vlane0,pf_w+32);
  poll_check<1>(pollv,rw,vpe,0u);
  asm volatile("":"+v"(xv[0][0]),"+v"(xv[0][1]),"+v"(xv[1][0]),"+v"(xv[1][1]),"+v"(xv[2][0]),"+v"(xv[2][1]));
  STS(5);
  auto rsw=__builtin_amdgcn_make_buffer_rsrc((void*)ws,0,WS_BYTES,0x00020000);
  auto rso=__builtin_amdgcn_make_buffer_rsrc((void*)out,0,S*D*2,0x00020000);
  auto ld_tile=[&](int t,u32x4* pv) AI {
    #pragma unroll
    for(int k=0;k<8;k++) pv[k]=__builtin_amdgcn_raw_buffer_load_b128(rsw,l16,WS_P+((k*36+j*9+t)*64+rp)*1024,16); };
  auto fin_tile=[&](int t,u32x4* pv,u32x4 sv,u32x2* xx) AI {
    float acc[8];
    #pragma unroll
    for(int e=0;e<4;e++){ acc[2*e]=bflo(pv[0][e]); acc[2*e+1]=bfhi(pv[0][e]); }
    #pragma unroll
    for(int k=1;k<8;k++)
      #pragma unroll
      for(int e=0;e<4;e++){ acc[2*e]+=bflo(pv[k][e]); acc[2*e+1]+=bfhi(pv[k][e]); }
    #pragma unroll
    for(int h2=0;h2<2;h2++){
      u32x2 o;
      #pragma unroll
      for(int e=0;e<2;e++){
        float d0=acc[h2*4+2*e],d1=acc[h2*4+2*e+1];
        float g0=d0*__builtin_amdgcn_rcpf(1.0f+__builtin_amdgcn_exp2f(-LOG2E*d0)), g1=d1*__builtin_amdgcn_rcpf(1.0f+__builtin_amdgcn_exp2f(-LOG2E*d1));
        unsigned s2=sv[h2*2+e], x2=xx[h2][e];
        o[e]=pack2(fmaf(bflo(s2),g0,bflo(x2)),fmaf(bfhi(s2),g1,bfhi(x2)));
      }
      __builtin_amdgcn_raw_buffer_store_b64(o,rso,vrow,srow+h2*2*(D*2)+t*32,0);
    }
  };
  u32x4 pv0[8],pv1[8],sv[3];
  ld_tile(tl[0],pv0); ld_tile(tl[1],pv1);
  #pragma unroll
  for(int i=0;i<3;i++) sv[i]=__builtin_amdgcn_raw_buffer_load_b128(rsw,l16,WS_SB+((j*9+tl[i])*64+rp)*1024,16);
  SB();
  fin_tile(tl[0],pv0,sv[0],xv[0]); fin_tile(tl[1],pv1,sv[1],xv[1]);
  SB();
  const unsigned vp8=(lane&7)*4;
  ld4_sc1(pollv,rw,vp8,pf_r+32); poll_check<0>(pollv,rw,vp8,pf_r+32);
  STS(6);
  ld_tile(tl[2],pv0);
  fin_tile(tl[2],pv0,sv[2],xv[2]);
}

extern "C" __global__ __launch_bounds__(256,1) void k1_main(const u16* __restrict__ x,const u16* __restrict__ wqkv,const u16* __restrict__ wo,const u16* __restrict__ wg,const u16* __restrict__ wu,const u16* __restrict__ wd,u16* __restrict__ out,char* __restrict__ ws
  ){
  u64* dbg=nullptr;
  extern __shared__ __attribute__((aligned(16))) char smem[];
  const unsigned lds0=(unsigned)(size_t)(lds_vp)smem;
  const int xs=blockIdx.x&7, idx=blockIdx.x>>3, mt=idx&7, j=idx>>3;
  if(j<3) body<0>(x,wg,wu,wd,wo,out,ws,lds0,mt,xs,j,dbg);
  else body<1>(x,wqkv,wo,wd,wo,out,ws,lds0,mt,xs,j,dbg);
}

extern "C" void kernel_launch(void* const* d_in,const int* in_sizes,int n_in,void* d_out,int out_size,void* d_ws,size_t ws_size,hipStream_t stream){
  const u16* x=(const u16*)d_in[0]; const u16* wqkv=(const u16*)d_in[1]; const u16* wo=(const u16*)d_in[2];
  const u16* wg=(const u16*)d_in[4]; const u16* wu=(const u16*)d_in[5]; const u16* wd=(const u16*)d_in[6];
  char* ws=(char*)d_ws;
  (void)hipFuncSetAttribute((const void*)k1_main,hipFuncAttributeMaxDynamicSharedMemorySize,6*SLOT+1024);
  k0_ln<<<256,256,0,stream>>>(x,ws,wqkv,wg,wu
  );
  k1_main<<<256,256,6*SLOT+1024,stream>>>(x,wqkv,wo,wg,wu,wd,(u16*)d_out,ws
  );
}
```

```cpp
#include <hip/hip_runtime.h>
#include <utility>
typedef unsigned short u16; typedef unsigned long long u64;
typedef __attribute__((ext_vector_type(4))) unsigned u32x4;
typedef __attribute__((ext_vector_type(2))) unsigned u32x2;
typedef __attribute__((ext_vector_type(4))) int i32x4;
typedef __attribute__((ext_vector_type(4))) short s16x4;
typedef __attribute__((ext_vector_type(8))) __bf16 bf16x8;
typedef __attribute__((ext_vector_type(2))) __bf16 bf16x2;
typedef __attribute__((ext_vector_type(4))) float f32x4;
typedef __attribute__((address_space(3))) s16x4* lds_s16x4_p;
typedef __attribute__((address_space(3))) void* lds_vp;
#define DI __device__ __forceinline__
#define SB() __builtin_amdgcn_sched_barrier(0)
#define AI __attribute__((always_inline))
#define IC(x) std::integral_constant<int,(x)>{}
template<class Fn,int... I> DI void sfor_impl(Fn&& f, std::integer_sequence<int,I...>){ (f(std::integral_constant<int,I>{}),...); }
template<int N,class Fn> DI void sfor(Fn&& f){ sfor_impl(f,std::make_integer_sequence<int,N>{}); }

constexpr int S=2048,D=576,F=4608;
constexpr float LOG2E=1.4426950408889634f;
constexpr unsigned WS_HF=0;
constexpr unsigned WS_PF=32768;
constexpr unsigned WS_SF=49152;
constexpr unsigned WS_OF=53248;
constexpr unsigned WS_KF=57344;
constexpr unsigned WS_FLAG_BYTES=131072;
constexpr unsigned WS_XN=1u<<20;
constexpr unsigned WS_O =4u<<20;
constexpr unsigned WS_OL=7u<<20;
constexpr unsigned WS_KV=11u<<20;
constexpr unsigned WS_SB=8u<<20;
constexpr unsigned WS_H =16u<<20;
constexpr unsigned WS_P =40u<<20;
constexpr unsigned WS_BYTES=64u<<20;
constexpr int SLOT=24576;
constexpr unsigned TAG=1;
constexpr int K1_CODE_BYTES=94*1024;
#define K0LINES 1

DI s16x4 trr(unsigned a){ return __builtin_amdgcn_ds_read_tr16_b64_v4i16((lds_s16x4_p)(size_t)a); }
DI bf16x8 cat(s16x4 a,s16x4 b){ return __builtin_bit_cast(bf16x8,__builtin_shufflevector(a,b,0,1,2,3,4,5,6,7)); }
DI unsigned pack2(float a,float b){ bf16x2 v={(__bf16)a,(__bf16)b}; return __builtin_bit_cast(unsigned,v); }
DI float bflo(unsigned v){ return __uint_as_float(v<<16); }
DI float bfhi(unsigned v){ return __uint_as_float(v&0xffff0000u); }
DI i32x4 mk_rsrc(const void* p, unsigned bytes){
  unsigned long long a=(unsigned long long)p; i32x4 r; r[0]=(int)a; r[1]=(int)(a>>32); r[2]=(int)bytes; r[3]=0x00020000; return r;
}
template<unsigned SO,unsigned LO> DI void glds16(i32x4 rsrc, unsigned voff, unsigned sbase, unsigned lbase){
  unsigned t;
  asm volatile("s_add_u32 m0, %4, %6\n\ts_add_u32 %0, %3, %5\n\tbuffer_load_dwordx4 %1, %2, %0 offen lds":"=&s"(t):"v"(voff),"s"(rsrc),"s"(sbase),"s"(lbase),"i"(SO),"i"(LO):"memory","scc");
}
DI void touch(i32x4 rsrc, unsigned voff, unsigned soff, unsigned ldummy){
  asm volatile("s_mov_b32 m0, %3\n\ts_nop 0\n\tbuffer_load_dword %0, %1, %2 offen lds"::"v"(voff),"s"(rsrc),"s"(soff),"s"(ldummy):"memory");
}
template<bool AG,bool SC1,int OFF> DI void ldfrag(bf16x8& d,i32x4 rsrc,unsigned voff,unsigned soff){
  if constexpr(AG){ if constexpr(SC1) asm volatile("buffer_load_dwordx4 %0, %1, %2, %3 offen offset:%4 sc1":"=a"(d):"v"(voff),"s"(rsrc),"s"(soff),"i"(OFF):"memory");
                    else asm volatile("buffer_load_dwordx4 %0, %1, %2, %3 offen offset:%4":"=a"(d):"v"(voff),"s"(rsrc),"s"(soff),"i"(OFF):"memory"); }
  else { if constexpr(SC1) asm volatile("buffer_load_dwordx4 %0, %1, %2, %3 offen offset:%4 sc1":"=v"(d):"v"(voff),"s"(rsrc),"s"(soff),"i"(OFF):"memory");
         else asm volatile("buffer_load_dwordx4 %0, %1, %2, %3 offen offset:%4":"=v"(d):"v"(voff),"s"(rsrc),"s"(soff),"i"(OFF):"memory"); }
}
template<int OFF> DI void st16_sc1(u32x4 v,i32x4 rsrc,unsigned voff,unsigned soff){
  asm volatile("buffer_store_dwordx4 %0, %1, %2, %3 offen offset:%4 sc1\n\ts_nop 1"::"v"(v),"v"(voff),"s"(rsrc),"s"(soff),"i"(OFF):"memory");
}
DI void st4_sc1(unsigned v,i32x4 rsrc,unsigned voff,unsigned soff){
  asm volatile("buffer_store_dword %0, %1, %2, %3 offen sc1"::"v"(v),"v"(voff),"s"(rsrc),"s"(soff):"memory");
}
DI void ld4_sc1(unsigned& d,i32x4 rsrc,unsigned voff,unsigned soff){
  asm volatile("buffer_load_dword %0, %1, %2, %3 offen sc1":"=v"(d):"v"(voff),"s"(rsrc),"s"(soff):"memory");
}
template<int OFF> DI void ld16_sc1(u32x4& d,i32x4 rsrc,unsigned voff,unsigned soff){
  asm volatile("buffer_load_dwordx4 %0, %1, %2, %3 offen offset:%4 sc1":"=v"(d):"v"(voff),"s"(rsrc),"s"(soff),"i"(OFF):"memory");
}
template<class T> DI void pin(T& v){ asm volatile("":"+v"(v)); }
template<int N> DI void waitvm(){ asm volatile("s_waitcnt vmcnt(%0)"::"i"(N>63?63:N):"memory"); }
template<int N> DI void poll_check(unsigned& v,i32x4 rsrc,unsigned voff,unsigned soff){
  unsigned cnt;
  asm volatile("s_waitcnt vmcnt(%5)\n\t"
               "s_mov_b32 %1, 0x4000\n"
               "1:\n\t"
               "v_cmp_ne_u32 vcc, 1, %0\n\t"
               "s_nop 0\n\t"
               "s_cbranch_vccz 2f\n\t"
               "s_sleep 1\n\t"
               "buffer_load_dword %0, %2, %3, %4 offen sc1\n\t"
               "s_sub_u32 %1, %1, 1\n\t"
               "s_waitcnt vmcnt(0)\n\t"
               "s_cbranch_scc0 1b\n"
               "2:"
               :"+v"(v),"=&s"(cnt):"v"(voff),"s"(rsrc),"s"(soff),"i"(N>63?63:N):"memory","vcc","scc");
}
template<bool FIRST,bool AG> DI void mma(f32x4& acc,const bf16x8& a,const bf16x8& b){
  if constexpr(FIRST){ if constexpr(AG) asm volatile("v_mfma_f32_16x16x32_bf16 %0, %1, %2, 0":"=&v"(acc):"v"(a),"a"(b)); else asm volatile("v_mfma_f32_16x16x32_bf16 %0, %1, %2, 0":"=&v"(acc):"v"(a),"v"(b)); }
  else { if constexpr(AG) asm volatile("v_mfma_f32_16x16x32_bf16 %0, %1, %2, %0":"+v"(acc):"v"(a),"a"(b)); else asm volatile("v_mfma_f32_16x16x32_bf16 %0, %1, %2, %0":"+v"(acc):"v"(a),"v"(b)); }
}

template<int CTRL> DI float dppadd(float v){ return v+__int_as_float(__builtin_amdgcn_update_dpp(0,__float_as_int(v),CTRL,0xf,0xf,true)); }
DI float red32(float v){
  v=dppadd<0xB1>(v); v=dppadd<0x4E>(v); v=dppadd<0x141>(v); v=dppadd<0x140>(v);
  auto r=__builtin_amdgcn_permlane16_swap(__float_as_uint(v),__float_as_uint(v),false,false);
  return __uint_as_float(r[0])+__uint_as_float(r[1]);
}
__global__ __launch_bounds__(512) void k0_ln(const u16* __restrict__ x,char* __restrict__ ws,const u16* __restrict__ wqkv,const u16* __restrict__ wg,const u16* __restrict__ wu
  ){
  const int tid=threadIdx.x, lane=tid&63;
  if(tid>=256){
    const int t=tid-256;
    if(t>=192){ if(t<224) *(u32x4*)(ws+blockIdx.x*512+(t-192)*16)=u32x4{0,0,0,0}; return; }
    unsigned long long pc; int rel;
    asm volatile("s_getpc_b64 %0\n\ts_mov_b32 %1, k1_main@rel32@lo+4":"=s"(pc),"=s"(rel));
    pc+=(long long)rel;
    const int xs=blockIdx.x&7, idx=blockIdx.x>>3, mt=idx&7, j=idx>>3;
    const u32x4* cp=(const u32x4*)(pc+min(idx*3072+t*16,K1_CODE_BYTES-16));
    const char *p0,*p1;
    const int tt=t<72*K0LINES?t:0; const int row=mt*72+tt/K0LINES, ln=tt%K0LINES;
    if(j<3){ const size_t o=(size_t)row*(F*2)+((xs*3+j)*3+ln)*128; p0=(const char*)wg+o; p1=(const char*)wu+o; }
    else { p0=(const char*)wqkv+(size_t)row*(3*D*2)+((1+ln)%3)*1152+xs*144; p1=p0+140; }
    u32x4 t0_; unsigned t1,t2;
    asm volatile("global_load_dwordx4 %0, %3, off\n\tglobal_load_dword %1, %4, off\n\tglobal_load_dword %2, %5, off\n\ts_waitcnt vmcnt(0)":"=&v"(t0_),"=&v"(t1),"=&v"(t2):"v"(cp),"v"(p0),"v"(p1):"memory");
    return; }
  const int l32=lane&31, gw=blockIdx.x>>3, rb=(blockIdx.x>>1)&3, m=(blockIdx.x&1)*8+(tid>>5);
  const int row=gw*64+4*(m&~1)+2*rb+(m&1);
  const bool has3=l32<8;
  const u16* xp=x+row*D+l32*8;
  u32x4 v[3];
  v[0]=*(const u32x4*)xp; v[1]=*(const u32x4*)(xp+256); v[2]=*(const u32x4*)(xp+(has3?512:0));
  const float piv=bflo(*(const unsigned*)(x+row*D));
  float f[24]; float s=0,q=0;
  #pragma unroll
  for(int i=0;i<3;i++){ float s1=0,q1=0;
    #pragma unroll
    for(int j=0;j<4;j++){ float a=bflo(v[i][j])-piv,b=bfhi(v[i][j])-piv; f[i*8+2*j]=a; f[i*8+2*j+1]=b; s1+=a+b; q1=fmaf(a,a,fmaf(b,b,q1)); }
    if(i<2||has3){ s+=s1; q+=q1; } }
  s=red32(s); q=red32(q);
  const float mean=s*(1.0f/D), var=fmaxf(q*(1.0f/D)-mean*mean,0.f);
  const float sc=LOG2E*rsqrtf(var+1e-5f), of=-mean*sc;
  char* dst=ws+WS_XN+(size_t)gw*18*4096+rb*1024+(l32>>2)*4096+((l32&3)*16+m)*16;
  #pragma unroll
  for(int i=0;i<3;i++){
    u32x4 o;
    #pragma unroll
    for(int j=0;j<4;j++) o[j]=pack2(fmaf(f[i*8+2*j],sc,of),fmaf(f[i*8+2*j+1],sc,of));
    if(i<2||has3) *(u32x4*)(dst+i*32768)=o;
  }
}

constexpr int PDF=3, NBUF=4;
enum { K_A=0,K_Q=1,K_W=2,K_B=3,K_C=4 };
struct Stage{ int kind; int idx; int nsteps; };
constexpr int NST0=26, NST1=24;
template<int ROLE> constexpr int nstage(){ return ROLE==0?NST0:NST1; }
template<int ROLE> constexpr Stage stage(int s){
  if(ROLE==0){
    if(s<18) return Stage{K_A,s,24};
    constexpr int G[8][3]={{K_B,0,24},{K_B,1,24},{K_B,2,24},{K_C,0,24},{K_B,3,24},{K_B,4,12},{K_C,1,24},{K_C,2,24}};
    return Stage{G[s-18][0],G[s-18][1],G[s-18][2]};
  } else {
    if(s<14) return Stage{K_Q,s,s==13?10:20};
    if(s<17) return Stage{K_B,s-14,24};
    if(s<22) return Stage{K_W,s-17,s==21?10:20};
    return Stage{K_B,s-19,s==23?12:24};
  }
}
constexpr int a_mat(int s){ return s<6? (s&1) : (s%6)/3; }
constexpr int a_kp(int s){ return s<6? (s>>1) : s%3; }
constexpr int q_sec(int kk){ return kk<36? 1+(kk&1) : 0; }
constexpr int q_ks(int kk){ return kk<36? kk>>1 : kk-36; }
template<int ROLE> constexpr int stage_start(int s){ int n=0; for(int i=0;i<s;i++) n+=stage<ROLE>(i).nsteps; return n; }
template<int ROLE> constexpr int stage_n(int s){ int k=stage<ROLE>(s).kind; return (k==K_Q||k==K_W)?5:6; }
template<int ROLE> constexpr int nstep(){ return stage_start<ROLE>(nstage<ROLE>()); }
struct Frag{ int ph; int ks; int c; bool first; int win; int base; int off; int d1; };
enum { O_NONE=0,O_GL,O_XL,O_HL,O_OL,O_HST,O_FLAG,O_POLL,O_TOUCH,O_PST,O_SST,O_KVST,O_KVLD };
struct Op{ short t; short i; short w; };
constexpr int NMAX=640;
struct Tab{ Frag f[NMAX]; Op op[NMAX][4]; short bar[NMAX]; short wait[NMAX]; short chk[NMAX]; short chkw[NMAX]; int tail_after_pst; int blkwait; int voB; int baseB[3]; int wopat; int olt; int err; };
constexpr int XPRO=24;
constexpr int silu_start(int p){ return p==0?121:144*p+73; }
constexpr int fin_start(int p,int t){ return p==0? (t==0?156:188) : 144*p+138+6*t; }
#define HFD0 30
#define HFD1 40
#define HFD2 26
#define PFA 100
#define PFW 160
#define PFD 220
template<int ROLE> constexpr Tab make_tab(){
  Tab t{};
  constexpr int NS=nstage<ROLE>(), N=nstep<ROLE>();
  int ss[32]={}; { int n=0; for(int s=0;s<=NS;s++){ ss[s]=n; if(s<NS) n+=stage<ROLE>(s).nsteps; } }
  int firstB[3]={-1,-1,-1};
  for(int s=0;s<NS;s++){ const Stage st=stage<ROLE>(s); const int sl=s%6;
    for(int i=0;i<st.nsteps;i++){ Frag f{}; const int n=ss[s]+i;
      if(st.kind==K_A){ int kl=i/4,c=i%4;
        if(s==6){ if(i<18){ kl=i/3; c=i%3; } else { kl=i-18; c=3; } }
        else if(s==12&&i<12){ kl=(i%6)/2; c=(i/6)*2+(i&1); }
        else if((s==11||s==17)&&i>=12){ kl=3+((i-12)%6)/2; c=((i-12)/6)*2+(i&1); }
        f.ph=a_mat(s); f.ks=a_kp(s)*6+kl; f.c=c; f.first=(f.ks==0); f.win=sl/2; f.base=c; f.off=(sl%2)*SLOT+kl*4096; f.d1=2048;
      } else if(st.kind==K_Q){ int kk=4*st.idx+i/5; f.ph=q_sec(kk)==1?3:q_sec(kk)==2?4:5; f.ks=q_ks(kk); f.c=i%5; f.first=(f.ks==0); f.win=sl/2; f.base=0; f.off=(sl%2)*SLOT+(i/5)*5120+f.c*8; f.d1=2560;
      } else if(st.kind==K_W){ int kk=4*st.idx+i/5; f.ph=6; f.ks=(kk+12)%18; f.c=i%5; f.first=(kk==0); f.win=sl/2; f.base=1; f.off=(sl%2)*SLOT+(i/5)*5120+f.c*32; f.d1=2560;
      } else if(st.kind==K_B){ f.ph=2; f.ks=4*st.idx+i/6; f.c=i%6; f.base=f.c&1; f.off=(sl%2)*SLOT+(i/6)*6144+(f.c&~1)*32; f.d1=3072; f.first=(f.ks==0); f.win=sl/2;
        if(firstB[f.win]<0) firstB[f.win]=n;
      } else { f.ph=7; f.ks=6*st.idx+i/4; f.c=6+i%4; f.base=i%4; f.off=(sl%2)*SLOT+(i/4)*4096; f.d1=2048; f.first=(f.ks==0); f.win=sl/2; }
      t.f[n]=f; } }
  for(int n=N;n<NMAX;n++) t.f[n]=t.f[N-1];
  for(int n=0;n<NMAX;n++){ t.bar[n]=-1; t.wait[n]=-1; t.chk[n]=-1; t.chkw[n]=0; }
  for(int w=0;w<3;w++) t.baseB[w]=firstB[w]<0?-1:firstB[w]-PDF-2;
  auto put=[&](int ty,int idx,int n,int maxper)->int{
    constexpr int pref[4]={2,3,1,0};
    for(;;n++){ int used=0; for(int r=0;r<4;r++) used+=t.op[n][r].t!=O_NONE; if(used>=maxper) continue;
      for(int k=0;k<4;k++){ int r=pref[k]; if(t.op[n][r].t==O_NONE){ t.op[n][r]=Op{(short)ty,(short)idx,0}; return n; } } } };
  auto findB=[&](int ks,int c,int from)->int{ for(int n=from;n<N;n++) if(t.f[n].ph==2&&t.f[n].ks==ks&&t.f[n].c==c) return n; return -1; };
  if(ROLE==0) for(int p=0;p<3;p++) for(int tt=0;tt<2;tt++) for(int rb=0;rb<4;rb++){ int i=rb*6+5; int n=fin_start(p,tt)+i/4; t.op[n][i%4]=Op{O_HST,(short)(p*8+tt*4+rb),0}; }
  { int n=0; auto X=[&](int a,int b){ for(int i=a*4;i<b*4;i++) n=put(O_XL,i,n,1); }; auto G=[&](int s){ if(s>=6&&n<ss[s-4]-PDF) n=ss[s-4]-PDF; for(int i=0;i<stage_n<ROLE>(s);i++) n=put(O_GL,s*8+i,n,1); };
    if(ROLE==0){ G(1); X(6,8); G(2); X(8,12); G(3); G(4); X(12,15); G(5); X(15,18); G(6); G(7); G(8); }
    else { G(1); G(2); X(6,8); G(3); X(8,10); G(4); X(10,12); G(5); X(12,14); G(6); X(14,16); G(7); X(16,18); G(8); } }
  for(int s=9;s<NS;s++){ int b0=ss[s-4]-PDF, len=stage<ROLE>(s-4).nsteps, cnt=stage_n<ROLE>(s); for(int i=0;i<cnt;i++){ int n=put(O_GL,s*8+i,b0+i*len/cnt,1); if(stage<ROLE>(s).kind==K_B&&t.voB==0) t.voB=n; } }
  put(O_TOUCH,0,PFA,2); put(O_TOUCH,1,PFA+2,2);
  const int b1done=findB(17,5,0)+1;
  const int u2=findB(12,0,0);
  if(ROLE==0){
    constexpr int HFD[3]={HFD0,HFD1,HFD2};
    for(int p=0;p<3;p++){ int n=put(O_FLAG,p,fin_start(p,1)+6+HFD[p],2); put(O_TOUCH,4+p,n+1,2); }
    put(O_TOUCH,2,PFW,2); put(O_TOUCH,3,PFD,2);
    put(O_POLL,0,348,2); t.chk[364]=0;
    for(int i=0;i<48;i++) put(O_HL,i,366+2*i,1);
    for(int i=0;i<12;i++) put(O_PST,i,b1done+2+i,1);
    put(O_FLAG,6,b1done+40,2);
  } else {
    for(int i=0;i<18;i++) put(O_KVST,i,182+i,1);
    put(O_FLAG,3,224,2); put(O_POLL,2,236,2); t.chk[250]=2;
    for(int i=0;i<5;i++) put(O_KVLD,i,251+i,2);
    put(O_POLL,3,258,2);
    t.chk[271]=4;
    for(int i=24;i<48;i++) put(O_HL,i,272+(i-24),1);
    put(O_FLAG,5,290,2);
    put(O_POLL,5,306,2); t.chk[320]=5;
    const int w0=ss[17];
    for(int i=0;i<16;i++) put(O_OL,48+i,321+i,1);
    for(int i=0;i<8;i++) put(O_OL,64+i,337+i,1);
    for(int i=0;i<48;i++) put(O_OL,i,346+i,1);
    t.olt=w0+18;
    for(int i=0;i<10;i++) put(O_SST,i,u2+3+i,1);
    put(O_FLAG,4,N-2,2);
  }
  put(O_POLL,1,u2-44,2); t.chk[u2-30]=1;
  for(int i=0;i<24;i++) put(O_HL,48+i,u2-29+3*i/2,1);
  int sGL[32]={},cGL[32]={},sS[18]={},gen[18][4]={},sHST[3]={},sPoll[8]={},sKVST=0,sKVLD=0,sSST=0,sPST=0,sOST=0,sOLL=-1,cOLL=0;
  bool chkd[8]={};
  for(int i=0;i<8;i++) sPoll[i]=-1;
  int seq=0; seq+=stage_n<ROLE>(0); sGL[0]=seq-1; cGL[0]=stage_n<ROLE>(0);
  for(int i=0;i<XPRO;i++){ sS[i/4]=seq++; gen[i/4][i%4]=1; }
  int need_done=-1, nb=1;
  for(int n=0;n<N;n++){
    if(ROLE==1&&n==270){ if(sPoll[3]<sKVLD) t.err=11000; t.blkwait=seq-sPoll[3]-1; sPoll[3]=-1; chkd[3]=true;
      for(int i=0;i<24;i++){ sS[i/4]=seq++; gen[i/4][i%4]=2; }
      seq+=9; sOST=seq-1; sPoll[4]=seq++; }
    int need=-1; const Frag f=t.f[n];
    if(nb<NS && n==ss[nb]-PDF-1){ t.bar[n]=nb; need=sGL[nb]; if(cGL[nb]!=stage_n<ROLE>(nb)) t.err=1000+n; nb++; }
    if(ROLE==1&&n==t.olt){ if(cOLL!=8) t.err=2000+n; if(sOLL>need) need=sOLL; for(int r=0;r<4;r++){ gen[16][r]=3; gen[17][r]=3; } sS[16]=sS[17]=-1; }
    { const int eg=(f.ph==2||f.ph==7)?2:f.ph==6?3:1; for(int r=0;r<4;r++) if(gen[f.ks][r]!=eg) t.err=3000+n; if(sS[f.ks]>need) need=sS[f.ks]; }
    if(need>need_done){ t.wait[n]=(short)(seq-need-1); need_done=need; }
    if(t.chk[n]>=0){ if(sPoll[t.chk[n]]<0) t.err=4000+n; t.chkw[n]=(short)(seq-sPoll[t.chk[n]]-1); chkd[t.chk[n]]=true; sPoll[t.chk[n]]=-1; }
    for(int r=0;r<4;r++){ Op& o=t.op[n][r]; if(o.t==O_NONE) continue;
      switch(o.t){
        case O_GL: sGL[o.i/8]=seq; cGL[o.i/8]++; if(o.i/8>=6 && nb<=o.i/8-4) t.err=5000+n; break;
        case O_XL: sS[o.i/4]=seq; gen[o.i/4][o.i%4]=1; break;
        case O_HL: { int k=(o.i%72)/4; if(k==f.ks) t.err=6000+n; sS[k]=seq; gen[k][o.i%4]=2;
                     int c=k>=12?1:(ROLE==0?0:(k<6?3:4)); if(!chkd[c]) t.err=7000+n; } break;
        case O_OL: if(!chkd[5]) t.err=8000+n; if(o.i<64){ int k=o.i/4; if(k==f.ks) t.err=6000+n; sS[k]=seq; gen[k][o.i%4]=3; } else { sOLL=seq; cOLL++; } break;
        case O_HST: sHST[o.i/8]=seq; break;
        case O_POLL: for(int i=0;i<8;i++) if(sPoll[i]>=0) t.err=9000+n; sPoll[o.i]=seq; break;
        case O_KVST: sKVST=seq; break; case O_KVLD: if(!chkd[2]) t.err=8500+n; sKVLD=seq; break;
        case O_SST: sSST=seq; break; case O_PST: sPST=seq; break;
        case O_FLAG: { int l=o.i<3?sHST[o.i]:o.i==3?sKVST:o.i==4?sSST:o.i==5?sOST:sPST; o.w=(short)(seq-l-1); } break;
        default: break; }
      seq++; }
  }
  for(int n=N;n<NMAX;n++) for(int r=0;r<4;r++) if(t.op[n][r].t!=O_NONE) t.err=10000+n;
  return t;
}
template<int ROLE> inline constexpr Tab TAB=make_tab<ROLE>();
template<int R,int E> struct ErrIs{ static_assert(E==0,"schedule error"); };
template struct ErrIs<0,TAB<0>.err>; template struct ErrIs<1,TAB<1>.err>;

DI float dot2(unsigned a,unsigned b,float c){ return __builtin_amdgcn_fdot2_f32_bf16(__builtin_bit_cast(bf16x2,a),__builtin_bit_cast(bf16x2,b),c,false); }
template<int CTRL> DI unsigned dppmov(unsigned old,unsigned src){ return (unsigned)__builtin_amdgcn_update_dpp((int)old,(int)src,CTRL,0xf,0xf,false); }
template<int DL,int BK> DI unsigned shiftc(unsigned cur,unsigned h){
  constexpr int sh=4*BK+(DL==1?2:0);
  unsigned t; if constexpr(sh==0) t=h; else t=dppmov<0x100+sh>(h,h);
  return dppmov<0x110+2*DL>(t,cur);
}

template<int ROLE>
DI void body(const u16* __restrict__ x,const u16* __restrict__ w0,const u16* __restrict__ w1,const u16* __restrict__ wd,const u16* __restrict__ wo,u16* __restrict__ out,char* __restrict__ ws,
             unsigned lds0,int mt,int xs,int j,u64* dbg){
  const int tid=threadIdx.x, lane=tid&63, w=__builtin_amdgcn_readfirstlane(tid>>6);
  const i32x4 r0=mk_rsrc(w0,ROLE==0?D*F*2:D*3*D*2), r1=mk_rsrc(w1,ROLE==0?D*F*2:D*D*2), rd=mk_rsrc(wd,D*F*2), rw=mk_rsrc(ws,WS_BYTES);
  const unsigned l16=lane*16;
  const int g=lane>>4, q=(lane&15)>>2, p=lane&3;
  const int gw=mt*4+w;
  const int cg=xs*3+j;
  constexpr int NSTEP=nstep<ROLE>();
  #define STS(i)
  STS(0);
  unsigned voA[5];
  unsigned soA[3];
  const int rot=ROLE==0?j:0;
  if constexpr(ROLE==0){
    voA[0]=(8*((lane>>5)&1)+((lane>>3)&3))*(F*2)+(((lane&7)^(((lane>>4)&3)<<1))*16);
    #pragma unroll
    for(int k=0;k<3;k++){ int kp=k+rot; kp=kp>=3?kp-3:kp; soA[k]=(kp*192+16*(w&1)+4*(w>>1))*(F*2)+cg*3*128; }
  } else {
    #pragma unroll
    for(int i=0;i<5;i++){ int idx=64*i+lane,rho=idx/10,pc=idx%10; int k=8*((rho>>2)&3)+4*(rho>>4)+(rho&3); voA[i]=k*(3*D*2)+(pc>8?8:pc)*16; }
    soA[0]=xs*144;
  }
  auto wo_pat=[&](int i,int kind) AI -> unsigned { unsigned ln=lane; pin(ln);
    int idx=64*i+ln,rho=idx/10,pc=idx-rho*10; int gg=(rho>>2)&3,jj=4*(rho>>4)+(rho&3);
    int slot=kind? 16+(jj&1) : 8*(w&1)+jj; int od=gg&1; int feat=od?(slot<2?16+slot:slot-2):slot;
    int row=18*gg+feat+(kind?72*(jj>>1):0); return row*(D*2)+pc*16; };
  const int gW=xs*4+w, tW=9*(mt>>1)+4*(mt&1);
  const unsigned soW=tW*32;
  unsigned vo1[3],voC;
  auto set_voB=[&]() AI { unsigned ln=lane; pin(ln);
    #pragma unroll
    for(int t=0;t<3;t++){ int idx=64*t+ln; int row=idx/12,pc=idx-row*12; vo1[t]=row*(D*2)+((pc^(((row>>2)&1)<<1))*16); pin(vo1[t]); }
    if constexpr(ROLE==0){ voC=(ln>>3)*(D*2)+(((ln&7)^(((ln>>4)&3)<<1))*16); pin(voC); } };
  const int tB=(0x1e180a04u>>(8*j))&255, tC=(0x00141000u>>(8*j))&255;
  const unsigned soB=(xs*576)*(D*2)+tB*32, soC=(xs*576+8*w)*(D*2)+tC*32;
  const unsigned lA=lds0+w*(ROLE==0?1024:5120), lB=lds0+w*6144;
  const unsigned w8=8*w;
  auto do_glds=[&](auto stc,auto ic) AI {
    constexpr int s_=decltype(stc)::value,i=decltype(ic)::value; constexpr Stage st=stage<ROLE>(s_); constexpr int sl=s_%6;
    if constexpr(st.kind==K_A){
      constexpr int pr=s_/6,mat=a_mat(s_),kp=a_kp(s_);
      glds16<pr*128+i*32*(F*2),sl*SLOT+i*4096>(mat?r1:r0,voA[0],soA[kp],lA);
    } else if constexpr(st.kind==K_Q){
      constexpr auto qoff=[](int kk){ if(kk>53) kk=53; return q_ks(kk)*3+q_sec(kk); };
      constexpr int b=4*st.idx;
      constexpr unsigned KP=qoff(b)|(qoff(b+1)<<8)|(qoff(b+2)<<16)|(qoff(b+3)<<24);
      const unsigned u=(KP>>w8)&255u; const unsigned ks=(u*171u)>>9, sec=u-3*ks;
      glds16<0,sl*SLOT+i*1024>(r0,voA[i],soA[0]+(ks*96+sec)*1152,lA);
    } else if constexpr(st.kind==K_W){
      constexpr auto woff=[](int kk){ if(kk>17) kk=17; return kk<4? kk>>1 : kk<6? (kk-4)*4 : (kk-2)>>1; };
      constexpr int b=4*st.idx;
      constexpr unsigned KP=woff(b)|(woff(b+1)<<8)|(woff(b+2)<<16)|(woff(b+3)<<24);
      unsigned u=(KP>>w8)&255u;
      if constexpr(st.idx==0){ voA[i]=wo_pat(i,0); pin(voA[i]); }
      unsigned vo=voA[i];
      if constexpr(st.idx==1){ const unsigned msk=(w<2)?0u:~0u; u=(u+(mt&msk))&7u; unsigned pl=wo_pat(i,1); vo=(w<2)?pl:vo; } else u=(u+mt)&7u;
      glds16<0,sl*SLOT+i*1024>(r1,vo,soW+u*(72*D*2),lA);
    } else if constexpr(st.kind==K_B){
      constexpr auto rowb=[](int kk){ if(kk>17) kk=17; int l=kk/2; return (3*(l%3)+l/3)*2+(kk&1); };
      constexpr int b=4*st.idx;
      constexpr unsigned KP=rowb(b)|(rowb(b+1)<<8)|(rowb(b+2)<<16)|(rowb(b+3)<<24);
      const unsigned rsel=((KP>>w8)&255u)*32;
      glds16<(i/3)*16*(D*2),sl*SLOT+i*1024>(rd,vo1[i%3],soB+rsel*(D*2),lB);
    } else {
      glds16<((3*(i/2)+st.idx)*64+(i&1)*32)*(D*2),sl*SLOT+i*4096>(rd,voC,soC,lA);
    }
  };
  unsigned soX[3];
  #pragma unroll
  for(int k=0;k<3;k++){ int kp=k+rot; kp=kp>=3?kp-3:kp; soX[k]=WS_XN+(gw*18+kp*6)*4096; }
  bf16x8 xr[18][4];
  auto ld_x=[&](auto ic) AI { constexpr int i=decltype(ic)::value,ks=i/4,r=i%4; ldfrag<(ks<16),false,r*1024>(xr[ks][r],rw,l16,soX[ks/6]+(ks%6)*4096); };
  auto ld_o=[&](auto ic) AI { constexpr int i=decltype(ic)::value,sl=i/4,r=i%4; constexpr int ks=sl>=12?sl-12:sl+4; ldfrag<true,true,r*1024>(xr[sl][r],rw,l16,WS_O+(gW*18+((ks+2*mt)&15))*4096); };
  auto ld_h=[&](auto ic) AI { constexpr int i=decltype(ic)::value%72,kk=i/4,r=i%4; constexpr int l=kk/2;
    ldfrag<(kk<16),true,r*1024>(xr[kk][r],rw,l16,WS_H+(gw*144+xs*18+(3*(l%3)+l/3)*2+(kk&1))*4096); };
  const unsigned hf_line=WS_HF+((mt*8+xs)*4+w)*128;
  const unsigned vlane0=(lane==0)?0u:0x80000000u;
  unsigned pollv=0;
  constexpr int G0=stage_n<ROLE>(0);
  sfor<G0>([&](auto i) AI { do_glds(IC(0),i); });
  sfor<XPRO>([&](auto i) AI { ld_x(i); });
  unsigned bw[3][4];
  if constexpr(ROLE==0){ const int row=4*g+q; unsigned b0=lds0+row*128+32*((row>>1)&3)+8*p;
    #pragma unroll
    for(int i=0;i<3;i++)
      #pragma unroll
      for(int c=0;c<4;c++){ bw[i][c]=(b0^(c*32))+i*49152; pin(bw[i][c]); } }
  else {
    #pragma unroll
    for(int i=0;i<3;i++){ bw[i][0]=lds0+i*49152+(4*g+q)*160+36*p-4*(p&1); bw[i][1]=lds0+i*49152+(4*g+q)*160+8*p; asm volatile("":"+v"(bw[i][0]),"+v"(bw[i][1])); } }
  unsigned bb[3][2];
  auto set_bases_B=[&](auto ic) AI { constexpr int i=decltype(ic)::value; unsigned ln=lane; pin(ln); const int g2=ln>>4,row=4*g2+((ln&15)>>2),p2=ln&3;
    bb[i][0]=lds0+i*49152+row*192+8*p2+(g2&1)*32; bb[i][1]=lds0+i*49152+row*192+8*p2+((g2&1)^1)*32; pin(bb[i][0]); pin(bb[i][1]); };
  float c2v=LOG2E*LOG2E; pin(c2v);
  waitvm<XPRO>();
  __builtin_amdgcn_s_barrier();
  STS(1);
  f32x4 ga[2][5][4],ua[5][4],da[10][4];
  bf16x8 a[NBUF];
  auto faddr=[&](auto nc) AI -> unsigned { constexpr Frag f=TAB<ROLE>.f[decltype(nc)::value]; if constexpr(f.ph!=2) return bw[f.win][f.base]+f.off; else return bb[f.win][f.base]+f.off; };
  sfor<PDF>([&](auto ic) AI { constexpr int i=decltype(ic)::value; constexpr Frag f=TAB<ROLE>.f[i]; unsigned ad=faddr(ic); a[i]=cat(trr(ad),trr(ad+f.d1)); });
  float e0=0,e1=0,e2=0; u32x4 hv;
  SB();
  auto fin=[&](auto tc,auto icn,auto prc) AI {
    constexpr int t=decltype(tc)::value, i=decltype(icn)::value, pr=decltype(prc)::value; constexpr int rb=i/6, jj=i%6;
    if constexpr(jj<4){ constexpr int cc=2*t+(jj>>1), e=(jj&1)*2; unsigned v=pack2(ga[pr&1][cc][rb][e]*ua[cc][rb][e],ga[pr&1][cc][rb][e+1]*ua[cc][rb][e+1]); pin(v); hv[jj]=v; }
  };
  auto pk8=[&](const f32x4& u0,const f32x4& u1) AI -> u32x4 { u32x4 v; v[0]=pack2(u0[0],u0[1]); v[1]=pack2(u0[2],u0[3]); v[2]=pack2(u1[0],u1[1]); v[3]=pack2(u1[2],u1[3]); return v; };
  const unsigned pbase=WS_P+(xs*36*64+gw*2)*1024;
  auto pstore=[&](auto tc,auto hc) AI { constexpr int t=decltype(tc)::value,hh=decltype(hc)::value; st16_sc1<hh*1024>(pk8(da[t][2*hh],da[t][2*hh+1]),rw,l16,pbase+(t<6?tB+t:tC+t-6)*65536); };
  const unsigned pf_w=WS_PF+((mt*4+j)*4+w)*128+xs*4;
  u32x4 kq[9],vq[9],hk[5],OL[8];
  const bool odd=g&1;
  const unsigned kf_own=WS_KF+(xs*32+gw)*128, kf_prev=WS_KF+(xs*32+(gw>0?gw-1:0))*128;
  const unsigned kv_prev=gw>0?WS_KV+(xs*32+gw-1)*5120:0x80000000u;

  auto pollvo=[&](int id) AI -> unsigned { unsigned ln=lane; pin(ln);
    return id==0? ((ln%6)/2*3+(ln%6)%2)*4 : id==1? ((ln%3)*3+2)*4 : id==2? 0u : id==3? (ln%3)*12 : id==4? ((ln%3)*3+1)*4 : (ln&7)*4; };
  auto pollso=[&](int id) AI -> unsigned { return id==2? kf_prev : id==5? WS_OF+gW*128 : hf_line; };
  sfor<NSTEP>([&](auto nc) AI {
    constexpr int n=decltype(nc)::value;
    constexpr Frag f=TAB<ROLE>.f[n];
    if constexpr(ROLE==1 && n==180){
      asm volatile("s_nop 7"); SB();
      #pragma unroll
      for(int r=0;r<4;r++){
        #pragma unroll
        for(int pi=0;pi<9;pi++) kq[pi][r]=pack2(ga[0][pi/2][r][(pi&1)*2],ga[0][pi/2][r][(pi&1)*2+1]);
        unsigned t9=pack2(ga[0][4][r][2],ga[0][4][r][3]); kq[0][r]=odd?t9:kq[0][r];
      }
      #pragma unroll
      for(int pi=0;pi<9;pi++) pin(kq[pi]);
      #pragma unroll
      for(int f2=0;f2<9;f2++)
        #pragma unroll
        for(int e2_=0;e2_<2;e2_++){ const int pos=2*f2+e2_;
          unsigned A=pack2(ua[pos/4][2][pos%4],ua[pos/4][3][pos%4]), B=pack2(ua[pos/4][0][pos%4],ua[pos/4][1][pos%4]);
          if(f2==0){ unsigned A9=pack2(ua[4][2][2+e2_],ua[4][3][2+e2_]), B9=pack2(ua[4][0][2+e2_],ua[4][1][2+e2_]); A=odd?A9:A; B=odd?B9:B; }
          vq[f2][2*e2_]=A; vq[f2][2*e2_+1]=B; }
      #pragma unroll
      for(int pi=0;pi<9;pi++) pin(vq[pi]);
      SB();
    }
    if constexpr(ROLE==1 && n==270){
      STS(2);
      poll_check<TAB<ROLE>.blkwait>(pollv,rw,pollvo(3),hf_line);
      sfor<24>([&](auto i) AI { ld_h(i); });
      asm volatile("s_nop 7"); pin(hk[0]); pin(hk[1]); pin(hk[2]); pin(hk[3]); pin(hk[4]); SB();
      unsigned qp[4][9];
      #pragma unroll
      for(int r=0;r<4;r++){
        #pragma unroll
        for(int pi=0;pi<9;pi++) qp[r][pi]=pack2(ga[0][pi/2][r][(pi&1)*2],ga[0][pi/2][r][(pi&1)*2+1]);
        unsigned t9=pack2(ga[0][4][r][2],ga[0][4][r][3]); qp[r][0]=odd?t9:qp[r][0];
      }
      const int am=(lane&15)>>1;
      const float b1=(gw==0&&am<1)?-1e30f:0.f, b2=(gw==0&&am<2)?-1e30f:0.f;
      float lg[4][8];
      #pragma unroll
      for(int r=0;r<4;r++)
        #pragma unroll
        for(int ww=0;ww<8;ww++) lg[r][ww]= ww<=r?0.f : (ww-r<=4? b1:b2);
      sfor<9>([&](auto pc) AI { constexpr int pi=decltype(pc)::value;
        unsigned ke[12];
        #pragma unroll
        for(int r=0;r<4;r++){ unsigned c=kq[pi][r], h=hk[pi/4][r]; ke[8+r]=c; ke[4+r]=shiftc<1,pi%4>(c,h); if(r>0) ke[r]=shiftc<2,pi%4>(c,h); }
        #pragma unroll
        for(int r=0;r<4;r++)
          #pragma unroll
          for(int ww=0;ww<8;ww++) lg[r][ww]=dot2(ke[8+r-ww],qp[r][pi],lg[r][ww]);
      });
      const float csc=0.23570226039551584f/LOG2E;
      unsigned pp[4][5]; float rinv[4];
      #pragma unroll
      for(int r=0;r<4;r++){
        float mx=fmaxf(fmaxf(fmaxf(lg[r][0],lg[r][1]),fmaxf(lg[r][2],lg[r][3])),fmaxf(fmaxf(lg[r][4],lg[r][5]),fmaxf(lg[r][6],lg[r][7])));
        const float mo=-mx*csc; float sum=0;
        #pragma unroll
        for(int ww=0;ww<8;ww++){ lg[r][ww]=__builtin_amdgcn_exp2f(fmaf(lg[r][ww],csc,mo)); sum+=lg[r][ww]; }
        rinv[r]=__builtin_amdgcn_rcpf(sum*LOG2E);
        if(r&1){
          #pragma unroll
          for(int t=0;t<4;t++) pp[r][t]=pack2(lg[r][2*t+1],lg[r][2*t]);
        } else {
          pp[r][0]=pack2(lg[r][0],0.f);
          #pragma unroll
          for(int t=1;t<4;t++) pp[r][t]=pack2(lg[r][2*t],lg[r][2*t-1]);
          pp[r][4]=pack2(0.f,lg[r][7]);
        }
      }
      unsigned op[4][9];
      sfor<9>([&](auto fc) AI { constexpr int f2=decltype(fc)::value; constexpr int si=9+f2;
        float o2[4][2];
        #pragma unroll
        for(int e2_=0;e2_<2;e2_++){
          unsigned A=vq[f2][2*e2_],B=vq[f2][2*e2_+1],hA=hk[si/4][2*e2_],hB=hk[si/4][2*e2_+1];
          unsigned pr[6]={A,B,shiftc<1,si%4>(A,hA),shiftc<1,si%4>(B,hB),shiftc<2,si%4>(A,hA),shiftc<2,si%4>(B,hB)};
          #pragma unroll
          for(int r=0;r<4;r++){ const int s0=(r>=2)?0:1; const int nt=(r&1)?4:5; float acc=0;
            #pragma unroll
            for(int t=0;t<nt;t++) acc=dot2(pr[s0+t],pp[r][t],acc);
            o2[r][e2_]=acc*rinv[r]; }
        }
        #pragma unroll
        for(int r=0;r<4;r++) op[r][f2]=pack2(o2[r][0],o2[r][1]);
      });
      sfor<8>([&](auto ic) AI { constexpr int r=decltype(ic)::value/2,s2=decltype(ic)::value%2;
        u32x4 v={op[r][4*s2],op[r][4*s2+1],op[r][4*s2+2],op[r][4*s2+3]}; st16_sc1<r*1024>(v,rw,l16,WS_O+(gw*18+2*xs+s2)*4096); });
      { u32x4 v={op[0][8],op[1][8],op[2][8],op[3][8]}; st16_sc1<0>(v,rw,l16,WS_OL+(gw*8+xs)*1024); }
      ld4_sc1(pollv,rw,pollvo(4),hf_line);
      STS(3);
      SB();
    }
    if constexpr(TAB<ROLE>.wait[n]>=0){ waitvm<TAB<ROLE>.wait[n]>(); SB(); }
    if constexpr(TAB<ROLE>.bar[n]>=0){ __builtin_amdgcn_s_barrier(); SB(); }
    if constexpr(ROLE==1 && n==TAB<ROLE>.olt){
      #pragma unroll
      for(int hh=0;hh<8;hh++) pin(OL[hh]);
      #pragma unroll
      for(int aa=0;aa<2;aa++)
        #pragma unroll
        for(int r=0;r<4;r++){ u32x4 v={OL[4*aa][r],OL[4*aa+1][r],OL[4*aa+2][r],OL[4*aa+3][r]}; pin(v); xr[16+aa][r]=__builtin_bit_cast(bf16x8,v); }
      SB();
    }
    if constexpr(TAB<ROLE>.chk[n]>=0){ poll_check<TAB<ROLE>.chkw[n]>(pollv,rw,pollvo(TAB<ROLE>.chk[n]),pollso(TAB<ROLE>.chk[n])); SB(); }
    if constexpr(n==TAB<ROLE>.voB){ set_voB(); SB(); }
    if constexpr(n==TAB<ROLE>.baseB[0]){ set_bases_B(IC(0)); SB(); }
    if constexpr(n==TAB<ROLE>.baseB[1]){ set_bases_B(IC(1)); SB(); }
    if constexpr(n==TAB<ROLE>.baseB[2]){ set_bases_B(IC(2)); SB(); }
    constexpr int nn=n+PDF; constexpr Frag fn=TAB<ROLE>.f[nn];
    s16x4 lo;
    sfor<4>([&](auto rc) AI {
      constexpr int r=decltype(rc)::value;
      if constexpr(f.ph==1||f.ph==4) mma<f.first,(f.ks<16)>(ua[f.c][r],a[n%NBUF],xr[f.ks][r]);
      else if constexpr(f.ph==2||f.ph==7) mma<f.first,(f.ks<16)>(da[f.c][r],a[n%NBUF],xr[f.ks][r]);
      else mma<f.first,(f.ks<16)>(ga[f.ph==0?(n/144)&1:0][f.c][r],a[n%NBUF],xr[f.ks][r]);
      SB();
      if constexpr(nn<NSTEP){
        if constexpr(r==0){ lo=trr(faddr(IC(nn))); SB(); }
        if constexpr(r==1){ a[nn%NBUF]=cat(lo,trr(faddr(IC(nn))+fn.d1)); SB(); }
      }
      constexpr Op o=TAB<ROLE>.op[n][r]; constexpr int oi=o.i;
      if constexpr(o.t==O_GL){ do_glds(IC(oi/8),IC(oi%8)); SB(); }
      if constexpr(o.t==O_XL){ ld_x(IC(oi)); SB(); }
      if constexpr(o.t==O_HL){ ld_h(IC(oi)); SB(); }
      if constexpr(o.t==O_OL){ if constexpr(oi<64) ld_o(IC(oi)); else ld16_sc1<(oi&3)*1024>(OL[oi&7],rw,l16,WS_OL+(gW*8+(oi&4))*1024); SB(); }
      if constexpr(o.t==O_HST){ st16_sc1<(oi%4)*1024>(hv,rw,l16,WS_H+(gw*144+(cg*3+oi/8)*2+(oi/4)%2)*4096); SB(); }
      if constexpr(o.t==O_PST){ pstore(IC(oi/2),IC(oi%2)); SB(); }
      if constexpr(o.t==O_SST){ constexpr int t=oi/2,hh=oi%2; st16_sc1<hh*1024>(pk8(ga[0][t][2*hh],ga[0][t][2*hh+1]),rw,l16,WS_SB+((tW+t)*64+gW*2)*1024); SB(); }
      if constexpr(o.t==O_KVST){ const unsigned vk=((lane&15)>=12)?(g*4+(lane&3))*16u:0x80000000u;
          st16_sc1<(oi%16)*256>(oi<9?kq[oi%9]:vq[oi%9],rw,vk,WS_KV+(xs*32+gw)*5120+(oi/16)*4096); SB(); }
      if constexpr(o.t==O_KVLD){ const unsigned vk=q*256+(g*4+p)*16; ld16_sc1<0>(hk[oi],rw,vk,kv_prev+oi*1024); SB(); }
      if constexpr(o.t==O_TOUCH){
          unsigned ln=lane; pin(ln); const unsigned ldm=lds0+6*SLOT;
          if constexpr(oi<2 && ROLE==0) touch(oi?r1:r0,ln<36?(ln>>1)*(F*2)+(ln&1)*128:0x80000000u,gw*18*(F*2)+(cg*3+1)*128,ldm);
          else if constexpr(oi<2) touch(r0,ln<36?(ln>>1)*(3*D*2)+(ln&1)*140:0x80000000u,gw*18*(3*D*2)+(oi?0:2)*1152+xs*144,ldm);
          else if constexpr(oi==2) touch(mk_rsrc(wo,D*D*2),ln<54?ln*128:0x80000000u,((j*8+mt)*4+w)*6912,ldm);
          else if constexpr(oi==3) touch(rd,ln<54?ln*128:0x80000000u,xs*576*(D*2)+((j*8+mt)*4+w)*6912,ldm);
          else touch(rw,ln*128,WS_H+(gw*144+(cg*3+(oi-4))*2)*4096,ldm);
          SB(); }
      if constexpr(o.t==O_POLL){ ld4_sc1(pollv,rw,pollvo(oi),pollso(oi)); SB(); }
      if constexpr(o.t==O_FLAG){ waitvm<o.w>();
          st4_sc1(TAG,rw,vlane0, oi<3? hf_line+(j*3+oi)*4 : oi==3? kf_own : oi==4? WS_SF+gW*128+mt*4 : oi==5? WS_OF+gw*128+xs*4 : pf_w); SB(); }
      if constexpr(ROLE==0){
        constexpr int p=n<200?0:n<300?1:2;
        constexpr int m=n-silu_start(p);
        if constexpr(r==2 && m>=0 && m<64){ constexpr int k=m; e0=__builtin_amdgcn_exp2f(-ga[p&1][k/16][(k/4)%4][k%4]); pin(e0); SB(); }
        if constexpr(r==0 && m>=1 && m<65){ e1=fmaf(e0,c2v,c2v); pin(e1); SB(); }
        if constexpr(r==1 && m>=2 && m<66){ constexpr int k=m-2; float gv=ga[p&1][k/16][(k/4)%4][k%4]*e2; pin(gv); ga[p&1][k/16][(k/4)%4][k%4]=gv; SB(); }
        if constexpr(r==3 && m>=1 && m<65){ e2=__builtin_amdgcn_rcpf(e1); pin(e2); SB(); }
        constexpr int i0=(n-fin_start(p,0))*4+r, i1=(n-fin_start(p,1))*4+r;
        if constexpr(i0>=1&&i0<24){ fin(IC(0),IC(i0-1),IC(p)); SB(); }
        if constexpr(i1>=1&&i1<24){ fin(IC(1),IC(i1-1),IC(p)); SB(); }
      }
    });
  });
  STS(4);
  const int gc=mt*4+(xs>>1), rp=mt*8+xs;
  const unsigned pf_r=WS_PF+((mt*4+j)*4+(xs>>1))*128;
  const int tl[3]={tB+w, tB+(int)((0x03020504u>>w8)&255u), tC+w};
  asm volatile("s_nop 7"); SB();
  if constexpr(ROLE==0) sfor<8>([&](auto i) AI { pstore(IC(6+decltype(i)::value/2),IC(decltype(i)::value%2)); });
  else sfor<12>([&](auto i) AI { pstore(IC(decltype(i)::value/2),IC(decltype(i)::value%2)); });
  constexpr int NT=ROLE==0?3:2;
  const unsigned vrow=(4*(lane&14)+(lane&1))*(D*2)+g*8;
  const unsigned srow=(gc*64+4*(xs&1))*(D*2);
  u32x2 xv[3][2];
  {
    const i32x4 rx=mk_rsrc(x,S*D*2);
    #pragma unroll
    for(int i=0;i<NT;i++)
      #pragma unroll
      for(int h2=0;h2<2;h2++)
        asm volatile("buffer_load_dwordx2 %0, %1, %2, %3 offen":"=v"(xv[i][h2]):"v"(vrow),"s"(rx),"s"(srow+h2*2*(D*2)+tl[i]*32):"memory");
  }
  const unsigned vpe=(lane&8)? (WS_SF+gc*128+(lane&7)*4) : (pf_r+(lane&7)*4);
  if constexpr(ROLE==0){
    ld4_sc1(pollv,rw,vpe,0u);
    waitvm<2*NT+1>();
    st4_sc1(TAG,rw,vlane0,pf_w+32);
    poll_check<1>(pollv,rw,vpe,0u);
  } else {
    waitvm<2*NT>();
    st4_sc1(TAG,rw,vlane0,pf_w);
    ld4_sc1(pollv,rw,vpe,0u);
    poll_check<0>(pollv,rw,vpe,0u);
  }
  #pragma unroll
  for(int i=0;i<NT;i++){ pin(xv[i][0]); pin(xv[i][1]); }
  STS(5);
  auto rsw=__builtin_amdgcn_make_buffer_rsrc((void*)ws,0,WS_BYTES,0x00020000);
  auto rso=__builtin_amdgcn_make_buffer_rsrc((void*)out,0,S*D*2,0x00020000);
  auto ld_tile=[&](int t,u32x4* pv) AI {
    #pragma unroll
    for(int k=0;k<8;k++) pv[k]=__builtin_amdgcn_raw_buffer_load_b128(rsw,l16,WS_P+((k*36+t)*64+rp)*1024,16); };
  auto fin_tile=[&](int t,u32x4* pv,u32x4 sv,u32x2* xx) AI {
    float acc[8];
    #pragma unroll
    for(int e=0;e<4;e++){ acc[2*e]=bflo(pv[0][e]); acc[2*e+1]=bfhi(pv[0][e]); }
    #pragma unroll
    for(int k=1;k<8;k++)
      #pragma unroll
      for(int e=0;e<4;e++){ acc[2*e]+=bflo(pv[k][e]); acc[2*e+1]+=bfhi(pv[k][e]); }
    #pragma unroll
    for(int h2=0;h2<2;h2++){
      u32x2 o;
      #pragma unroll
      for(int e=0;e<2;e++){
        float d0=acc[h2*4+2*e],d1=acc[h2*4+2*e+1];
        float g0=d0*__builtin_amdgcn_rcpf(1.0f+__builtin_amdgcn_exp2f(-LOG2E*d0)), g1=d1*__builtin_amdgcn_rcpf(1.0f+__builtin_amdgcn_exp2f(-LOG2E*d1));
        unsigned s2=sv[h2*2+e], x2=xx[h2][e];
        o[e]=pack2(fmaf(bflo(s2),g0,bflo(x2)),fmaf(bfhi(s2),g1,bfhi(x2)));
      }
      __builtin_amdgcn_raw_buffer_store_b64(o,rso,vrow,srow+h2*2*(D*2)+t*32,0);
    }
  };
  u32x4 pv0[8],pv1[8],sv[3];
  ld_tile(tl[0],pv0); ld_tile(tl[1],pv1);
  #pragma unroll
  for(int i=0;i<NT;i++) sv[i]=__builtin_amdgcn_raw_buffer_load_b128(rsw,l16,WS_SB+(tl[i]*64+rp)*1024,16);
  SB();
  fin_tile(tl[0],pv0,sv[0],xv[0]); fin_tile(tl[1],pv1,sv[1],xv[1]);
  SB();
  if constexpr(ROLE==0){
    const unsigned vp8=(lane&7)*4;
    ld4_sc1(pollv,rw,vp8,pf_r+32); poll_check<0>(pollv,rw,vp8,pf_r+32);
    STS(6);
    ld_tile(tl[2],pv0);
    fin_tile(tl[2],pv0,sv[2],xv[2]);
  }
}

extern "C" __global__ __launch_bounds__(256,1) void k1_main(const u16* __restrict__ x,const u16* __restrict__ wqkv,const u16* __restrict__ wo,const u16* __restrict__ wg,const u16* __restrict__ wu,const u16* __restrict__ wd,u16* __restrict__ out,char* __restrict__ ws
  ){
  u64* dbg=nullptr;
  extern __shared__ __attribute__((aligned(16))) char smem[];
  const unsigned lds0=(unsigned)(size_t)(lds_vp)smem;
  const int xs=blockIdx.x&7, idx=blockIdx.x>>3, mt=idx&7, j=idx>>3;
  if(j<3) body<0>(x,wg,wu,wd,wo,out,ws,lds0,mt,xs,j,dbg);
  else body<1>(x,wqkv,wo,wd,wo,out,ws,lds0,mt,xs,j,dbg);
}

extern "C" void kernel_launch(void* const* d_in,const int* in_sizes,int n_in,void* d_out,int out_size,void* d_ws,size_t ws_size,hipStream_t stream){
  const u16* x=(const u16*)d_in[0]; const u16* wqkv=(const u16*)d_in[1]; const u16* wo=(const u16*)d_in[2];
  const u16* wg=(const u16*)d_in[4]; const u16* wu=(const u16*)d_in[5]; const u16* wd=(const u16*)d_in[6];
  char* ws=(char*)d_ws;
  (void)hipFuncSetAttribute((const void*)k1_main,hipFuncAttributeMaxDynamicSharedMemorySize,6*SLOT+1024);
  k0_ln<<<256,512,0,stream>>>(x,ws,wqkv,wg,wu
  );
  k1_main<<<256,256,6*SLOT+1024,stream>>>(x,wqkv,wo,wg,wu,wd,(u16*)d_out,ws
  );
}
```

```cpp
#include <hip/hip_runtime.h>
#include <utility>
typedef unsigned short u16; typedef unsigned long long u64;
typedef __attribute__((ext_vector_type(4))) unsigned u32x4;
typedef __attribute__((ext_vector_type(2))) unsigned u32x2;
typedef __attribute__((ext_vector_type(4))) int i32x4;
typedef __attribute__((ext_vector_type(4))) short s16x4;
typedef __attribute__((ext_vector_type(8))) __bf16 bf16x8;
typedef __attribute__((ext_vector_type(2))) __bf16 bf16x2;
typedef __attribute__((ext_vector_type(4))) float f32x4;
typedef __attribute__((address_space(3))) s16x4* lds_s16x4_p;
typedef __attribute__((address_space(3))) void* lds_vp;
#define DI __device__ __forceinline__
#define SB() __builtin_amdgcn_sched_barrier(0)
#define AI __attribute__((always_inline))
#define IC(x) std::integral_constant<int,(x)>{}
template<class Fn,int... I> DI void sfor_impl(Fn&& f, std::integer_sequence<int,I...>){ (f(std::integral_constant<int,I>{}),...); }
template<int N,class Fn> DI void sfor(Fn&& f){ sfor_impl(f,std::make_integer_sequence<int,N>{}); }

constexpr int S=2048,D=576,F=4608;
constexpr float LOG2E=1.4426950408889634f;
constexpr unsigned WS_HF=0;
constexpr unsigned WS_PF=32768;
constexpr unsigned WS_SF=49152;
constexpr unsigned WS_OF=53248;
constexpr unsigned WS_KF=57344;
constexpr unsigned WS_FLAG_BYTES=131072;
constexpr unsigned WS_XN=1u<<20;
constexpr unsigned WS_O =4u<<20;
constexpr unsigned WS_OL=7u<<20;
constexpr unsigned WS_KV=11u<<20;
constexpr unsigned WS_SB=8u<<20;
constexpr unsigned WS_H =16u<<20;
constexpr unsigned WS_P =40u<<20;
constexpr unsigned WS_BYTES=64u<<20;
constexpr int SLOT=24576;
constexpr unsigned TAG=1;
#define NTW ""
#define NTT ""
#define NTK ""
#define K0LINES 1

DI s16x4 trr(unsigned a){ return __builtin_amdgcn_ds_read_tr16_b64_v4i16((lds_s16x4_p)(size_t)a); }
DI bf16x8 cat(s16x4 a,s16x4 b){ return __builtin_bit_cast(bf16x8,__builtin_shufflevector(a,b,0,1,2,3,4,5,6,7)); }
DI unsigned pack2(float a,float b){ bf16x2 v={(__bf16)a,(__bf16)b}; return __builtin_bit_cast(unsigned,v); }
DI float bflo(unsigned v){ return __uint_as_float(v<<16); }
DI float bfhi(unsigned v){ return __uint_as_float(v&0xffff0000u); }
DI i32x4 mk_rsrc(const void* p, unsigned bytes){
  unsigned long long a=(unsigned long long)p; i32x4 r; r[0]=(int)a; r[1]=(int)(a>>32); r[2]=(int)bytes; r[3]=0x00020000; return r;
}
template<unsigned SO,unsigned LO> DI void glds16(i32x4 rsrc, unsigned voff, unsigned sbase, unsigned lbase){
  unsigned t;
  asm volatile("s_add_u32 m0, %4, %6\n\ts_add_u32 %0, %3, %5\n\tbuffer_load_dwordx4 %1, %2, %0 offen" NTW " lds":"=&s"(t):"v"(voff),"s"(rsrc),"s"(sbase),"s"(lbase),"i"(SO),"i"(LO):"memory","scc");
}
DI void touch(i32x4 rsrc, unsigned voff, unsigned soff, unsigned ldummy){
  asm volatile("s_mov_b32 m0, %3\n\ts_nop 0\n\tbuffer_load_dword %0, %1, %2 offen" NTT " lds"::"v"(voff),"s"(rsrc),"s"(soff),"s"(ldummy):"memory");
}
#define CPFA 8192
DI void code_touch(i32x4 rc, unsigned voff, unsigned ahead, unsigned ldummy){
  unsigned long long pc; asm volatile("s_getpc_b64 %0":"=s"(pc));
  touch(rc,voff,(unsigned)pc-(unsigned)rc[0]+ahead,ldummy);
}
template<bool AG,bool SC1,int OFF> DI void ldfrag(bf16x8& d,i32x4 rsrc,unsigned voff,unsigned soff){
  if constexpr(AG){ if constexpr(SC1) asm volatile("buffer_load_dwordx4 %0, %1, %2, %3 offen offset:%4 sc1":"=a"(d):"v"(voff),"s"(rsrc),"s"(soff),"i"(OFF):"memory");
                    else asm volatile("buffer_load_dwordx4 %0, %1, %2, %3 offen offset:%4":"=a"(d):"v"(voff),"s"(rsrc),"s"(soff),"i"(OFF):"memory"); }
  else { if constexpr(SC1) asm volatile("buffer_load_dwordx4 %0, %1, %2, %3 offen offset:%4 sc1":"=v"(d):"v"(voff),"s"(rsrc),"s"(soff),"i"(OFF):"memory");
         else asm volatile("buffer_load_dwordx4 %0, %1, %2, %3 offen offset:%4":"=v"(d):"v"(voff),"s"(rsrc),"s"(soff),"i"(OFF):"memory"); }
}
template<int OFF> DI void st16_sc1(u32x4 v,i32x4 rsrc,unsigned voff,unsigned soff){
  asm volatile("buffer_store_dwordx4 %0, %1, %2, %3 offen offset:%4 sc1\n\ts_nop 1"::"v"(v),"v"(voff),"s"(rsrc),"s"(soff),"i"(OFF):"memory");
}
DI void st4_sc1(unsigned v,i32x4 rsrc,unsigned voff,unsigned soff){
  asm volatile("buffer_store_dword %0, %1, %2, %3 offen sc1"::"v"(v),"v"(voff),"s"(rsrc),"s"(soff):"memory");
}
DI void ld4_sc1(unsigned& d,i32x4 rsrc,unsigned voff,unsigned soff){
  asm volatile("buffer_load_dword %0, %1, %2, %3 offen sc1":"=v"(d):"v"(voff),"s"(rsrc),"s"(soff):"memory");
}
template<int OFF> DI void ld16_sc1(u32x4& d,i32x4 rsrc,unsigned voff,unsigned soff){
  asm volatile("buffer_load_dwordx4 %0, %1, %2, %3 offen offset:%4 sc1":"=v"(d):"v"(voff),"s"(rsrc),"s"(soff),"i"(OFF):"memory");
}
template<class T> DI void pin(T& v){ asm volatile("":"+v"(v)); }
template<int N> DI void waitvm(){ asm volatile("s_waitcnt vmcnt(%0)"::"i"(N>63?63:N):"memory"); }
template<int N> DI void poll_check(unsigned& v,i32x4 rsrc,unsigned voff,unsigned soff){
  unsigned cnt;
  asm volatile("s_waitcnt vmcnt(%5)\n\t"
               "s_mov_b32 %1, 0x4000\n"
               "1:\n\t"
               "v_cmp_ne_u32 vcc, 1, %0\n\t"
               "s_nop 0\n\t"
               "s_cbranch_vccz 2f\n\t"
               "s_sleep 1\n\t"
               "buffer_load_dword %0, %2, %3, %4 offen sc1\n\t"
               "s_sub_u32 %1, %1, 1\n\t"
               "s_waitcnt vmcnt(0)\n\t"
               "s_cbranch_scc0 1b\n"
               "2:"
               :"+v"(v),"=&s"(cnt):"v"(voff),"s"(rsrc),"s"(soff),"i"(N>63?63:N):"memory","vcc","scc");
}
template<bool FIRST,bool AG> DI void mma(f32x4& acc,const bf16x8& a,const bf16x8& b){
  if constexpr(FIRST){ if constexpr(AG) asm volatile("v_mfma_f32_16x16x32_bf16 %0, %1, %2, 0":"=&v"(acc):"v"(a),"a"(b)); else asm volatile("v_mfma_f32_16x16x32_bf16 %0, %1, %2, 0":"=&v"(acc):"v"(a),"v"(b)); }
  else { if constexpr(AG) asm volatile("v_mfma_f32_16x16x32_bf16 %0, %1, %2, %0":"+v"(acc):"v"(a),"a"(b)); else asm volatile("v_mfma_f32_16x16x32_bf16 %0, %1, %2, %0":"+v"(acc):"v"(a),"v"(b)); }
}

constexpr int PDF=3, NBUF=4;
enum { K_A=0,K_Q=1,K_W=2,K_B=3,K_C=4 };
struct Stage{ int kind; int idx; int nsteps; };
constexpr int NST0=26, NST1=24;
template<int ROLE> constexpr int nstage(){ return ROLE==0?NST0:NST1; }
template<int ROLE> constexpr Stage stage(int s){
  if(ROLE==0){
    if(s<18) return Stage{K_A,s,24};
    constexpr int G[8][3]={{K_B,0,24},{K_B,1,24},{K_B,2,24},{K_B,3,24},{K_B,4,12},{K_C,0,24},{K_C,1,24},{K_C,2,24}};
    return Stage{G[s-18][0],G[s-18][1],G[s-18][2]};
  } else {
    if(s<14) return Stage{K_Q,s,s==13?10:20};
    if(s<17) return Stage{K_B,s-14,24};
    if(s<22) return Stage{K_W,s-17,s==21?10:20};
    return Stage{K_B,s-19,s==23?12:24};
  }
}
constexpr int a_mat(int s){ return s<6? (s&1) : (s%6)/3; }
constexpr int a_kp(int s){ return s<6? (s>>1) : s%3; }
constexpr int q_sec(int kk){ return kk<36? 1+(kk&1) : 0; }
constexpr int q_ks(int kk){ return kk<36? kk>>1 : kk-36; }
template<int ROLE> constexpr int stage_start(int s){ int n=0; for(int i=0;i<s;i++) n+=stage<ROLE>(i).nsteps; return n; }
template<int ROLE> constexpr int stage_n(int s){ int k=stage<ROLE>(s).kind; return (k==K_Q||k==K_W)?5:6; }
template<int ROLE> constexpr int nstep(){ return stage_start<ROLE>(nstage<ROLE>()); }
struct Frag{ int ph; int ks; int c; bool first; int win; int base; int off; int d1; int bd; };
enum { O_NONE=0,O_GL,O_XL,O_HL,O_OL,O_HST,O_FLAG,O_POLL,O_TOUCH,O_PST,O_SST,O_KVST,O_KVLD,O_POIS,O_PLD,O_CODE };
struct Op{ short t; short i; short w; };
constexpr int NMAX=640;
struct Tab{ Frag f[NMAX]; Op op[NMAX][4]; short bar[NMAX]; short wait[NMAX]; short chk[NMAX]; short chkw[NMAX]; int tail_after_pst; int blkwait; int voB; int baseB[3]; int wopat; int olt; int err; };
constexpr int XPRO=12;
constexpr int silu_start(int p){ return p==0?121:144*p+73; }
constexpr int fin_start(int p,int t){ return p==0? (t==0?156:188) : 144*p+138+6*t; }
#define HFD0 30
#define HFD1 40
#define HFD2 20
#define CPFS 90
#define P2P 24
#define P2C 10
#define TA1 0
#define G6AT 96
#define TA2 126
#define TA3 164
#define PLD0 22
#define PFD0 18
#define HLEAD 28
#define PFA 100
#define PFW 198
#define PFD 204
template<int ROLE> constexpr Tab make_tab(){
  Tab t{};
  constexpr int NS=nstage<ROLE>(), N=nstep<ROLE>();
  int ss[32]={}; { int n=0; for(int s=0;s<=NS;s++){ ss[s]=n; if(s<NS) n+=stage<ROLE>(s).nsteps; } }
  int firstB[3]={-1,-1,-1};
  for(int s=0;s<NS;s++){ const Stage st=stage<ROLE>(s); const int sl=s%6;
    for(int i=0;i<st.nsteps;i++){ Frag f{}; const int n=ss[s]+i;
      if(st.kind==K_A){ int kl=i/4,c=i%4;
        if(s==6){ if(i<18){ kl=i/3; c=i%3; } else { kl=i-18; c=3; } }
        else if(s==12&&i<12){ kl=(i%6)/2; c=(i/6)*2+(i&1); }
        else if((s==11||s==17)&&i>=12){ kl=3+((i-12)%6)/2; c=((i-12)/6)*2+(i&1); }
        f.ph=a_mat(s); f.ks=a_kp(s)*6+kl; f.c=c; f.first=(f.ks==0); f.win=sl/2; f.base=c; f.off=(sl%2)*SLOT+kl*4096; f.d1=2048;
      } else if(st.kind==K_Q){ int kk=4*st.idx+i/5; f.ph=q_sec(kk)==1?3:q_sec(kk)==2?4:5; f.ks=q_ks(kk); f.c=i%5; f.first=(f.ks==0); f.win=sl/2; f.base=0; f.off=(sl%2)*SLOT+(i/5)*5120+f.c*8; f.d1=2560;
      } else if(st.kind==K_W){ int kk=4*st.idx+i/5; f.ph=6; f.ks=(kk+12)%18; f.c=i%5; f.first=(kk==0); f.win=sl/2; f.base=1; f.off=(sl%2)*SLOT+(i/5)*5120+f.c*32; f.d1=2560;
      } else if(st.kind==K_B){ f.ph=2; f.ks=4*st.idx+i/6; f.c=i%6; f.base=f.c&1; f.off=(sl%2)*SLOT+(i/6)*6144+(f.c&~1)*32; f.d1=3072; f.first=(f.ks==0); f.win=sl/2;
        if(firstB[f.win]<0) firstB[f.win]=n;
      } else { const int kl=st.idx==2?i%6:i/4, c=st.idx==2?i/6:i%4; f.ph=7; f.ks=6*st.idx+kl; f.c=6+c; f.base=c; f.off=(sl%2)*SLOT+kl*4096; f.d1=2048; f.first=(f.ks==0); f.win=sl/2; }
      t.f[n]=f; } }
  { int cur[6]={0,0,0,0,0,0}; for(int n=0;n<N;n++){ Frag& f=t.f[n]; if(f.ph==2) f.base+=4; f.bd=(f.win-cur[f.base])*49152; cur[f.base]=f.win; } }
  for(int n=N;n<NMAX;n++) t.f[n]=t.f[N-1];
  for(int n=0;n<NMAX;n++){ t.bar[n]=-1; t.wait[n]=-1; t.chk[n]=-1; t.chkw[n]=0; }
  { int fb=N; for(int w=0;w<3;w++) if(firstB[w]>=0&&firstB[w]<fb) fb=firstB[w]; t.baseB[0]=fb-PDF-2; }
  auto put=[&](int ty,int idx,int n,int maxper)->int{
    constexpr int pref[4]={2,3,1,0};
    for(;;n++){ int used=0; for(int r=0;r<4;r++) used+=t.op[n][r].t!=O_NONE; if(used>=maxper) continue;
      for(int k=0;k<4;k++){ int r=pref[k]; if(t.op[n][r].t==O_NONE){ t.op[n][r]=Op{(short)ty,(short)idx,0}; return n; } } } };
  auto findB=[&](int ks,int c,int from)->int{ for(int n=from;n<N;n++) if(t.f[n].ph==2&&t.f[n].ks==ks&&t.f[n].c==c) return n; return -1; };
  if(ROLE==0) for(int p=0;p<3;p++) for(int tt=0;tt<2;tt++) for(int rb=0;rb<4;rb++){ int i=rb*6+5; int n=fin_start(p,tt)+i/4; t.op[n][i%4]=Op{O_HST,(short)(p*8+tt*4+rb),0}; }
  { int n=0; auto X=[&](int a,int b){ for(int i=a*4;i<b*4;i++) n=put(O_XL,i,n,1); }; auto G=[&](int s){ if(s>=6&&n<ss[s-4]-PDF) n=ss[s-4]-PDF; for(int i=0;i<stage_n<ROLE>(s);i++) n=put(O_GL,s*8+i,n,1); };
    if(ROLE==0){ X(3,4); G(1); X(4,8); G(2); X(8,12); G(3); n=put(O_TOUCH,0,n,2); n=put(O_TOUCH,1,n+TA1,2); G(4); X(12,15); G(5); X(15,18); if(n<G6AT) n=G6AT; G(6); G(7); G(8); put(O_TOUCH,8,TA2,2); put(O_TOUCH,9,TA3,2); }
    else { G(1); X(3,4); G(2); X(4,8); G(3); X(8,10); G(4); X(10,12); G(5); X(12,14); G(6); X(14,16); G(7); X(16,18); G(8); } }
  for(int s=9;s<NS;s++){ int b0=ss[s-4]-PDF, len=stage<ROLE>(s-4).nsteps, cnt=stage_n<ROLE>(s); for(int i=0;i<cnt;i++){ int n=put(O_GL,s*8+i,b0+i*len/cnt,1); if(stage<ROLE>(s).kind==K_B&&t.voB==0) t.voB=n; } }
  if(ROLE==1) put(O_TOUCH,1,PFA,2);
  const int b1done=findB(17,5,0)+1;
  const int u2=findB(12,0,0);
  for(int k=0;k*CPFS+10<N-CPFS;k++) put(O_CODE,k,k*CPFS+10,2);
  if(ROLE==0){
    constexpr int HFD[3]={HFD0,HFD1,HFD2};
    int nf2=0;
    for(int p=0;p<3;p++){ int n=put(O_FLAG,p,fin_start(p,1)+6+HFD[p],2); if(p<2) put(O_TOUCH,4+p,n+1,2); nf2=n; }
    put(O_TOUCH,2,PFW,2); put(O_TOUCH,3,PFD,2);
    for(int i=0;i<8;i++) put(O_POIS,12+i,300+4*i,1);
    put(O_POLL,0,376,2); t.chk[392]=0;
    for(int i=0;i<48;i++){ int e=432+6*(i/4)-HLEAD+(i%4); put(O_HL,i,i<16?394+2*i:e,1); }
    for(int i=0;i<8;i++){ int e=u2-HLEAD+i; put(O_HL,48+i,e>nf2+1?e:nf2+1,1); }
    put(O_POLL,1,u2-P2P,2); t.chk[u2-P2C]=1;
    for(int i=8;i<24;i++) put(O_HL,48+i,u2-P2C+1+(i-8),1);
    for(int i=0;i<12;i++) put(O_PST,i,b1done+2+i,1);
    put(O_FLAG,6,b1done+14+PFD0,2);
    for(int i=0;i<6;i++) put(O_PST,12+i,N-18+6*(i/2)+2+(i&1),1);
    put(O_POLL,6,N-PLD0-14,2); t.chk[N-PLD0]=6;
    for(int i=0;i<16;i++) put(O_PLD,i,N-PLD0+(i*(PLD0-3))/16,2);
    put(O_POLL,7,N-1,3);
  } else {
    for(int i=0;i<18;i++) put(O_KVST,i,182+i,1);
    put(O_FLAG,3,224,2); put(O_POLL,2,236,2); t.chk[250]=2;
    for(int i=0;i<5;i++) put(O_KVLD,i,251+i,2);
    put(O_POLL,3,258,2);
    t.chk[271]=4;
    for(int i=24;i<48;i++) put(O_HL,i,272+(i-24),1);
    put(O_FLAG,5,288,2);
    put(O_POLL,5,308,2); t.chk[322]=5;
    const int w0=ss[17];
    for(int i=0;i<16;i++) put(O_OL,48+i,323+i,1);
    for(int i=0;i<8;i++) put(O_OL,64+i,339+i,1);
    for(int i=0;i<48;i++) put(O_OL,i,348+i,1);
    t.olt=w0+19;
    for(int i=0;i<10;i++) put(O_SST,i,u2+3+i,1);
    put(O_FLAG,4,N-6,2);
    for(int i=0;i<12;i++) put(O_POIS,i,120+3*i,1);
    put(O_FLAG,6,232,2);
    put(O_POLL,1,u2-44,2); t.chk[u2-30]=1;
    for(int i=0;i<24;i++) put(O_HL,48+i,u2-29+3*i/2,1);
    put(O_POLL,6,u2+2,2); t.chk[u2+20]=6;
    put(O_POLL,7,N-2,2);
  }
  int sGL[32]={},cGL[32]={},sS[18]={},gen[18][4]={},sHST[3]={},sPoll[9]={},sKVST=0,sKVLD=0,sSST=0,sPST=0,sOST=0,sOLL=-1,cOLL=0;
  bool chkd[9]={};
  for(int i=0;i<9;i++) sPoll[i]=-1;
  int seq=0; seq+=stage_n<ROLE>(0); sGL[0]=seq-1; cGL[0]=stage_n<ROLE>(0);
  for(int i=0;i<XPRO;i++){ sS[i/4]=seq++; gen[i/4][i%4]=1; }
  int need_done=-1, nb=1;
  for(int n=0;n<N;n++){
    if(ROLE==1&&n==270){ if(sPoll[3]<sKVLD) t.err=11000; t.blkwait=seq-sPoll[3]-1; sPoll[3]=-1; chkd[3]=true;
      for(int i=0;i<24;i++){ sS[i/4]=seq++; gen[i/4][i%4]=2; }
      sPoll[4]=seq++; seq+=9; sOST=seq-1; }
    int need=-1; const Frag f=t.f[n];
    if(nb<NS && n==ss[nb]-PDF-1){ t.bar[n]=nb; need=sGL[nb]; if(cGL[nb]!=stage_n<ROLE>(nb)) t.err=1000+n; nb++; }
    if(ROLE==1&&n==t.olt){ if(cOLL!=8) t.err=2000+n; if(sOLL>need) need=sOLL; for(int r=0;r<4;r++){ gen[16][r]=3; gen[17][r]=3; } sS[16]=sS[17]=-1; }
    { const int eg=(f.ph==2||f.ph==7)?2:f.ph==6?3:1; for(int r=0;r<4;r++) if(gen[f.ks][r]!=eg) t.err=3000+n; if(sS[f.ks]>need) need=sS[f.ks]; }
    if(need>need_done){ t.wait[n]=(short)(seq-need-1); need_done=need; }
    if(t.chk[n]>=0){ if(sPoll[t.chk[n]]<0) t.err=4000+n; t.chkw[n]=(short)(seq-sPoll[t.chk[n]]-1); chkd[t.chk[n]]=true; sPoll[t.chk[n]]=-1; }
    for(int r=0;r<4;r++){ Op& o=t.op[n][r]; if(o.t==O_NONE) continue;
      switch(o.t){
        case O_GL: sGL[o.i/8]=seq; cGL[o.i/8]++; if(o.i/8>=6 && nb<=o.i/8-4) t.err=5000+n; break;
        case O_XL: sS[o.i/4]=seq; gen[o.i/4][o.i%4]=1; break;
        case O_HL: { int k=(o.i%72)/4; if(k==f.ks) t.err=6000+n; sS[k]=seq; gen[k][o.i%4]=2;
                     int c=k>=12?((ROLE==0&&k<14)?8:1):(ROLE==0?0:(k<6?3:4)); if(!chkd[c]) t.err=7000+n; } break;
        case O_PLD: if(!chkd[6]) t.err=7500+n; break;
        case O_OL: if(!chkd[5]) t.err=8000+n; if(o.i<64){ int k=o.i/4; if(k==f.ks) t.err=6000+n; sS[k]=seq; gen[k][o.i%4]=3; } else { sOLL=seq; cOLL++; } break;
        case O_HST: sHST[o.i/8]=seq; break;
        case O_POLL: for(int i=0;i<8;i++) if(sPoll[i]>=0) t.err=9000+n; sPoll[o.i]=seq; break;
        case O_KVST: sKVST=seq; break; case O_KVLD: if(!chkd[2]) t.err=8500+n; sKVLD=seq; break;
        case O_SST: sSST=seq; break; case O_PST: case O_POIS: sPST=seq; break;
        case O_FLAG: { int l=o.i<3?sHST[o.i]:o.i==3?sKVST:o.i==4?sSST:o.i==5?sOST:sPST; o.w=(short)(seq-l-1); if(o.i==2) chkd[8]=true; } break;
        default: break; }
      seq++; }
  }
  t.tail_after_pst=seq-sPoll[7]-1;
  for(int n=N;n<NMAX;n++) for(int r=0;r<4;r++) if(t.op[n][r].t!=O_NONE) t.err=10000+n;
  return t;
}
template<int ROLE> inline constexpr Tab TAB=make_tab<ROLE>();
template<int R,int E> struct ErrIs{ static_assert(E==0,"schedule error"); };
template struct ErrIs<0,TAB<0>.err>; template struct ErrIs<1,TAB<1>.err>;

DI void dot2a(float& acc,unsigned a,unsigned b){ asm volatile("v_dot2_f32_bf16 %0, %1, %2, %0":"+v"(acc):"v"(a),"v"(b)); }
DI void dot2i(float& acc,unsigned a,unsigned b,float c){ asm volatile("v_dot2_f32_bf16 %0, %1, %2, %3":"=v"(acc):"v"(a),"v"(b),"v"(c)); }
DI void dot2z(float& acc,unsigned a,unsigned b){ asm volatile("v_dot2_f32_bf16 %0, %1, %2, 0":"=v"(acc):"v"(a),"v"(b)); }
template<int CTRL> DI unsigned dppmov(unsigned old,unsigned src){ return (unsigned)__builtin_amdgcn_update_dpp((int)old,(int)src,CTRL,0xf,0xf,false); }
template<int DL,int BK> DI unsigned shiftc(unsigned cur,unsigned h){
  constexpr int sh=4*BK+(DL==1?2:0);
  unsigned t; if constexpr(sh==0) t=h; else t=(unsigned)__builtin_amdgcn_mov_dpp((int)h,0x100+sh,0xf,0xf,true);
  return dppmov<0x110+2*DL>(t,cur);
}

template<int ROLE>
DI void body(const u16* __restrict__ x,const u16* __restrict__ w0,const u16* __restrict__ w1,const u16* __restrict__ wd,const u16* __restrict__ wo,u16* __restrict__ out,char* __restrict__ ws,
             unsigned lds0,int mt,int xs,int j,u64* dbg){
  const int tid=threadIdx.x, lane=tid&63, w=__builtin_amdgcn_readfirstlane(tid>>6);
  const i32x4 r0=mk_rsrc(w0,ROLE==0?D*F*2:D*3*D*2), r1=mk_rsrc(w1,ROLE==0?D*F*2:D*D*2), rd=mk_rsrc(wd,D*F*2), rw=mk_rsrc(ws,WS_BYTES);
  const unsigned l16=lane*16;
  const int g=lane>>4, q=(lane&15)>>2, p=lane&3;
  const int gw=mt*4+w;
  const int cg=xs*3+j;
  constexpr int NSTEP=nstep<ROLE>();
  #define STS(i)
  STS(0);
  i32x4 rc;
  { unsigned long long pc; int r0_,r1_;
    asm volatile("s_getpc_b64 %0\n\ts_mov_b32 %1, k1_main@rel32@lo+4\n\ts_mov_b32 %2, k0_ln@rel32@lo+12":"=s"(pc),"=s"(r0_),"=s"(r1_));
    rc=mk_rsrc((const void*)(pc+(long long)r0_), r1_>r0_? (unsigned)(r1_-r0_):0u); }
  unsigned voA[5];
  unsigned soA[3];
  const int rot=ROLE==0?j:0;
  const int rotx=0;
  if constexpr(ROLE==0){
    voA[0]=(8*((lane>>5)&1)+((lane>>3)&3))*(F*2)+(((lane&7)^(((lane>>4)&3)<<1))*16);
    #pragma unroll
    for(int k=0;k<3;k++){ int kp=k+rotx; kp=kp>=3?kp-3:kp; soA[k]=(kp*192+16*(w&1)+4*(w>>1))*(F*2)+cg*3*128; }
  } else {
    #pragma unroll
    for(int i=0;i<5;i++){ int idx=64*i+lane,rho=idx/10,pc=idx%10; int k=8*((rho>>2)&3)+4*(rho>>4)+(rho&3); voA[i]=k*(3*D*2)+(pc>8?8:pc)*16; }
    soA[0]=xs*144;
  }
  auto wo_pat=[&](int i,int kind) AI -> unsigned { unsigned ln=lane; pin(ln);
    int idx=64*i+ln,rho=idx/10,pc=idx-rho*10; int gg=(rho>>2)&3,jj=4*(rho>>4)+(rho&3);
    int slot=kind? 16+(jj&1) : 8*(w&1)+jj; int od=gg&1; int feat=od?(slot<2?16+slot:slot-2):slot;
    int row=18*gg+feat+(kind?72*(jj>>1):0); return row*(D*2)+pc*16; };
  const int gW=xs*4+w, tW=9*(mt>>1)+4*(mt&1);
  const unsigned soW=tW*32;
  unsigned vo1[3],voC;
  auto set_voB=[&]() AI { unsigned ln=lane; pin(ln);
    #pragma unroll
    for(int t=0;t<3;t++){ int idx=64*t+ln; int row=idx/12,pc=idx-row*12; vo1[t]=row*(D*2)+((pc^(((row>>2)&1)<<1))*16); pin(vo1[t]); }
    if constexpr(ROLE==0){ voC=(ln>>3)*(D*2)+(((ln&7)^(((ln>>4)&3)<<1))*16); pin(voC); } };
  const int tB=(0x1e180a04u>>(8*j))&255, tC=(0x00141000u>>(8*j))&255;
  const unsigned soB=(xs*576)*(D*2)+tB*32;
  unsigned soC[3],soH[3];
  #pragma unroll
  for(int k=0;k<3;k++){ int jp=k+rot; jp=jp>=3?jp-3:jp; soC[k]=(xs*576+jp*192+8*w)*(D*2)+tC*32; soH[k]=WS_H+(gw*144+xs*18+jp*6)*4096; }
  const unsigned lA=lds0+w*(ROLE==0?1024:5120), lB=lds0+w*6144;
  const unsigned w8=8*w;
  auto do_glds=[&](auto stc,auto ic) AI {
    constexpr int s_=decltype(stc)::value,i=decltype(ic)::value; constexpr Stage st=stage<ROLE>(s_); constexpr int sl=s_%6;
    if constexpr(st.kind==K_A){
      constexpr int pr=s_/6,mat=a_mat(s_),kp=a_kp(s_);
      glds16<pr*128+i*32*(F*2),sl*SLOT+i*4096>(mat?r1:r0,voA[0],soA[kp],lA);
    } else if constexpr(st.kind==K_Q){
      constexpr auto qoff=[](int kk){ if(kk>53) kk=53; return q_ks(kk)*3+q_sec(kk); };
      constexpr int b=4*st.idx;
      constexpr unsigned KP=qoff(b)|(qoff(b+1)<<8)|(qoff(b+2)<<16)|(qoff(b+3)<<24);
      const unsigned u=(KP>>w8)&255u; const unsigned ks=(u*171u)>>9, sec=u-3*ks;
      glds16<0,sl*SLOT+i*1024>(r0,voA[i],soA[0]+(ks*96+sec)*1152,lA);
    } else if constexpr(st.kind==K_W){
      constexpr auto woff=[](int kk){ if(kk>17) kk=17; return kk<4? kk>>1 : kk<6? (kk-4)*4 : (kk-2)>>1; };
      constexpr int b=4*st.idx;
      constexpr unsigned KP=woff(b)|(woff(b+1)<<8)|(woff(b+2)<<16)|(woff(b+3)<<24);
      unsigned u=(KP>>w8)&255u;
      if constexpr(st.idx==0){ voA[i]=wo_pat(i,0); pin(voA[i]); }
      unsigned vo=voA[i];
      if constexpr(st.idx==1){ const unsigned msk=(w<2)?0u:~0u; u=(u+(mt&msk))&7u; unsigned pl=wo_pat(i,1); vo=(w<2)?pl:vo; } else u=(u+mt)&7u;
      glds16<0,sl*SLOT+i*1024>(r1,vo,soW+u*(72*D*2),lA);
    } else if constexpr(st.kind==K_B){
      constexpr auto rowb=[](int kk){ if(kk>17) kk=17; int l=kk/2; return (l%3)*6+(l/3)*2+(kk&1); };
      constexpr int b=4*st.idx;
      constexpr unsigned KP=rowb(b)|(rowb(b+1)<<8)|(rowb(b+2)<<16)|(rowb(b+3)<<24);
      unsigned rsel=((KP>>w8)&255u)+rot*6; rsel=rsel>=18?rsel-18:rsel;
      glds16<(i/3)*16*(D*2),sl*SLOT+i*1024>(rd,vo1[i%3],soB+rsel*(32*D*2),lB);
    } else {
      glds16<(st.idx*64+(i&1)*32)*(D*2),sl*SLOT+i*4096>(rd,voC,soC[i/2],lA);
    }
  };
  unsigned soX[3];
  #pragma unroll
  for(int k=0;k<3;k++){ int kp=k+rotx; kp=kp>=3?kp-3:kp; soX[k]=WS_XN+(gw*18+kp*6)*4096; }
  bf16x8 xr[18][4];
  auto ld_x=[&](auto ic) AI { constexpr int i=decltype(ic)::value,ks=i/4,r=i%4; ldfrag<(ks<16),false,r*1024>(xr[ks][r],rw,l16,soX[ks/6]+(ks%6)*4096); };
  auto ld_o=[&](auto ic) AI { constexpr int i=decltype(ic)::value,sl=i/4,r=i%4; constexpr int ks=sl>=12?sl-12:sl+4; ldfrag<true,true,r*1024>(xr[sl][r],rw,l16,WS_O+(gW*18+((ks+2*mt)&15))*4096); };
  auto ld_h=[&](auto ic) AI { constexpr int i=decltype(ic)::value%72,kk=i/4,r=i%4; constexpr int l=kk/2;
    ldfrag<(kk<16),true,r*1024>(xr[kk][r],rw,l16,soH[l%3]+((l/3)*2+(kk&1))*4096); };
  const unsigned hf_line=WS_HF+((mt*8+xs)*4+w)*128;
  const unsigned vlane0=(lane==0)?0u:0x80000000u;
  unsigned pollv=0;
  constexpr int G0=stage_n<ROLE>(0);
  sfor<G0>([&](auto i) AI { do_glds(IC(0),i); });
  sfor<XPRO>([&](auto i) AI { ld_x(i); });
  unsigned bw[6];
  if constexpr(ROLE==0){ const int row=4*g+q; unsigned b0=lds0+row*128+32*((row>>1)&3)+8*p;
    #pragma unroll
    for(int c=0;c<4;c++){ bw[c]=b0^(c*32); pin(bw[c]); } }
  else { bw[0]=lds0+(4*g+q)*160+36*p-4*(p&1); bw[1]=lds0+(4*g+q)*160+8*p; pin(bw[0]); pin(bw[1]); }
  auto set_bases_B=[&]() AI { unsigned ln=lane; pin(ln); const int g2=ln>>4,row=4*g2+((ln&15)>>2),p2=ln&3;
    bw[4]=lds0+row*192+8*p2+(g2&1)*32; bw[5]=bw[4]+(((g2&1)^1)-(g2&1))*32; pin(bw[4]); pin(bw[5]); };
  float c2v=LOG2E*LOG2E; pin(c2v);
  waitvm<XPRO>();
  __builtin_amdgcn_s_barrier();
  STS(1);
  f32x4 ga[2][5][4],ua[5][4],da[10][4];
  bf16x8 a[NBUF];
  auto faddr=[&](auto nc) AI -> unsigned { constexpr Frag f=TAB<ROLE>.f[decltype(nc)::value]; return bw[f.base]+f.off; };
  sfor<PDF>([&](auto ic) AI { constexpr int i=decltype(ic)::value; constexpr Frag f=TAB<ROLE>.f[i]; unsigned ad=faddr(ic); a[i]=cat(trr(ad),trr(ad+f.d1)); });
  float e0=0,e1=0,e2=0; u32x4 hv;
  SB();
  auto fin=[&](auto tc,auto icn,auto prc) AI {
    constexpr int t=decltype(tc)::value, i=decltype(icn)::value, pr=decltype(prc)::value; constexpr int rb=i/6, jj=i%6;
    if constexpr(jj<4){ constexpr int cc=2*t+(jj>>1), e=(jj&1)*2; unsigned v=pack2(ga[pr&1][cc][rb][e]*ua[cc][rb][e],ga[pr&1][cc][rb][e+1]*ua[cc][rb][e+1]); pin(v); hv[jj]=v; }
  };
  auto pk8=[&](const f32x4& u0,const f32x4& u1) AI -> u32x4 { u32x4 v; v[0]=pack2(u0[0],u0[1]); v[1]=pack2(u0[2],u0[3]); v[2]=pack2(u1[0],u1[1]); v[3]=pack2(u1[2],u1[3]); return v; };
  const unsigned pbase=WS_P+(xs*36*64+gw*2)*1024;
  auto pstore=[&](auto tc,auto hc) AI { constexpr int t=decltype(tc)::value,hh=decltype(hc)::value; st16_sc1<hh*1024>(pk8(da[t][2*hh],da[t][2*hh+1]),rw,l16,pbase+(t<6?tB+t:tC+t-6)*65536); };
  const unsigned pf_w=WS_PF+((mt*4+j)*4+w)*128+xs*4;
  u32x4 kq[9],vq[9],hk[5],OL[8];
  const bool odd=g&1;
  const unsigned kf_own=WS_KF+(xs*32+gw)*128, kf_prev=WS_KF+(xs*32+(gw>0?gw-1:0))*128;
  const unsigned kv_prev=gw>0?WS_KV+(xs*32+gw-1)*5120:0x80000000u;

  const int gc=mt*4+(xs>>1), rp=mt*8+xs;
  const unsigned pf_r=WS_PF+((mt*4+j)*4+(xs>>1))*128;
  const int tl0=tB+w, tl1=tB+(int)((0x03020504u>>w8)&255u);
  u32x4 pv0[8],pv1[8]; bool early=false; unsigned pld_base=0;
  auto pollvo=[&](int id) AI -> unsigned { unsigned ln=lane; pin(ln);
    return id==0? ((ln%6)/2*3+(ln%6)%2)*4 : id==1? ((ln%3)*3+2)*4 : id==2? 0u : id==3? (ln%3)*12 : id==4? ((ln%3)*3+1)*4 : (ln&7)*4; };
  auto pollso=[&](int id) AI -> unsigned { return id==2? kf_prev : id==5? WS_OF+gW*128 : id==6? pf_r : id==7? WS_SF+gc*128 : hf_line; };
  sfor<NSTEP>([&](auto nc) AI {
    constexpr int n=decltype(nc)::value;
    constexpr Frag f=TAB<ROLE>.f[n];
    if constexpr(ROLE==1 && n==180){
      asm volatile("s_nop 7"); SB();
      #pragma unroll
      for(int r=0;r<4;r++){
        #pragma unroll
        for(int pi=0;pi<9;pi++) kq[pi][r]=pack2(ga[0][pi/2][r][(pi&1)*2],ga[0][pi/2][r][(pi&1)*2+1]);
        unsigned t9=pack2(ga[0][4][r][2],ga[0][4][r][3]), t0=kq[0][r]; pin(t9); pin(t0); kq[0][r]=odd?t9:t0;
      }
      #pragma unroll
      for(int pi=0;pi<9;pi++) pin(kq[pi]);
      #pragma unroll
      for(int f2=0;f2<9;f2++)
        #pragma unroll
        for(int e2_=0;e2_<2;e2_++){ const int pos=2*f2+e2_;
          unsigned A=pack2(ua[pos/4][2][pos%4],ua[pos/4][3][pos%4]), B=pack2(ua[pos/4][0][pos%4],ua[pos/4][1][pos%4]);
          if(f2==0){ unsigned A9=pack2(ua[4][2][2+e2_],ua[4][3][2+e2_]), B9=pack2(ua[4][0][2+e2_],ua[4][1][2+e2_]); pin(A); pin(B); pin(A9); pin(B9); A=odd?A9:A; B=odd?B9:B; }
          vq[f2][2*e2_]=A; vq[f2][2*e2_+1]=B; }
      #pragma unroll
      for(int pi=0;pi<9;pi++) pin(vq[pi]);
      SB();
    }
    if constexpr(ROLE==1 && n==270){
      STS(2);
      poll_check<TAB<ROLE>.blkwait>(pollv,rw,pollvo(3),hf_line);
      asm volatile("s_nop 7"); pin(hk[0]); pin(hk[1]); pin(hk[2]); pin(hk[3]); pin(hk[4]); SB();
      unsigned qp[4][9];
      #pragma unroll
      for(int r=0;r<4;r++){
        #pragma unroll
        for(int pi=0;pi<9;pi++){ qp[r][pi]=pack2(ga[0][pi/2][r][(pi&1)*2],ga[0][pi/2][r][(pi&1)*2+1]); pin(qp[r][pi]); }
        unsigned t9=pack2(ga[0][4][r][2],ga[0][4][r][3]); pin(t9); qp[r][0]=odd?t9:qp[r][0];
      }
      const int am=(lane&15)>>1;
      const float b1=(gw==0&&am<1)?-1e30f:0.f, b2=(gw==0&&am<2)?-1e30f:0.f;
      float lg[4][8];
      sfor<9>([&](auto pc) AI { constexpr int pi=decltype(pc)::value;
        if constexpr(pi<8){ ld_h(IC(3*pi)); ld_h(IC(3*pi+1)); ld_h(IC(3*pi+2)); }
        unsigned ke[12];
        #pragma unroll
        for(int r=0;r<4;r++){ unsigned c=kq[pi][r], h=hk[pi/4][r]; ke[8+r]=c; ke[4+r]=shiftc<1,pi%4>(c,h); if(r>0) ke[r]=shiftc<2,pi%4>(c,h); }
        #pragma unroll
        for(int ww=0;ww<8;ww++)
          #pragma unroll
          for(int r=3;r>=0;r--){
            if(pi>0) dot2a(lg[r][ww],ke[8+r-ww],qp[r][pi]);
            else if(ww<=r) dot2z(lg[r][ww],ke[8+r-ww],qp[r][pi]);
            else dot2i(lg[r][ww],ke[8+r-ww],qp[r][pi],ww-r<=4?b1:b2);
          }
      });
      asm volatile("s_nop 2"); SB();
      const float csc=0.23570226039551584f/LOG2E;
      unsigned pp[4][5];
      #pragma unroll
      for(int r=0;r<4;r++){
        float mx=__builtin_fmaxf(__builtin_fmaxf(__builtin_fmaxf(lg[r][0],lg[r][1]),__builtin_fmaxf(lg[r][2],lg[r][3])),__builtin_fmaxf(__builtin_fmaxf(lg[r][4],lg[r][5]),__builtin_fmaxf(lg[r][6],lg[r][7])));
        const float mo=-mx*csc; float sum=0;
        #pragma unroll
        for(int ww=0;ww<8;ww++){ lg[r][ww]=__builtin_amdgcn_exp2f(fmaf(lg[r][ww],csc,mo)); sum+=lg[r][ww]; }
        const float ri=__builtin_amdgcn_rcpf(sum*LOG2E);
        #pragma unroll
        for(int ww=0;ww<8;ww++) lg[r][ww]*=ri;
        if(r&1){
          #pragma unroll
          for(int t=0;t<4;t++) pp[r][t]=pack2(lg[r][2*t+1],lg[r][2*t]);
        } else {
          pp[r][0]=pack2(lg[r][0],0.f);
          #pragma unroll
          for(int t=1;t<4;t++) pp[r][t]=pack2(lg[r][2*t],lg[r][2*t-1]);
          pp[r][4]=pack2(0.f,lg[r][7]);
        }
      }
      SB();
      unsigned op[4][9];
      float o2[2][4][2];
      sfor<10>([&](auto fc) AI { constexpr int f2=decltype(fc)::value; constexpr int si=9+f2;
        if constexpr(f2==5) ld4_sc1(pollv,rw,pollvo(4),hf_line);
        if constexpr(f2<9){
          unsigned pr[2][6];
          #pragma unroll
          for(int e2_=0;e2_<2;e2_++){
            unsigned A=vq[f2][2*e2_],B=vq[f2][2*e2_+1],hA=hk[si/4][2*e2_],hB=hk[si/4][2*e2_+1];
            pr[e2_][0]=A; pr[e2_][1]=B; pr[e2_][2]=shiftc<1,si%4>(A,hA); pr[e2_][3]=shiftc<1,si%4>(B,hB); pr[e2_][4]=shiftc<2,si%4>(A,hA); pr[e2_][5]=shiftc<2,si%4>(B,hB); }
          #pragma unroll
          for(int t=0;t<5;t++)
            #pragma unroll
            for(int r=3;r>=0;r--)
              #pragma unroll
              for(int e2_=0;e2_<2;e2_++){ const int s0=(r>=2)?0:1;
                if(t==0) dot2z(o2[f2&1][r][e2_],pr[e2_][s0],pp[r][0]);
                else if(t<4||!(r&1)) dot2a(o2[f2&1][r][e2_],pr[e2_][s0+t],pp[r][t]); }
        } else asm volatile("s_nop 2");
        SB();
        if constexpr(f2>0){
          #pragma unroll
          for(int r=0;r<4;r++){ op[r][f2-1]=pack2(o2[(f2-1)&1][r][0],o2[(f2-1)&1][r][1]); pin(op[r][f2-1]); }
        }
      });
      sfor<8>([&](auto ic) AI { constexpr int r=decltype(ic)::value/2,s2=decltype(ic)::value%2;
        u32x4 v={op[r][4*s2],op[r][4*s2+1],op[r][4*s2+2],op[r][4*s2+3]}; st16_sc1<r*1024>(v,rw,l16,WS_O+(gw*18+2*xs+s2)*4096); });
      { u32x4 v={op[0][8],op[1][8],op[2][8],op[3][8]}; st16_sc1<0>(v,rw,l16,WS_OL+(gw*8+xs)*1024); }
      STS(3);
      SB();
    }
    if constexpr(TAB<ROLE>.wait[n]>=0){ waitvm<TAB<ROLE>.wait[n]>(); SB(); }
    if constexpr(TAB<ROLE>.bar[n]>=0){ __builtin_amdgcn_s_barrier(); SB(); }
    if constexpr(ROLE==1 && n==TAB<ROLE>.olt){
      #pragma unroll
      for(int hh=0;hh<8;hh++) pin(OL[hh]);
      #pragma unroll
      for(int aa=0;aa<2;aa++)
        #pragma unroll
        for(int r=0;r<4;r++){ u32x4 v={OL[4*aa][r],OL[4*aa+1][r],OL[4*aa+2][r],OL[4*aa+3][r]}; pin(v); xr[16+aa][r]=__builtin_bit_cast(bf16x8,v); }
      SB();
    }
    if constexpr(ROLE==0&&TAB<ROLE>.chk[n]==6){ waitvm<TAB<ROLE>.chkw[n]>(); pin(pollv); early=__builtin_amdgcn_ballot_w64(pollv!=TAG)==0; pld_base=early?WS_P+rp*1024:0x80000000u; SB(); }
    else if constexpr(TAB<ROLE>.chk[n]>=0){ poll_check<TAB<ROLE>.chkw[n]>(pollv,rw,pollvo(TAB<ROLE>.chk[n]),pollso(TAB<ROLE>.chk[n])); SB(); }
    if constexpr(n==TAB<ROLE>.voB){ set_voB(); SB(); }
    if constexpr(n==TAB<ROLE>.baseB[0]){ set_bases_B(); SB(); }
    constexpr int nn=n+PDF; constexpr Frag fn=TAB<ROLE>.f[nn];
    s16x4 lo;
    sfor<4>([&](auto rc) AI {
      constexpr int r=decltype(rc)::value;
      if constexpr(f.ph==1||f.ph==4) mma<f.first,(f.ks<16)>(ua[f.c][r],a[n%NBUF],xr[f.ks][r]);
      else if constexpr(f.ph==2||f.ph==7) mma<f.first,(f.ks<16)>(da[f.c][r],a[n%NBUF],xr[f.ks][r]);
      else mma<f.first,(f.ks<16)>(ga[f.ph==0?(n/144)&1:0][f.c][r],a[n%NBUF],xr[f.ks][r]);
      SB();
      if constexpr(nn<NSTEP){
        if constexpr(r==0){ if constexpr(fn.bd!=0){ bw[fn.base]+=fn.bd; pin(bw[fn.base]); } lo=trr(faddr(IC(nn))); SB(); }
        if constexpr(r==1){ a[nn%NBUF]=cat(lo,trr(faddr(IC(nn))+fn.d1)); SB(); }
      }
      constexpr Op o=TAB<ROLE>.op[n][r]; constexpr int oi=o.i;
      if constexpr(o.t==O_GL){ do_glds(IC(oi/8),IC(oi%8)); SB(); }
      if constexpr(o.t==O_XL){ ld_x(IC(oi)); SB(); }
      if constexpr(o.t==O_HL){ ld_h(IC(oi)); SB(); }
      if constexpr(o.t==O_OL){ if constexpr(oi<64) ld_o(IC(oi)); else ld16_sc1<(oi&3)*1024>(OL[oi&7],rw,l16,WS_OL+(gW*8+(oi&4))*1024); SB(); }
      if constexpr(o.t==O_HST){ st16_sc1<(oi%4)*1024>(hv,rw,l16,WS_H+(gw*144+(cg*3+oi/8)*2+(oi/4)%2)*4096); SB(); }
      if constexpr(o.t==O_PST){ pstore(IC(oi/2),IC(oi%2)); SB(); }
      if constexpr(o.t==O_PLD){ ld16_sc1<0>(oi<8?pv0[oi&7]:pv1[oi&7],rw,l16,pld_base+((oi&7)*36+(oi<8?tl0:tl1))*65536); SB(); }
      if constexpr(o.t==O_CODE){ unsigned ln=lane; pin(ln); code_touch(rc,ln<16?ln*128:0x80000000u,w*2048+CPFA,lds0+6*SLOT); SB(); }
      if constexpr(o.t==O_POIS){ constexpr int t=oi/2,hh=oi%2; unsigned m1=~0u; pin(m1); u32x4 v={m1,m1,m1,m1}; st16_sc1<hh*1024>(v,rw,l16,pbase+(t<6?tB+t:tC+t-6)*65536); SB(); }
      if constexpr(o.t==O_SST){ constexpr int t=oi/2,hh=oi%2; st16_sc1<hh*1024>(pk8(ga[0][t][2*hh],ga[0][t][2*hh+1]),rw,l16,WS_SB+((tW+t)*64+gW*2)*1024); SB(); }
      if constexpr(o.t==O_KVST){ const unsigned vk=((lane&15)>=12)?(g*4+(lane&3))*16u:0x80000000u;
          st16_sc1<(oi%16)*256>(oi<9?kq[oi%9]:vq[oi%9],rw,vk,WS_KV+(xs*32+gw)*5120+(oi/16)*4096); SB(); }
      if constexpr(o.t==O_KVLD){ const unsigned vk=q*256+(g*4+p)*16; ld16_sc1<0>(hk[oi],rw,vk,kv_prev+oi*1024); SB(); }
      if constexpr(o.t==O_TOUCH){
          unsigned ln=lane; pin(ln); const unsigned ldm=lds0+6*SLOT;
          if constexpr((oi<2||oi>=8) && ROLE==0) touch((oi&1)?r1:r0,ln<18?ln*(F*2):0x80000000u,gw*18*(F*2)+(cg*3+1+oi/8)*128,ldm);
          else if constexpr(oi<2) touch(r0,ln<36?(ln>>1)*(3*D*2)+(ln&1)*140:0x80000000u,gw*18*(3*D*2)+(oi?0:2)*1152+xs*144,ldm);
          else if constexpr(oi==2) touch(mk_rsrc(wo,D*D*2),ln<54?ln*128:0x80000000u,((j*8+mt)*4+w)*6912,ldm);
          else if constexpr(oi==3) touch(rd,ln<54?ln*128:0x80000000u,xs*576*(D*2)+((j*8+mt)*4+w)*6912,ldm);
          else touch(rw,ln*128,WS_H+(gw*144+(cg*3+(oi-4))*2)*4096,ldm);
          SB(); }
      if constexpr(o.t==O_POLL){ ld4_sc1(pollv,rw,pollvo(oi),pollso(oi)); SB(); }
      if constexpr(o.t==O_FLAG){ waitvm<o.w>();
          st4_sc1(TAG,rw,vlane0, oi<3? hf_line+(j*3+oi)*4 : oi==3? kf_own : oi==4? WS_SF+gW*128+mt*4 : oi==5? WS_OF+gw*128+xs*4 : pf_w); SB(); }
      if constexpr(ROLE==0){
        constexpr int p=n<200?0:n<300?1:2;
        constexpr int m=n-silu_start(p);
        if constexpr(r==2 && m>=0 && m<64){ constexpr int k=m; e0=__builtin_amdgcn_exp2f(-ga[p&1][k/16][(k/4)%4][k%4]); pin(e0); SB(); }
        if constexpr(r==0 && m>=1 && m<65){ e1=fmaf(e0,c2v,c2v); pin(e1); SB(); }
        if constexpr(r==1 && m>=2 && m<66){ constexpr int k=m-2; float gv=ga[p&1][k/16][(k/4)%4][k%4]*e2; pin(gv); ga[p&1][k/16][(k/4)%4][k%4]=gv; SB(); }
        if constexpr(r==3 && m>=1 && m<65){ e2=__builtin_amdgcn_rcpf(e1); pin(e2); SB(); }
        constexpr int i0=(n-fin_start(p,0))*4+r, i1=(n-fin_start(p,1))*4+r;
        if constexpr(i0>=1&&i0<24){ fin(IC(0),IC(i0-1),IC(p)); SB(); }
        if constexpr(i1>=1&&i1<24){ fin(IC(1),IC(i1-1),IC(p)); SB(); }
      }
    });
  });
  STS(4);
  const int tl[3]={tl0,tl1,tC+w};
  asm volatile("s_nop 7"); SB();
  constexpr int NLATE=ROLE==0?2:12;
  if constexpr(ROLE==0) sfor<2>([&](auto i) AI { pstore(IC(9),i); });
  else sfor<12>([&](auto i) AI { pstore(IC(decltype(i)::value/2),IC(decltype(i)::value%2)); });
  constexpr int NT=ROLE==0?3:2;
  const unsigned vrow=(4*(lane&14)+(lane&1))*(D*2)+g*8;
  const unsigned srow=(gc*64+4*(xs&1))*(D*2);
  poll_check<TAB<ROLE>.tail_after_pst+NLATE>(pollv,rw,pollvo(7),pollso(7));
  if constexpr(ROLE==0){
    #pragma unroll
    for(int k=0;k<8;k++){ pin(pv0[k]); pin(pv1[k]); } }
  STS(5);
  auto rsx=__builtin_amdgcn_make_buffer_rsrc((void*)x,0,S*D*2,0x00020000);
  auto rsw=__builtin_amdgcn_make_buffer_rsrc((void*)ws,0,WS_BYTES,0x00020000);
  auto rso=__builtin_amdgcn_make_buffer_rsrc((void*)out,0,S*D*2,0x00020000);
  auto ld_tile=[&](int t,u32x4* pv) AI {
    #pragma unroll
    for(int k=0;k<8;k++) pv[k]=__builtin_amdgcn_raw_buffer_load_b128(rsw,l16,WS_P+((k*36+t)*64+rp)*1024,16); };
  auto fin_tile=[&](int t,u32x4* pv,u32x4 sv,u32x2* xx) AI {
    float acc[8];
    #pragma unroll
    for(int e=0;e<4;e++){ acc[2*e]=bflo(pv[0][e]); acc[2*e+1]=bfhi(pv[0][e]); }
    #pragma unroll
    for(int k=1;k<8;k++)
      #pragma unroll
      for(int e=0;e<4;e++){ acc[2*e]+=bflo(pv[k][e]); acc[2*e+1]+=bfhi(pv[k][e]); }
    #pragma unroll
    for(int h2=0;h2<2;h2++){
      u32x2 o;
      #pragma unroll
      for(int e=0;e<2;e++){
        float d0=acc[h2*4+2*e],d1=acc[h2*4+2*e+1];
        float g0=d0*__builtin_amdgcn_rcpf(1.0f+__builtin_amdgcn_exp2f(-LOG2E*d0)), g1=d1*__builtin_amdgcn_rcpf(1.0f+__builtin_amdgcn_exp2f(-LOG2E*d1));
        unsigned s2=sv[h2*2+e], x2=xx[h2][e];
        o[e]=pack2(fmaf(bflo(s2),g0,bflo(x2)),fmaf(bfhi(s2),g1,bfhi(x2)));
      }
      __builtin_amdgcn_raw_buffer_store_b64(o,rso,vrow,srow+h2*2*(D*2)+t*32,0);
    }
  };
  auto bad_tile=[&](u32x4* pv) AI -> unsigned { unsigned m=0;
    #pragma unroll
    for(int k=0;k<8;k++) m=max(max(m,max(pv[k][0],pv[k][1])),max(pv[k][2],pv[k][3]));
    return m; };
  auto fix_tile=[&](int t,u32x4* pv) AI {
    for(int it=0;it<8192;it++){ bool any=false;
      #pragma unroll
      for(int k=0;k<8;k++){ unsigned m=max(max(pv[k][0],pv[k][1]),max(pv[k][2],pv[k][3]));
        if(__any(m==~0u)){ any=true; pv[k]=__builtin_amdgcn_raw_buffer_load_b128(rsw,l16,WS_P+((k*36+t)*64+rp)*1024,16); } }
      if(!any) break;
      asm volatile("":::"memory"); } };
  u32x4 pv2[8],sv[3]; u32x2 xv[3][2];
  if constexpr(ROLE==0){ if(!early){ ld4_sc1(pollv,rw,pollvo(6),pollso(6)); poll_check<0>(pollv,rw,pollvo(6),pollso(6)); ld_tile(tl[0],pv0); ld_tile(tl[1],pv1); } }
  #pragma unroll
  for(int i=0;i<NT;i++){ sv[i]=__builtin_amdgcn_raw_buffer_load_b128(rsw,l16,WS_SB+(tl[i]*64+rp)*1024,16);
    #pragma unroll
    for(int h2=0;h2<2;h2++) xv[i][h2]=__builtin_amdgcn_raw_buffer_load_b64(rsx,vrow,srow+h2*2*(D*2)+tl[i]*32,0); }
  if constexpr(ROLE==0) ld_tile(tl[2],pv2); else { ld_tile(tl[0],pv0); ld_tile(tl[1],pv1); }
  SB();
  if constexpr(ROLE==1){ fix_tile(tl[0],pv0); fix_tile(tl[1],pv1); }
  fin_tile(tl[0],pv0,sv[0],xv[0]); fin_tile(tl[1],pv1,sv[1],xv[1]);
  STS(6);
  if constexpr(ROLE==0){ fix_tile(tl[2],pv2); fin_tile(tl[2],pv2,sv[2],xv[2]); }
}

extern "C" __global__ __launch_bounds__(256,1) void k1_main(const u16* __restrict__ x,const u16* __restrict__ wqkv,const u16* __restrict__ wo,const u16* __restrict__ wg,const u16* __restrict__ wu,const u16* __restrict__ wd,u16* __restrict__ out,char* __restrict__ ws
  ){
  u64* dbg=nullptr;
  extern __shared__ __attribute__((aligned(16))) char smem[];
  const unsigned lds0=(unsigned)(size_t)(lds_vp)smem;
  const int xs=blockIdx.x&7, idx=blockIdx.x>>3, mt=idx&7, j=idx>>3;
  if(j<3) body<0>(x,wg,wu,wd,wo,out,ws,lds0,mt,xs,j,dbg);
  else body<1>(x,wqkv,wo,wd,wo,out,ws,lds0,mt,xs,j,dbg);
}

template<int CTRL> DI float dppadd(float v){ return v+__int_as_float(__builtin_amdgcn_update_dpp(0,__float_as_int(v),CTRL,0xf,0xf,true)); }
DI float red32(float v){
  v=dppadd<0xB1>(v); v=dppadd<0x4E>(v); v=dppadd<0x141>(v); v=dppadd<0x140>(v);
  auto r=__builtin_amdgcn_permlane16_swap(__float_as_uint(v),__float_as_uint(v),false,false);
  return __uint_as_float(r[0])+__uint_as_float(r[1]);
}
extern "C" __global__ __launch_bounds__(512) void k0_ln(const u16* __restrict__ x,char* __restrict__ ws,const u16* __restrict__ wqkv,const u16* __restrict__ wg,const u16* __restrict__ wu
  ){
  const int tid=threadIdx.x, lane=tid&63;
  if(tid>=256){
    const int t=tid-256;
    if(t>=192){ if(t<224) *(u32x4*)(ws+blockIdx.x*512+(t-192)*16)=u32x4{0,0,0,0}; return; }
    unsigned long long pc; int rel;
    asm volatile("s_getpc_b64 %0\n\ts_mov_b32 %1, k1_main@rel32@lo+4":"=s"(pc),"=s"(rel));
    pc+=(long long)rel;
    const int xs=blockIdx.x&7, idx=blockIdx.x>>3, mt=idx&7, j=idx>>3;
    const int csz=rel<-16? -rel-16 : 0;
    const u32x4* cp=(const u32x4*)(pc+min(idx*4096+t*16,csz)); const u32x4* cp2=(const u32x4*)(pc+min(idx*4096+3072+(t&63)*16,csz));
    const char *p0,*p1,*p2;
    const int nl=j<3?K0LINES:2; const int tt=t<72*nl?t:0; const int row=mt*72+(nl==2?tt>>1:tt), ln=nl==2?tt&1:0;
    if(j<3){ const size_t o=(size_t)row*(F*2)+((xs*3+j)*3+ln)*128; p0=(const char*)wg+o; p1=(const char*)wu+o; }
    else { p0=(const char*)wqkv+(size_t)row*(3*D*2)+((1+ln)%3)*1152+xs*144; p1=p0+140; }
    p2=p0;
    unsigned t0_,t1,t2,t3,t4;
    asm volatile("global_load_dword %0, %5, off\n\tglobal_load_dword %1, %6, off" NTK "\n\tglobal_load_dword %2, %7, off" NTK "\n\tglobal_load_dword %3, %8, off\n\tglobal_load_dword %4, %9, off" NTK "\n\ts_waitcnt vmcnt(0)":"=&v"(t0_),"=&v"(t1),"=&v"(t2),"=&v"(t3),"=&v"(t4):"v"(cp),"v"(p0),"v"(p1),"v"(cp2),"v"(p2):"memory");
    return; }
  const int l32=lane&31, gw=blockIdx.x>>3, rb=(blockIdx.x>>1)&3, m=(blockIdx.x&1)*8+(tid>>5);
  const int row=gw*64+4*(m&~1)+2*rb+(m&1);
  const bool has3=l32<8;
  const u16* xp=x+row*D+l32*8;
  u32x4 v[3];
  v[0]=*(const u32x4*)xp; v[1]=*(const u32x4*)(xp+256); v[2]=*(const u32x4*)(xp+(has3?512:0));
  const float piv=bflo(*(const unsigned*)(x+row*D));
  float f[24]; float s=0,q=0;
  #pragma unroll
  for(int i=0;i<3;i++){ float s1=0,q1=0;
    #pragma unroll
    for(int j=0;j<4;j++){ float a=bflo(v[i][j])-piv,b=bfhi(v[i][j])-piv; f[i*8+2*j]=a; f[i*8+2*j+1]=b; s1+=a+b; q1=fmaf(a,a,fmaf(b,b,q1)); }
    if(i<2||has3){ s+=s1; q+=q1; } }
  s=red32(s); q=red32(q);
  const float mean=s*(1.0f/D), var=fmaxf(q*(1.0f/D)-mean*mean,0.f);
  const float sc=LOG2E*rsqrtf(var+1e-5f), of=-mean*sc;
  char* dst=ws+WS_XN+(size_t)gw*18*4096+rb*1024+(l32>>2)*4096+((l32&3)*16+m)*16;
  #pragma unroll
  for(int i=0;i<3;i++){
    u32x4 o;
    #pragma unroll
    for(int j=0;j<4;j++) o[j]=pack2(fmaf(f[i*8+2*j],sc,of),fmaf(f[i*8+2*j+1],sc,of));
    if(i<2||has3) *(u32x4*)(dst+i*32768)=o;
  }
}

extern "C" void kernel_launch(void* const* d_in,const int* in_sizes,int n_in,void* d_out,int out_size,void* d_ws,size_t ws_size,hipStream_t stream){
  const u16* x=(const u16*)d_in[0]; const u16* wqkv=(const u16*)d_in[1]; const u16* wo=(const u16*)d_in[2];
  const u16* wg=(const u16*)d_in[4]; const u16* wu=(const u16*)d_in[5]; const u16* wd=(const u16*)d_in[6];
  char* ws=(char*)d_ws;
  (void)hipFuncSetAttribute((const void*)k1_main,hipFuncAttributeMaxDynamicSharedMemorySize,6*SLOT+1024);
  k0_ln<<<256,512,0,stream>>>(x,ws,wqkv,wg,wu
  );
  k1_main<<<256,256,6*SLOT+1024,stream>>>(x,wqkv,wo,wg,wu,wd,(u16*)d_out,ws
  );
}
```

```cpp
#include <hip/hip_runtime.h>
#include <utility>
typedef unsigned short u16; typedef unsigned long long u64;
typedef __attribute__((ext_vector_type(4))) unsigned u32x4;
typedef __attribute__((ext_vector_type(2))) unsigned u32x2;
typedef __attribute__((ext_vector_type(4))) int i32x4;
typedef __attribute__((ext_vector_type(4))) short s16x4;
typedef __attribute__((ext_vector_type(8))) __bf16 bf16x8;
typedef __attribute__((ext_vector_type(2))) __bf16 bf16x2;
typedef __attribute__((ext_vector_type(4))) float f32x4;
typedef __attribute__((address_space(3))) s16x4* lds_s16x4_p;
typedef __attribute__((address_space(3))) void* lds_vp;
#define DI __device__ __forceinline__
#define SB() __builtin_amdgcn_sched_barrier(0)
#define AI __attribute__((always_inline))
#define IC(x) std::integral_constant<int,(x)>{}
template<class Fn,int... I> DI void sfor_impl(Fn&& f, std::integer_sequence<int,I...>){ (f(std::integral_constant<int,I>{}),...); }
template<int N,class Fn> DI void sfor(Fn&& f){ sfor_impl(f,std::make_integer_sequence<int,N>{}); }

constexpr int S=2048,D=576,F=4608;
constexpr float LOG2E=1.4426950408889634f;
constexpr unsigned WS_HF=0;
constexpr unsigned WS_PF=32768;
constexpr unsigned WS_SF=49152;
constexpr unsigned WS_OF=53248;
constexpr unsigned WS_KF=57344;
constexpr unsigned WS_FLAG_BYTES=131072;
constexpr unsigned WS_XN=1u<<20;
constexpr unsigned WS_O =4u<<20;
constexpr unsigned WS_OL=7u<<20;
constexpr unsigned WS_KV=11u<<20;
constexpr unsigned WS_SB=8u<<20;
constexpr unsigned WS_H =16u<<20;
constexpr unsigned WS_P =40u<<20;
constexpr unsigned WS_BYTES=64u<<20;
constexpr int SLOT=24576;
constexpr unsigned TAG=1;
#define NTW ""
#define NTT ""
#define NTK ""
#define K0LINES 1

DI s16x4 trr(unsigned a){ return __builtin_amdgcn_ds_read_tr16_b64_v4i16((lds_s16x4_p)(size_t)a); }
DI bf16x8 cat(s16x4 a,s16x4 b){ return __builtin_bit_cast(bf16x8,__builtin_shufflevector(a,b,0,1,2,3,4,5,6,7)); }
DI unsigned pack2(float a,float b){ bf16x2 v={(__bf16)a,(__bf16)b}; return __builtin_bit_cast(unsigned,v); }
DI float bflo(unsigned v){ return __uint_as_float(v<<16); }
DI float bfhi(unsigned v){ return __uint_as_float(v&0xffff0000u); }
DI i32x4 mk_rsrc(const void* p, unsigned bytes){
  unsigned long long a=(unsigned long long)p; i32x4 r; r[0]=(int)a; r[1]=(int)(a>>32); r[2]=(int)bytes; r[3]=0x00020000; return r;
}
template<unsigned SO,unsigned LO> DI void glds16(i32x4 rsrc, unsigned voff, unsigned sbase, unsigned lbase){
  unsigned t;
  asm volatile("s_add_u32 m0, %4, %6\n\ts_add_u32 %0, %3, %5\n\tbuffer_load_dwordx4 %1, %2, %0 offen" NTW " lds":"=&s"(t):"v"(voff),"s"(rsrc),"s"(sbase),"s"(lbase),"i"(SO),"i"(LO):"memory","scc");
}
DI void touch(i32x4 rsrc, unsigned voff, unsigned soff, unsigned ldummy){
  asm volatile("s_mov_b32 m0, %3\n\ts_nop 0\n\tbuffer_load_dword %0, %1, %2 offen" NTT " lds"::"v"(voff),"s"(rsrc),"s"(soff),"s"(ldummy):"memory");
}
#define CPFA 8192
DI void code_touch(i32x4 rc, unsigned voff, unsigned ahead, unsigned ldummy){
  unsigned long long pc; asm volatile("s_getpc_b64 %0":"=s"(pc));
  touch(rc,voff,(unsigned)pc-(unsigned)rc[0]+ahead,ldummy);
}
template<bool AG,bool SC1,int OFF> DI void ldfrag(bf16x8& d,i32x4 rsrc,unsigned voff,unsigned soff){
  if constexpr(AG){ if constexpr(SC1) asm volatile("buffer_load_dwordx4 %0, %1, %2, %3 offen offset:%4 sc1":"=a"(d):"v"(voff),"s"(rsrc),"s"(soff),"i"(OFF):"memory");
                    else asm volatile("buffer_load_dwordx4 %0, %1, %2, %3 offen offset:%4":"=a"(d):"v"(voff),"s"(rsrc),"s"(soff),"i"(OFF):"memory"); }
  else { if constexpr(SC1) asm volatile("buffer_load_dwordx4 %0, %1, %2, %3 offen offset:%4 sc1":"=v"(d):"v"(voff),"s"(rsrc),"s"(soff),"i"(OFF):"memory");
         else asm volatile("buffer_load_dwordx4 %0, %1, %2, %3 offen offset:%4":"=v"(d):"v"(voff),"s"(rsrc),"s"(soff),"i"(OFF):"memory"); }
}
template<int OFF> DI void st16_sc1(u32x4 v,i32x4 rsrc,unsigned voff,unsigned soff){
  asm volatile("buffer_store_dwordx4 %0, %1, %2, %3 offen offset:%4 sc1\n\ts_nop 1"::"v"(v),"v"(voff),"s"(rsrc),"s"(soff),"i"(OFF):"memory");
}
DI void st4_sc1(unsigned v,i32x4 rsrc,unsigned voff,unsigned soff){
  asm volatile("buffer_store_dword %0, %1, %2, %3 offen sc1"::"v"(v),"v"(voff),"s"(rsrc),"s"(soff):"memory");
}
DI void ld4_sc1(unsigned& d,i32x4 rsrc,unsigned voff,unsigned soff){
  asm volatile("buffer_load_dword %0, %1, %2, %3 offen sc1":"=v"(d):"v"(voff),"s"(rsrc),"s"(soff):"memory");
}
template<int OFF> DI void ld16_sc1(u32x4& d,i32x4 rsrc,unsigned voff,unsigned soff){
  asm volatile("buffer_load_dwordx4 %0, %1, %2, %3 offen offset:%4 sc1":"=v"(d):"v"(voff),"s"(rsrc),"s"(soff),"i"(OFF):"memory");
}
DI void ld16_plain(u32x4& d,i32x4 rsrc,unsigned voff,unsigned soff){
  asm volatile("buffer_load_dwordx4 %0, %1, %2, %3 offen":"=v"(d):"v"(voff),"s"(rsrc),"s"(soff):"memory");
}
template<class T> DI void pin(T& v){ asm volatile("":"+v"(v)); }
template<int N> DI void waitvm(){ asm volatile("s_waitcnt vmcnt(%0)"::"i"(N>63?63:N):"memory"); }
template<int N> DI void poll_check(unsigned& v,i32x4 rsrc,unsigned voff,unsigned soff){
  unsigned cnt;
  asm volatile("s_waitcnt vmcnt(%5)\n\t"
               "s_mov_b32 %1, 0x4000\n"
               "1:\n\t"
               "v_cmp_ne_u32 vcc, 1, %0\n\t"
               "s_nop 0\n\t"
               "s_cbranch_vccz 2f\n\t"
               "s_sleep 1\n\t"
               "buffer_load_dword %0, %2, %3, %4 offen sc1\n\t"
               "s_sub_u32 %1, %1, 1\n\t"
               "s_waitcnt vmcnt(0)\n\t"
               "s_cbranch_scc0 1b\n"
               "2:"
               :"+v"(v),"=&s"(cnt):"v"(voff),"s"(rsrc),"s"(soff),"i"(N>63?63:N):"memory","vcc","scc");
}
template<bool FIRST,bool AG> DI void mma(f32x4& acc,const bf16x8& a,const bf16x8& b){
  if constexpr(FIRST){ if constexpr(AG) asm volatile("v_mfma_f32_16x16x32_bf16 %0, %1, %2, 0":"=&v"(acc):"v"(a),"a"(b)); else asm volatile("v_mfma_f32_16x16x32_bf16 %0, %1, %2, 0":"=&v"(acc):"v"(a),"v"(b)); }
  else { if constexpr(AG) asm volatile("v_mfma_f32_16x16x32_bf16 %0, %1, %2, %0":"+v"(acc):"v"(a),"a"(b)); else asm volatile("v_mfma_f32_16x16x32_bf16 %0, %1, %2, %0":"+v"(acc):"v"(a),"v"(b)); }
}

constexpr int PDF=3, NBUF=4;
enum { K_A=0,K_Q=1,K_W=2,K_B=3,K_C=4 };
struct Stage{ int kind; int idx; int nsteps; };
constexpr int NST0=26, NST1=24;
template<int ROLE> constexpr int nstage(){ return ROLE==0?NST0:NST1; }
template<int ROLE> constexpr Stage stage(int s){
  if(ROLE==0){
    if(s<18) return Stage{K_A,s,24};
    constexpr int G[8][3]={{K_B,0,24},{K_B,1,24},{K_B,2,24},{K_B,3,24},{K_B,4,12},{K_C,0,24},{K_C,1,24},{K_C,2,24}};
    return Stage{G[s-18][0],G[s-18][1],G[s-18][2]};
  } else {
    if(s<14) return Stage{K_Q,s,s==13?10:20};
    if(s<17) return Stage{K_B,s-14,24};
    if(s<22) return Stage{K_W,s-17,s==21?10:20};
    return Stage{K_B,s-19,s==23?12:24};
  }
}
constexpr int a_mat(int s){ return s<6? (s&1) : (s%6)/3; }
constexpr int a_kp(int s){ return s<6? (s>>1) : s%3; }
constexpr int q_sec(int kk){ return kk<36? 1+(kk&1) : 0; }
constexpr int q_ks(int kk){ return kk<36? kk>>1 : kk-36; }
template<int ROLE> constexpr int stage_start(int s){ int n=0; for(int i=0;i<s;i++) n+=stage<ROLE>(i).nsteps; return n; }
template<int ROLE> constexpr int stage_n(int s){ int k=stage<ROLE>(s).kind; return (k==K_Q||k==K_W)?5:6; }
template<int ROLE> constexpr int nstep(){ return stage_start<ROLE>(nstage<ROLE>()); }
struct Frag{ int ph; int ks; int c; bool first; int win; int base; int off; int d1; int bd; };
enum { O_NONE=0,O_GL,O_XL,O_HL,O_OL,O_HST,O_FLAG,O_POLL,O_TOUCH,O_PST,O_SST,O_KVST,O_KVLD,O_POIS,O_PLD,O_CODE };
struct Op{ short t; short i; short w; };
constexpr int NMAX=640;
struct Tab{ Frag f[NMAX]; Op op[NMAX][4]; short bar[NMAX]; short wait[NMAX]; short chk[NMAX]; short chkw[NMAX]; int tail_after_pst; int blkwait; int voB; int baseB[3]; int wopat; int olt; int err; };
constexpr int XPRO=12;
constexpr int silu_start(int p){ return p==0?121:144*p+73; }
constexpr int fin_start(int p,int t){ return p==0? (t==0?156:188) : 144*p+138+6*t; }
#define HFD0 30
#define HFD1 40
#define HFD2 20
#define CPFS 90
#define P2P 24
#define P2C 10
#define PSTS 2
#define TA1 0
#define G6AT 96
#define TA2 126
#define TA3 164
#define PLD0 22
#define PFD0 18
#define HLEAD 28
#define PFA 100
#define PFW 198
#define PFD 204
template<int ROLE> constexpr Tab make_tab(){
  Tab t{};
  constexpr int NS=nstage<ROLE>(), N=nstep<ROLE>();
  int ss[32]={}; { int n=0; for(int s=0;s<=NS;s++){ ss[s]=n; if(s<NS) n+=stage<ROLE>(s).nsteps; } }
  int firstB[3]={-1,-1,-1};
  for(int s=0;s<NS;s++){ const Stage st=stage<ROLE>(s); const int sl=s%6;
    for(int i=0;i<st.nsteps;i++){ Frag f{}; const int n=ss[s]+i;
      if(st.kind==K_A){ int kl=i/4,c=i%4;
        if(s==6){ if(i<18){ kl=i/3; c=i%3; } else { kl=i-18; c=3; } }
        else if(s==12&&i<12){ kl=(i%6)/2; c=(i/6)*2+(i&1); }
        else if((s==11||s==17)&&i>=12){ kl=3+((i-12)%6)/2; c=((i-12)/6)*2+(i&1); }
        f.ph=a_mat(s); f.ks=a_kp(s)*6+kl; f.c=c; f.first=(f.ks==0); f.win=sl/2; f.base=c; f.off=(sl%2)*SLOT+kl*4096; f.d1=2048;
      } else if(st.kind==K_Q){ int kk=4*st.idx+i/5; f.ph=q_sec(kk)==1?3:q_sec(kk)==2?4:5; f.ks=q_ks(kk); f.c=i%5; f.first=(f.ks==0); f.win=sl/2; f.base=0; f.off=(sl%2)*SLOT+(i/5)*5120+f.c*8; f.d1=2560;
      } else if(st.kind==K_W){ int kk=4*st.idx+i/5; f.ph=6; f.ks=(kk+12)%18; f.c=i%5; f.first=(kk==0); f.win=sl/2; f.base=1; f.off=(sl%2)*SLOT+(i/5)*5120+f.c*32; f.d1=2560;
      } else if(st.kind==K_B){ f.ph=2; f.ks=4*st.idx+i/6; f.c=i%6; f.base=f.c&1; f.off=(sl%2)*SLOT+(i/6)*6144+(f.c&~1)*32; f.d1=3072; f.first=(f.ks==0); f.win=sl/2;
        if(firstB[f.win]<0) firstB[f.win]=n;
      } else { const int kl=st.idx==2?i%6:i/4, c=st.idx==2?i/6:i%4; f.ph=7; f.ks=6*st.idx+kl; f.c=6+c; f.base=c; f.off=(sl%2)*SLOT+kl*4096; f.d1=2048; f.first=(f.ks==0); f.win=sl/2; }
      t.f[n]=f; } }
  { int cur[6]={0,0,0,0,0,0}; for(int n=0;n<N;n++){ Frag& f=t.f[n]; if(f.ph==2) f.base+=4; f.bd=(f.win-cur[f.base])*49152; cur[f.base]=f.win; } }
  for(int n=N;n<NMAX;n++) t.f[n]=t.f[N-1];
  for(int n=0;n<NMAX;n++){ t.bar[n]=-1; t.wait[n]=-1; t.chk[n]=-1; t.chkw[n]=0; }
  { int fb=N; for(int w=0;w<3;w++) if(firstB[w]>=0&&firstB[w]<fb) fb=firstB[w]; t.baseB[0]=fb-PDF-2; }
  auto put=[&](int ty,int idx,int n,int maxper)->int{
    constexpr int pref[4]={2,3,1,0};
    for(;;n++){ int used=0; for(int r=0;r<4;r++) used+=t.op[n][r].t!=O_NONE; if(used>=maxper) continue;
      for(int k=0;k<4;k++){ int r=pref[k]; if(t.op[n][r].t==O_NONE){ t.op[n][r]=Op{(short)ty,(short)idx,0}; return n; } } } };
  auto findB=[&](int ks,int c,int from)->int{ for(int n=from;n<N;n++) if(t.f[n].ph==2&&t.f[n].ks==ks&&t.f[n].c==c) return n; return -1; };
  if(ROLE==0) for(int p=0;p<3;p++) for(int tt=0;tt<2;tt++) for(int rb=0;rb<4;rb++){ int i=rb*6+5; int n=fin_start(p,tt)+i/4; t.op[n][i%4]=Op{O_HST,(short)(p*8+tt*4+rb),0}; }
  { int n=0; auto X=[&](int a,int b){ for(int i=a*4;i<b*4;i++) n=put(O_XL,i,n,1); }; auto G=[&](int s){ if(s>=6&&n<ss[s-4]-PDF) n=ss[s-4]-PDF; for(int i=0;i<stage_n<ROLE>(s);i++) n=put(O_GL,s*8+i,n,1); };
    if(ROLE==0){ X(3,4); G(1); X(4,8); G(2); X(8,12); G(3); n=put(O_TOUCH,0,n,2); n=put(O_TOUCH,1,n+TA1,2); G(4); X(12,15); G(5); X(15,18); if(n<G6AT) n=G6AT; G(6); G(7); G(8); put(O_TOUCH,8,TA2,2); put(O_TOUCH,9,TA3,2); }
    else { G(1); X(3,4); G(2); X(4,8); G(3); X(8,10); G(4); X(10,12); G(5); X(12,14); G(6); X(14,16); G(7); X(16,18); G(8); } }
  for(int s=9;s<NS;s++){ int b0=ss[s-4]-PDF, len=stage<ROLE>(s-4).nsteps, cnt=stage_n<ROLE>(s); for(int i=0;i<cnt;i++){ int n=put(O_GL,s*8+i,b0+i*len/cnt,1); if(stage<ROLE>(s).kind==K_B&&t.voB==0) t.voB=n; } }
  if(ROLE==1) put(O_TOUCH,1,PFA,2);
  const int b1done=findB(17,5,0)+1;
  const int u2=findB(12,0,0);
  for(int k=0;k*CPFS+10<N-CPFS;k++) put(O_CODE,k,k*CPFS+10,2);
  if(ROLE==0){
    constexpr int HFD[3]={HFD0,HFD1,HFD2};
    int nf2=0;
    for(int p=0;p<3;p++){ int n=put(O_FLAG,p,fin_start(p,1)+6+HFD[p],2); if(p<2) put(O_TOUCH,4+p,n+1,2); nf2=n; }
    put(O_TOUCH,2,PFW,2); put(O_TOUCH,3,PFD,2);
    for(int i=0;i<8;i++) put(O_POIS,12+i,300+4*i,1);
    put(O_POLL,0,376,2); t.chk[392]=0;
    for(int i=0;i<48;i++){ int e=432+6*(i/4)-HLEAD+(i%4); put(O_HL,i,i<16?394+2*i:e,1); }
    for(int i=0;i<8;i++){ int e=u2-HLEAD+i; put(O_HL,48+i,e>nf2+1?e:nf2+1,1); }
    put(O_POLL,1,u2-P2P,2); t.chk[u2-P2C]=1;
    for(int i=8;i<24;i++) put(O_HL,48+i,u2-P2C+1+(i-8),1);
    for(int i=0;i<12;i++) put(O_PST,i,b1done+2+PSTS*i,1);
    put(O_FLAG,6,b1done+2+12*PSTS+PFD0,2);
    for(int i=0;i<6;i++) put(O_PST,12+i,N-18+6*(i/2)+2+(i&1),1);
    for(int i=0;i<3;i++) put(O_PLD,i,b1done+2+12*PSTS+3*i,1);
    put(O_POLL,7,N-8,3);
  } else {
    for(int i=0;i<18;i++) put(O_KVST,i,182+i,1);
    put(O_FLAG,3,224,2); put(O_POLL,2,236,2); t.chk[250]=2;
    for(int i=0;i<5;i++) put(O_KVLD,i,251+i,2);
    put(O_POLL,3,258,2);
    t.chk[271]=4;
    for(int i=24;i<48;i++) put(O_HL,i,272+(i-24),1);
    put(O_FLAG,5,288,2);
    put(O_POLL,5,308,2); t.chk[322]=5;
    const int w0=ss[17];
    for(int i=0;i<16;i++) put(O_OL,48+i,323+i,1);
    for(int i=0;i<8;i++) put(O_OL,64+i,339+i,1);
    for(int i=0;i<48;i++) put(O_OL,i,348+i,1);
    t.olt=w0+19;
    for(int i=0;i<10;i++) put(O_SST,i,u2+3+i,1);
    put(O_FLAG,4,N-6,2);
    for(int i=0;i<12;i++) put(O_POIS,i,120+3*i,1);
    put(O_FLAG,6,232,2);
    put(O_POLL,1,u2-44,2); t.chk[u2-30]=1;
    for(int i=0;i<24;i++) put(O_HL,48+i,u2-29+3*i/2,1);
    put(O_POLL,6,u2+2,2); t.chk[u2+20]=6;
    for(int i=0;i<2;i++) put(O_PLD,i,u2+21+3*i,1);
    put(O_POLL,7,N-2,2);
  }
  int sGL[32]={},cGL[32]={},sS[18]={},gen[18][4]={},sHST[3]={},sPoll[9]={},sKVST=0,sKVLD=0,sSST=0,sPST=0,sOST=0,sOLL=-1,cOLL=0;
  bool chkd[9]={};
  for(int i=0;i<9;i++) sPoll[i]=-1;
  int seq=0; seq+=stage_n<ROLE>(0); sGL[0]=seq-1; cGL[0]=stage_n<ROLE>(0);
  for(int i=0;i<XPRO;i++){ sS[i/4]=seq++; gen[i/4][i%4]=1; }
  int need_done=-1, nb=1;
  for(int n=0;n<N;n++){
    if(ROLE==1&&n==270){ if(sPoll[3]<sKVLD) t.err=11000; t.blkwait=seq-sPoll[3]-1; sPoll[3]=-1; chkd[3]=true;
      for(int i=0;i<24;i++){ sS[i/4]=seq++; gen[i/4][i%4]=2; }
      sPoll[4]=seq++; seq+=9; sOST=seq-1; }
    int need=-1; const Frag f=t.f[n];
    if(nb<NS && n==ss[nb]-PDF-1){ t.bar[n]=nb; need=sGL[nb]; if(cGL[nb]!=stage_n<ROLE>(nb)) t.err=1000+n; nb++; }
    if(ROLE==1&&n==t.olt){ if(cOLL!=8) t.err=2000+n; if(sOLL>need) need=sOLL; for(int r=0;r<4;r++){ gen[16][r]=3; gen[17][r]=3; } sS[16]=sS[17]=-1; }
    { const int eg=(f.ph==2||f.ph==7)?2:f.ph==6?3:1; for(int r=0;r<4;r++) if(gen[f.ks][r]!=eg) t.err=3000+n; if(sS[f.ks]>need) need=sS[f.ks]; }
    if(need>need_done){ t.wait[n]=(short)(seq-need-1); need_done=need; }
    if(t.chk[n]>=0){ if(sPoll[t.chk[n]]<0) t.err=4000+n; t.chkw[n]=(short)(seq-sPoll[t.chk[n]]-1); chkd[t.chk[n]]=true; sPoll[t.chk[n]]=-1; }
    for(int r=0;r<4;r++){ Op& o=t.op[n][r]; if(o.t==O_NONE) continue;
      switch(o.t){
        case O_GL: sGL[o.i/8]=seq; cGL[o.i/8]++; if(o.i/8>=6 && nb<=o.i/8-4) t.err=5000+n; break;
        case O_XL: sS[o.i/4]=seq; gen[o.i/4][o.i%4]=1; break;
        case O_HL: { int k=(o.i%72)/4; if(k==f.ks) t.err=6000+n; sS[k]=seq; gen[k][o.i%4]=2;
                     int c=k>=12?((ROLE==0&&k<14)?8:1):(ROLE==0?0:(k<6?3:4)); if(!chkd[c]) t.err=7000+n; } break;
        case O_OL: if(!chkd[5]) t.err=8000+n; if(o.i<64){ int k=o.i/4; if(k==f.ks) t.err=6000+n; sS[k]=seq; gen[k][o.i%4]=3; } else { sOLL=seq; cOLL++; } break;
        case O_HST: sHST[o.i/8]=seq; break;
        case O_POLL: for(int i=0;i<8;i++) if(sPoll[i]>=0) t.err=9000+n; sPoll[o.i]=seq; break;
        case O_KVST: sKVST=seq; break; case O_KVLD: if(!chkd[2]) t.err=8500+n; sKVLD=seq; break;
        case O_SST: sSST=seq; break; case O_PST: case O_POIS: sPST=seq; break;
        case O_FLAG: { int l=o.i<3?sHST[o.i]:o.i==3?sKVST:o.i==4?sSST:o.i==5?sOST:sPST; o.w=(short)(seq-l-1); if(o.i==2) chkd[8]=true; } break;
        default: break; }
      seq++; }
  }
  t.tail_after_pst=seq-sPoll[7]-1;
  for(int n=N;n<NMAX;n++) for(int r=0;r<4;r++) if(t.op[n][r].t!=O_NONE) t.err=10000+n;
  return t;
}
template<int ROLE> inline constexpr Tab TAB=make_tab<ROLE>();
template<int R,int E> struct ErrIs{ static_assert(E==0,"schedule error"); };
template struct ErrIs<0,TAB<0>.err>; template struct ErrIs<1,TAB<1>.err>;

DI void dot2a(float& acc,unsigned a,unsigned b){ asm volatile("v_dot2_f32_bf16 %0, %1, %2, %0":"+v"(acc):"v"(a),"v"(b)); }
DI void dot2i(float& acc,unsigned a,unsigned b,float c){ asm volatile("v_dot2_f32_bf16 %0, %1, %2, %3":"=v"(acc):"v"(a),"v"(b),"v"(c)); }
DI void dot2z(float& acc,unsigned a,unsigned b){ asm volatile("v_dot2_f32_bf16 %0, %1, %2, 0":"=v"(acc):"v"(a),"v"(b)); }
template<int CTRL> DI unsigned dppmov(unsigned old,unsigned src){ return (unsigned)__builtin_amdgcn_update_dpp((int)old,(int)src,CTRL,0xf,0xf,false); }
template<int DL,int BK> DI unsigned shiftc(unsigned cur,unsigned h){
  constexpr int sh=4*BK+(DL==1?2:0);
  unsigned t; if constexpr(sh==0) t=h; else t=(unsigned)__builtin_amdgcn_mov_dpp((int)h,0x100+sh,0xf,0xf,true);
  return dppmov<0x110+2*DL>(t,cur);
}

template<int ROLE>
DI void body(const u16* __restrict__ x,const u16* __restrict__ w0,const u16* __restrict__ w1,const u16* __restrict__ wd,const u16* __restrict__ wo,u16* __restrict__ out,char* __restrict__ ws,
             unsigned lds0,int mt,int xs,int j,u64* dbg){
  const int tid=threadIdx.x, lane=tid&63, w=__builtin_amdgcn_readfirstlane(tid>>6);
  const i32x4 r0=mk_rsrc(w0,ROLE==0?D*F*2:D*3*D*2), r1=mk_rsrc(w1,ROLE==0?D*F*2:D*D*2), rd=mk_rsrc(wd,D*F*2), rw=mk_rsrc(ws,WS_BYTES);
  const unsigned l16=lane*16;
  const int g=lane>>4, q=(lane&15)>>2, p=lane&3;
  const int gw=mt*4+w;
  const int cg=xs*3+j;
  constexpr int NSTEP=nstep<ROLE>();
  #define STS(i)
  STS(0);
  i32x4 rc;
  { unsigned long long pc; int r0_,r1_;
    asm volatile("s_getpc_b64 %0\n\ts_mov_b32 %1, k1_main@rel32@lo+4\n\ts_mov_b32 %2, k0_ln@rel32@lo+12":"=s"(pc),"=s"(r0_),"=s"(r1_));
    rc=mk_rsrc((const void*)(pc+(long long)r0_), r1_>r0_? (unsigned)(r1_-r0_):0u); }
  unsigned voA[5];
  unsigned soA[3];
  const int rot=ROLE==0?j:0;
  const int rotx=0;
  if constexpr(ROLE==0){
    voA[0]=(8*((lane>>5)&1)+((lane>>3)&3))*(F*2)+(((lane&7)^(((lane>>4)&3)<<1))*16);
    #pragma unroll
    for(int k=0;k<3;k++){ int kp=k+rotx; kp=kp>=3?kp-3:kp; soA[k]=(kp*192+16*(w&1)+4*(w>>1))*(F*2)+cg*3*128; }
  } else {
    #pragma unroll
    for(int i=0;i<5;i++){ int idx=64*i+lane,rho=idx/10,pc=idx%10; int k=8*((rho>>2)&3)+4*(rho>>4)+(rho&3); voA[i]=k*(3*D*2)+(pc>8?8:pc)*16; }
    soA[0]=xs*144;
  }
  auto wo_pat=[&](int i,int kind) AI -> unsigned { unsigned ln=lane; pin(ln);
    int idx=64*i+ln,rho=idx/10,pc=idx-rho*10; int gg=(rho>>2)&3,jj=4*(rho>>4)+(rho&3);
    int slot=kind? 16+(jj&1) : 8*(w&1)+jj; int od=gg&1; int feat=od?(slot<2?16+slot:slot-2):slot;
    int row=18*gg+feat+(kind?72*(jj>>1):0); return row*(D*2)+pc*16; };
  const int gW=xs*4+w, tW=9*(mt>>1)+4*(mt&1);
  const unsigned soW=tW*32;
  unsigned vo1[3],voC;
  auto set_voB=[&]() AI { unsigned ln=lane; pin(ln);
    #pragma unroll
    for(int t=0;t<3;t++){ int idx=64*t+ln; int row=idx/12,pc=idx-row*12; vo1[t]=row*(D*2)+((pc^(((row>>2)&1)<<1))*16); pin(vo1[t]); }
    if constexpr(ROLE==0){ voC=(ln>>3)*(D*2)+(((ln&7)^(((ln>>4)&3)<<1))*16); pin(voC); } };
  const int tB=(0x1e180a04u>>(8*j))&255, tC=(0x00141000u>>(8*j))&255;
  const unsigned soB=(xs*576)*(D*2)+tB*32;
  unsigned soC[3],soH[3];
  #pragma unroll
  for(int k=0;k<3;k++){ int jp=k+rot; jp=jp>=3?jp-3:jp; soC[k]=(xs*576+jp*192+8*w)*(D*2)+tC*32; soH[k]=WS_H+(gw*144+xs*18+jp*6)*4096; }
  const unsigned lA=lds0+w*(ROLE==0?1024:5120), lB=lds0+w*6144;
  const unsigned w8=8*w;
  auto do_glds=[&](auto stc,auto ic) AI {
    constexpr int s_=decltype(stc)::value,i=decltype(ic)::value; constexpr Stage st=stage<ROLE>(s_); constexpr int sl=s_%6;
    if constexpr(st.kind==K_A){
      constexpr int pr=s_/6,mat=a_mat(s_),kp=a_kp(s_);
      glds16<pr*128+i*32*(F*2),sl*SLOT+i*4096>(mat?r1:r0,voA[0],soA[kp],lA);
    } else if constexpr(st.kind==K_Q){
      constexpr auto qoff=[](int kk){ if(kk>53) kk=53; return q_ks(kk)*3+q_sec(kk); };
      constexpr int b=4*st.idx;
      constexpr unsigned KP=qoff(b)|(qoff(b+1)<<8)|(qoff(b+2)<<16)|(qoff(b+3)<<24);
      const unsigned u=(KP>>w8)&255u; const unsigned ks=(u*171u)>>9, sec=u-3*ks;
      glds16<0,sl*SLOT+i*1024>(r0,voA[i],soA[0]+(ks*96+sec)*1152,lA);
    } else if constexpr(st.kind==K_W){
      constexpr auto woff=[](int kk){ if(kk>17) kk=17; return kk<4? kk>>1 : kk<6? (kk-4)*4 : (kk-2)>>1; };
      constexpr int b=4*st.idx;
      constexpr unsigned KP=woff(b)|(woff(b+1)<<8)|(woff(b+2)<<16)|(woff(b+3)<<24);
      unsigned u=(KP>>w8)&255u;
      if constexpr(st.idx==0){ voA[i]=wo_pat(i,0); pin(voA[i]); }
      unsigned vo=voA[i];
      if constexpr(st.idx==1){ const unsigned msk=(w<2)?0u:~0u; u=(u+(mt&msk))&7u; unsigned pl=wo_pat(i,1); vo=(w<2)?pl:vo; } else u=(u+mt)&7u;
      glds16<0,sl*SLOT+i*1024>(r1,vo,soW+u*(72*D*2),lA);
    } else if constexpr(st.kind==K_B){
      constexpr auto rowb=[](int kk){ if(kk>17) kk=17; int l=kk/2; return (l%3)*6+(l/3)*2+(kk&1); };
      constexpr int b=4*st.idx;
      constexpr unsigned KP=rowb(b)|(rowb(b+1)<<8)|(rowb(b+2)<<16)|(rowb(b+3)<<24);
      unsigned rsel=((KP>>w8)&255u)+rot*6; rsel=rsel>=18?rsel-18:rsel;
      glds16<(i/3)*16*(D*2),sl*SLOT+i*1024>(rd,vo1[i%3],soB+rsel*(32*D*2),lB);
    } else {
      glds16<(st.idx*64+(i&1)*32)*(D*2),sl*SLOT+i*4096>(rd,voC,soC[i/2],lA);
    }
  };
  unsigned soX[3];
  #pragma unroll
  for(int k=0;k<3;k++){ int kp=k+rotx; kp=kp>=3?kp-3:kp; soX[k]=WS_XN+(gw*18+kp*6)*4096; }
  bf16x8 xr[18][4];
  auto ld_x=[&](auto ic) AI { constexpr int i=decltype(ic)::value,ks=i/4,r=i%4; ldfrag<(ks<16),false,r*1024>(xr[ks][r],rw,l16,soX[ks/6]+(ks%6)*4096); };
  auto ld_o=[&](auto ic) AI { constexpr int i=decltype(ic)::value,sl=i/4,r=i%4; constexpr int ks=sl>=12?sl-12:sl+4; ldfrag<true,true,r*1024>(xr[sl][r],rw,l16,WS_O+(gW*18+((ks+2*mt)&15))*4096); };
  auto ld_h=[&](auto ic) AI { constexpr int i=decltype(ic)::value%72,kk=i/4,r=i%4; constexpr int l=kk/2;
    ldfrag<(kk<16),true,r*1024>(xr[kk][r],rw,l16,soH[l%3]+((l/3)*2+(kk&1))*4096); };
  const unsigned hf_line=WS_HF+((mt*8+xs)*4+w)*128;
  const unsigned vlane0=(lane==0)?0u:0x80000000u;
  unsigned pollv=0;
  constexpr int G0=stage_n<ROLE>(0);
  sfor<G0>([&](auto i) AI { do_glds(IC(0),i); });
  sfor<XPRO>([&](auto i) AI { ld_x(i); });
  unsigned bw[6];
  if constexpr(ROLE==0){ const int row=4*g+q; unsigned b0=lds0+row*128+32*((row>>1)&3)+8*p;
    #pragma unroll
    for(int c=0;c<4;c++){ bw[c]=b0^(c*32); pin(bw[c]); } }
  else { bw[0]=lds0+(4*g+q)*160+36*p-4*(p&1); bw[1]=lds0+(4*g+q)*160+8*p; pin(bw[0]); pin(bw[1]); }
  auto set_bases_B=[&]() AI { unsigned ln=lane; pin(ln); const int g2=ln>>4,row=4*g2+((ln&15)>>2),p2=ln&3;
    bw[4]=lds0+row*192+8*p2+(g2&1)*32; bw[5]=bw[4]+(((g2&1)^1)-(g2&1))*32; pin(bw[4]); pin(bw[5]); };
  float c2v=LOG2E*LOG2E; pin(c2v);
  waitvm<XPRO>();
  __builtin_amdgcn_s_barrier();
  STS(1);
  f32x4 ga[2][5][4],ua[5][4],da[10][4];
  bf16x8 a[NBUF];
  auto faddr=[&](auto nc) AI -> unsigned { constexpr Frag f=TAB<ROLE>.f[decltype(nc)::value]; return bw[f.base]+f.off; };
  sfor<PDF>([&](auto ic) AI { constexpr int i=decltype(ic)::value; constexpr Frag f=TAB<ROLE>.f[i]; unsigned ad=faddr(ic); a[i]=cat(trr(ad),trr(ad+f.d1)); });
  float e0=0,e1=0,e2=0; u32x4 hv;
  SB();
  auto fin=[&](auto tc,auto icn,auto prc) AI {
    constexpr int t=decltype(tc)::value, i=decltype(icn)::value, pr=decltype(prc)::value; constexpr int rb=i/6, jj=i%6;
    if constexpr(jj<4){ constexpr int cc=2*t+(jj>>1), e=(jj&1)*2; unsigned v=pack2(ga[pr&1][cc][rb][e]*ua[cc][rb][e],ga[pr&1][cc][rb][e+1]*ua[cc][rb][e+1]); pin(v); hv[jj]=v; }
  };
  auto pk8=[&](const f32x4& u0,const f32x4& u1) AI -> u32x4 { u32x4 v; v[0]=pack2(u0[0],u0[1]); v[1]=pack2(u0[2],u0[3]); v[2]=pack2(u1[0],u1[1]); v[3]=pack2(u1[2],u1[3]); return v; };
  const unsigned pbase=WS_P+(xs*36*64+gw*2)*1024;
  auto pstore=[&](auto tc,auto hc) AI { constexpr int t=decltype(tc)::value,hh=decltype(hc)::value; st16_sc1<hh*1024>(pk8(da[t][2*hh],da[t][2*hh+1]),rw,l16,pbase+(t<6?tB+t:tC+t-6)*65536); };
  const unsigned pf_w=WS_PF+((mt*4+j)*4+w)*128+xs*4;
  u32x4 kq[9],vq[9],hk[5],OL[8];
  const bool odd=g&1;
  const unsigned kf_own=WS_KF+(xs*32+gw)*128, kf_prev=WS_KF+(xs*32+(gw>0?gw-1:0))*128;
  const unsigned kv_prev=gw>0?WS_KV+(xs*32+gw-1)*5120:0x80000000u;

  const int gc=mt*4+(xs>>1), rp=mt*8+xs;
  const unsigned pf_r=WS_PF+((mt*4+j)*4+(xs>>1))*128;
  const int tl0=tB+w, tl1=tB+(int)((0x03020504u>>w8)&255u);
  u32x4 xv[3];
  auto vrow=[&]() AI -> unsigned { unsigned ln=lane; pin(ln); return (4*(ln&14)+(ln&1)+2*((ln>>4)&1))*(D*2)+(ln>>5)*16; };
  const unsigned srow=(gc*64+4*(xs&1))*(D*2);
  auto pollvo=[&](int id) AI -> unsigned { unsigned ln=lane; pin(ln);
    return id==0? ((ln%6)/2*3+(ln%6)%2)*4 : id==1? ((ln%3)*3+2)*4 : id==2? 0u : id==3? (ln%3)*12 : id==4? ((ln%3)*3+1)*4 : (id==7&&ROLE==0)? ((ln&8)? (WS_SF+gc*128+(ln&7)*4) : (pf_r+(ln&7)*4)) : (ln&7)*4; };
  auto pollso=[&](int id) AI -> unsigned { return id==2? kf_prev : id==5? WS_OF+gW*128 : id==6? pf_r : id==7? (ROLE==0?0u:WS_SF+gc*128) : hf_line; };
  sfor<NSTEP>([&](auto nc) AI {
    constexpr int n=decltype(nc)::value;
    constexpr Frag f=TAB<ROLE>.f[n];
    if constexpr(ROLE==1 && n==180){
      asm volatile("s_nop 7"); SB();
      #pragma unroll
      for(int r=0;r<4;r++){
        #pragma unroll
        for(int pi=0;pi<9;pi++) kq[pi][r]=pack2(ga[0][pi/2][r][(pi&1)*2],ga[0][pi/2][r][(pi&1)*2+1]);
        unsigned t9=pack2(ga[0][4][r][2],ga[0][4][r][3]), t0=kq[0][r]; pin(t9); pin(t0); kq[0][r]=odd?t9:t0;
      }
      #pragma unroll
      for(int pi=0;pi<9;pi++) pin(kq[pi]);
      #pragma unroll
      for(int f2=0;f2<9;f2++)
        #pragma unroll
        for(int e2_=0;e2_<2;e2_++){ const int pos=2*f2+e2_;
          unsigned A=pack2(ua[pos/4][2][pos%4],ua[pos/4][3][pos%4]), B=pack2(ua[pos/4][0][pos%4],ua[pos/4][1][pos%4]);
          if(f2==0){ unsigned A9=pack2(ua[4][2][2+e2_],ua[4][3][2+e2_]), B9=pack2(ua[4][0][2+e2_],ua[4][1][2+e2_]); pin(A); pin(B); pin(A9); pin(B9); A=odd?A9:A; B=odd?B9:B; }
          vq[f2][2*e2_]=A; vq[f2][2*e2_+1]=B; }
      #pragma unroll
      for(int pi=0;pi<9;pi++) pin(vq[pi]);
      SB();
    }
    if constexpr(ROLE==1 && n==270){
      STS(2);
      poll_check<TAB<ROLE>.blkwait>(pollv,rw,pollvo(3),hf_line);
      asm volatile("s_nop 7"); pin(hk[0]); pin(hk[1]); pin(hk[2]); pin(hk[3]); pin(hk[4]); SB();
      unsigned qp[4][9];
      #pragma unroll
      for(int r=0;r<4;r++){
        #pragma unroll
        for(int pi=0;pi<9;pi++){ qp[r][pi]=pack2(ga[0][pi/2][r][(pi&1)*2],ga[0][pi/2][r][(pi&1)*2+1]); pin(qp[r][pi]); }
        unsigned t9=pack2(ga[0][4][r][2],ga[0][4][r][3]); pin(t9); qp[r][0]=odd?t9:qp[r][0];
      }
      const int am=(lane&15)>>1;
      const float b1=(gw==0&&am<1)?-1e30f:0.f, b2=(gw==0&&am<2)?-1e30f:0.f;
      float lg[4][8];
      sfor<9>([&](auto pc) AI { constexpr int pi=decltype(pc)::value;
        if constexpr(pi<8){ ld_h(IC(3*pi)); ld_h(IC(3*pi+1)); ld_h(IC(3*pi+2)); }
        unsigned ke[12];
        #pragma unroll
        for(int r=0;r<4;r++){ unsigned c=kq[pi][r], h=hk[pi/4][r]; ke[8+r]=c; ke[4+r]=shiftc<1,pi%4>(c,h); if(r>0) ke[r]=shiftc<2,pi%4>(c,h); }
        #pragma unroll
        for(int ww=0;ww<8;ww++)
          #pragma unroll
          for(int r=3;r>=0;r--){
            if(pi>0) dot2a(lg[r][ww],ke[8+r-ww],qp[r][pi]);
            else if(ww<=r) dot2z(lg[r][ww],ke[8+r-ww],qp[r][pi]);
            else dot2i(lg[r][ww],ke[8+r-ww],qp[r][pi],ww-r<=4?b1:b2);
          }
      });
      asm volatile("s_nop 2"); SB();
      const float csc=0.23570226039551584f/LOG2E;
      unsigned pp[4][5];
      #pragma unroll
      for(int r=0;r<4;r++){
        float mx=__builtin_fmaxf(__builtin_fmaxf(__builtin_fmaxf(lg[r][0],lg[r][1]),__builtin_fmaxf(lg[r][2],lg[r][3])),__builtin_fmaxf(__builtin_fmaxf(lg[r][4],lg[r][5]),__builtin_fmaxf(lg[r][6],lg[r][7])));
        const float mo=-mx*csc; float sum=0;
        #pragma unroll
        for(int ww=0;ww<8;ww++){ lg[r][ww]=__builtin_amdgcn_exp2f(fmaf(lg[r][ww],csc,mo)); sum+=lg[r][ww]; }
        const float ri=__builtin_amdgcn_rcpf(sum*LOG2E);
        #pragma unroll
        for(int ww=0;ww<8;ww++) lg[r][ww]*=ri;
        if(r&1){
          #pragma unroll
          for(int t=0;t<4;t++) pp[r][t]=pack2(lg[r][2*t+1],lg[r][2*t]);
        } else {
          pp[r][0]=pack2(lg[r][0],0.f);
          #pragma unroll
          for(int t=1;t<4;t++) pp[r][t]=pack2(lg[r][2*t],lg[r][2*t-1]);
          pp[r][4]=pack2(0.f,lg[r][7]);
        }
      }
      SB();
      unsigned op[4][9];
      float o2[2][4][2];
      sfor<10>([&](auto fc) AI { constexpr int f2=decltype(fc)::value; constexpr int si=9+f2;
        if constexpr(f2==5) ld4_sc1(pollv,rw,pollvo(4),hf_line);
        if constexpr(f2<9){
          unsigned pr[2][6];
          #pragma unroll
          for(int e2_=0;e2_<2;e2_++){
            unsigned A=vq[f2][2*e2_],B=vq[f2][2*e2_+1],hA=hk[si/4][2*e2_],hB=hk[si/4][2*e2_+1];
            pr[e2_][0]=A; pr[e2_][1]=B; pr[e2_][2]=shiftc<1,si%4>(A,hA); pr[e2_][3]=shiftc<1,si%4>(B,hB); pr[e2_][4]=shiftc<2,si%4>(A,hA); pr[e2_][5]=shiftc<2,si%4>(B,hB); }
          #pragma unroll
          for(int t=0;t<5;t++)
            #pragma unroll
            for(int r=3;r>=0;r--)
              #pragma unroll
              for(int e2_=0;e2_<2;e2_++){ const int s0=(r>=2)?0:1;
                if(t==0) dot2z(o2[f2&1][r][e2_],pr[e2_][s0],pp[r][0]);
                else if(t<4||!(r&1)) dot2a(o2[f2&1][r][e2_],pr[e2_][s0+t],pp[r][t]); }
        } else asm volatile("s_nop 2");
        SB();
        if constexpr(f2>0){
          #pragma unroll
          for(int r=0;r<4;r++){ op[r][f2-1]=pack2(o2[(f2-1)&1][r][0],o2[(f2-1)&1][r][1]); pin(op[r][f2-1]); }
        }
      });
      sfor<8>([&](auto ic) AI { constexpr int r=decltype(ic)::value/2,s2=decltype(ic)::value%2;
        u32x4 v={op[r][4*s2],op[r][4*s2+1],op[r][4*s2+2],op[r][4*s2+3]}; st16_sc1<r*1024>(v,rw,l16,WS_O+(gw*18+2*xs+s2)*4096); });
      { u32x4 v={op[0][8],op[1][8],op[2][8],op[3][8]}; st16_sc1<0>(v,rw,l16,WS_OL+(gw*8+xs)*1024); }
      STS(3);
      SB();
    }
    if constexpr(TAB<ROLE>.wait[n]>=0){ waitvm<TAB<ROLE>.wait[n]>(); SB(); }
    if constexpr(TAB<ROLE>.bar[n]>=0){ __builtin_amdgcn_s_barrier(); SB(); }
    if constexpr(ROLE==1 && n==TAB<ROLE>.olt){
      #pragma unroll
      for(int hh=0;hh<8;hh++) pin(OL[hh]);
      #pragma unroll
      for(int aa=0;aa<2;aa++)
        #pragma unroll
        for(int r=0;r<4;r++){ u32x4 v={OL[4*aa][r],OL[4*aa+1][r],OL[4*aa+2][r],OL[4*aa+3][r]}; pin(v); xr[16+aa][r]=__builtin_bit_cast(bf16x8,v); }
      SB();
    }
    if constexpr(TAB<ROLE>.chk[n]>=0){ poll_check<TAB<ROLE>.chkw[n]>(pollv,rw,pollvo(TAB<ROLE>.chk[n]),pollso(TAB<ROLE>.chk[n])); SB(); }
    if constexpr(n==TAB<ROLE>.voB){ set_voB(); SB(); }
    if constexpr(n==TAB<ROLE>.baseB[0]){ set_bases_B(); SB(); }
    constexpr int nn=n+PDF; constexpr Frag fn=TAB<ROLE>.f[nn];
    s16x4 lo;
    sfor<4>([&](auto rc) AI {
      constexpr int r=decltype(rc)::value;
      if constexpr(f.ph==1||f.ph==4) mma<f.first,(f.ks<16)>(ua[f.c][r],a[n%NBUF],xr[f.ks][r]);
      else if constexpr(f.ph==2||f.ph==7) mma<f.first,(f.ks<16)>(da[f.c][r],a[n%NBUF],xr[f.ks][r]);
      else mma<f.first,(f.ks<16)>(ga[f.ph==0?(n/144)&1:0][f.c][r],a[n%NBUF],xr[f.ks][r]);
      SB();
      if constexpr(nn<NSTEP){
        if constexpr(r==0){ if constexpr(fn.bd!=0){ bw[fn.base]+=fn.bd; pin(bw[fn.base]); } lo=trr(faddr(IC(nn))); SB(); }
        if constexpr(r==1){ a[nn%NBUF]=cat(lo,trr(faddr(IC(nn))+fn.d1)); SB(); }
      }
      constexpr Op o=TAB<ROLE>.op[n][r]; constexpr int oi=o.i;
      if constexpr(o.t==O_GL){ do_glds(IC(oi/8),IC(oi%8)); SB(); }
      if constexpr(o.t==O_XL){ ld_x(IC(oi)); SB(); }
      if constexpr(o.t==O_HL){ ld_h(IC(oi)); SB(); }
      if constexpr(o.t==O_OL){ if constexpr(oi<64) ld_o(IC(oi)); else ld16_sc1<(oi&3)*1024>(OL[oi&7],rw,l16,WS_OL+(gW*8+(oi&4))*1024); SB(); }
      if constexpr(o.t==O_HST){ st16_sc1<(oi%4)*1024>(hv,rw,l16,WS_H+(gw*144+(cg*3+oi/8)*2+(oi/4)%2)*4096); SB(); }
      if constexpr(o.t==O_PST){ pstore(IC(oi/2),IC(oi%2)); SB(); }
      if constexpr(o.t==O_PLD){ ld16_plain(xv[oi],mk_rsrc(x,S*D*2),vrow(),srow+(oi==0?tl0:oi==1?tl1:tC+w)*32); SB(); }
      if constexpr(o.t==O_CODE){ unsigned ln=lane; pin(ln); code_touch(rc,ln<16?ln*128:0x80000000u,w*2048+CPFA,lds0+6*SLOT); SB(); }
      if constexpr(o.t==O_POIS){ constexpr int t=oi/2,hh=oi%2; unsigned m1=~0u; pin(m1); u32x4 v={m1,m1,m1,m1}; st16_sc1<hh*1024>(v,rw,l16,pbase+(t<6?tB+t:tC+t-6)*65536); SB(); }
      if constexpr(o.t==O_SST){ constexpr int t=oi/2,hh=oi%2; unsigned so=WS_SB+((tW+t)*64+gW*2)*1024; if constexpr(t==0) so|=(unsigned)(mt&1)<<31;
          st16_sc1<hh*1024>(pk8(ga[0][t][2*hh],ga[0][t][2*hh+1]),rw,l16,so); SB(); }
      if constexpr(o.t==O_KVST){ const unsigned vk=((lane&15)>=12)?(g*4+(lane&3))*16u:0x80000000u;
          st16_sc1<(oi%16)*256>(oi<9?kq[oi%9]:vq[oi%9],rw,vk,WS_KV+(xs*32+gw)*5120+(oi/16)*4096); SB(); }
      if constexpr(o.t==O_KVLD){ const unsigned vk=q*256+(g*4+p)*16; ld16_sc1<0>(hk[oi],rw,vk,kv_prev+oi*1024); SB(); }
      if constexpr(o.t==O_TOUCH){
          unsigned ln=lane; pin(ln); const unsigned ldm=lds0+6*SLOT;
          if constexpr((oi<2||oi>=8) && ROLE==0) touch((oi&1)?r1:r0,ln<18?ln*(F*2):0x80000000u,gw*18*(F*2)+(cg*3+1+oi/8)*128,ldm);
          else if constexpr(oi<2) touch(r0,ln<36?(ln>>1)*(3*D*2)+(ln&1)*140:0x80000000u,gw*18*(3*D*2)+(oi?0:2)*1152+xs*144,ldm);
          else if constexpr(oi==2) touch(mk_rsrc(wo,D*D*2),ln<54?ln*128:0x80000000u,((j*8+mt)*4+w)*6912,ldm);
          else if constexpr(oi==3) touch(rd,ln<54?ln*128:0x80000000u,xs*576*(D*2)+((j*8+mt)*4+w)*6912,ldm);
          else touch(rw,ln*128,WS_H+(gw*144+(cg*3+(oi-4))*2)*4096,ldm);
          SB(); }
      if constexpr(o.t==O_POLL){ ld4_sc1(pollv,rw,pollvo(oi),pollso(oi)); SB(); }
      if constexpr(o.t==O_FLAG){ waitvm<o.w>();
          st4_sc1(TAG,rw,vlane0, oi<3? hf_line+(j*3+oi)*4 : oi==3? kf_own : oi==4? WS_SF+gW*128+mt*4 : oi==5? WS_OF+gw*128+xs*4 : pf_w); SB(); }
      if constexpr(ROLE==0){
        constexpr int p=n<200?0:n<300?1:2;
        constexpr int m=n-silu_start(p);
        if constexpr(r==2 && m>=0 && m<64){ constexpr int k=m; e0=__builtin_amdgcn_exp2f(-ga[p&1][k/16][(k/4)%4][k%4]); pin(e0); SB(); }
        if constexpr(r==0 && m>=1 && m<65){ e1=fmaf(e0,c2v,c2v); pin(e1); SB(); }
        if constexpr(r==1 && m>=2 && m<66){ constexpr int k=m-2; float gv=ga[p&1][k/16][(k/4)%4][k%4]*e2; pin(gv); ga[p&1][k/16][(k/4)%4][k%4]=gv; SB(); }
        if constexpr(r==3 && m>=1 && m<65){ e2=__builtin_amdgcn_rcpf(e1); pin(e2); SB(); }
        constexpr int i0=(n-fin_start(p,0))*4+r, i1=(n-fin_start(p,1))*4+r;
        if constexpr(i0>=1&&i0<24){ fin(IC(0),IC(i0-1),IC(p)); SB(); }
        if constexpr(i1>=1&&i1<24){ fin(IC(1),IC(i1-1),IC(p)); SB(); }
      }
    });
  });
  STS(4);
  const int tl[3]={tl0,tl1,tC+w};
  asm volatile("s_nop 7"); SB();
  constexpr int NLATE=ROLE==0?2:12;
  if constexpr(ROLE==0) sfor<2>([&](auto i) AI { pstore(IC(9),i); });
  else sfor<12>([&](auto i) AI { pstore(IC(decltype(i)::value/2),IC(decltype(i)::value%2)); });
  constexpr int NT=ROLE==0?3:2;
  poll_check<TAB<ROLE>.tail_after_pst+NLATE>(pollv,rw,pollvo(7),pollso(7));
  #pragma unroll
  for(int i=0;i<NT;i++) pin(xv[i]);
  STS(5);
  auto rsw=__builtin_amdgcn_make_buffer_rsrc((void*)ws,0,WS_BYTES,0x00020000);
  auto rso=__builtin_amdgcn_make_buffer_rsrc((void*)out,0,S*D*2,0x00020000);
  const unsigned vr=vrow();
  const unsigned skip1=(w<2)?0u:0x80000000u;
  auto ld_tile=[&](int t,u32x4* pv,unsigned sk) AI {
    #pragma unroll
    for(int k=0;k<8;k++) pv[k]=__builtin_amdgcn_raw_buffer_load_b128(rsw,l16,(WS_P+((k*36+t)*64+rp)*1024)|sk,16); };
  auto fin_tile=[&](int t,u32x4* pv,u32x4 sv,u32x4 xx,unsigned sk) AI {
    float acc[8];
    #pragma unroll
    for(int e=0;e<4;e++){ acc[2*e]=bflo(pv[0][e]); acc[2*e+1]=bfhi(pv[0][e]); }
    #pragma unroll
    for(int k=1;k<8;k++)
      #pragma unroll
      for(int e=0;e<4;e++){ acc[2*e]+=bflo(pv[k][e]); acc[2*e+1]+=bfhi(pv[k][e]); }
    #pragma unroll
    for(int e=0;e<8;e++){ const unsigned s2=sv[e>>1]; const float d0=acc[e];
      acc[e]=((e&1)?bfhi(s2):bflo(s2))*d0*__builtin_amdgcn_rcpf(1.0f+__builtin_amdgcn_exp2f(-LOG2E*d0)); }
    #pragma unroll
    for(int e=0;e<4;e++){ auto r=__builtin_amdgcn_permlane16_swap(__float_as_uint(acc[e]),__float_as_uint(acc[4+e]),false,false); acc[e]=__uint_as_float(r[0]); acc[4+e]=__uint_as_float(r[1]); }
    u32x4 o;
    #pragma unroll
    for(int e=0;e<4;e++) o[e]=pack2(acc[2*e]+bflo(xx[e]),acc[2*e+1]+bfhi(xx[e]));
    __builtin_amdgcn_raw_buffer_store_b128(o,rso,vr,(srow+t*32)|sk,0);
  };
  auto fix_tile=[&](int t,u32x4* pv) AI {
    for(int it=0;it<8192;it++){ bool any=false;
      #pragma unroll
      for(int k=0;k<8;k++){ unsigned m=max(max(pv[k][0],pv[k][1]),max(pv[k][2],pv[k][3]));
        if(__any(m==~0u)){ any=true; pv[k]=__builtin_amdgcn_raw_buffer_load_b128(rsw,l16,WS_P+((k*36+t)*64+rp)*1024,16); } }
      if(!any) break;
      asm volatile("":::"memory"); } };
  u32x4 pv0[8],pv1[8],pv2[8],sv[3];
  #pragma unroll
  for(int i=0;i<NT;i++) sv[i]=__builtin_amdgcn_raw_buffer_load_b128(rsw,l16,(WS_SB+(tl[i]*64+rp)*1024)|(i==1?skip1:0u),16);
  ld_tile(tl[0],pv0,0u); ld_tile(tl[1],pv1,skip1);
  if constexpr(ROLE==0) ld_tile(tl[2],pv2,0u);
  SB();
  if constexpr(ROLE==1) fix_tile(tl[0],pv0);
  fin_tile(tl[0],pv0,sv[0],xv[0],0u);
  if constexpr(ROLE==1) fix_tile(tl[1],pv1);
  fin_tile(tl[1],pv1,sv[1],xv[1],skip1);
  STS(6);
  if constexpr(ROLE==0){ fix_tile(tl[2],pv2); fin_tile(tl[2],pv2,sv[2],xv[2],0u); }
}

extern "C" __global__ __launch_bounds__(256,1) void k1_main(const u16* __restrict__ x,const u16* __restrict__ wqkv,const u16* __restrict__ wo,const u16* __restrict__ wg,const u16* __restrict__ wu,const u16* __restrict__ wd,u16* __restrict__ out,char* __restrict__ ws
  ){
  u64* dbg=nullptr;
  extern __shared__ __attribute__((aligned(16))) char smem[];
  const unsigned lds0=(unsigned)(size_t)(lds_vp)smem;
  const int xs=blockIdx.x&7, idx=blockIdx.x>>3, mt=idx&7, j=idx>>3;
  if(j<3) body<0>(x,wg,wu,wd,wo,out,ws,lds0,mt,xs,j,dbg);
  else body<1>(x,wqkv,wo,wd,wo,out,ws,lds0,mt,xs,j,dbg);
}

template<int CTRL> DI float dppadd(float v){ return v+__int_as_float(__builtin_amdgcn_update_dpp(0,__float_as_int(v),CTRL,0xf,0xf,true)); }
DI float red32(float v){
  v=dppadd<0xB1>(v); v=dppadd<0x4E>(v); v=dppadd<0x141>(v); v=dppadd<0x140>(v);
  auto r=__builtin_amdgcn_permlane16_swap(__float_as_uint(v),__float_as_uint(v),false,false);
  return __uint_as_float(r[0])+__uint_as_float(r[1]);
}
extern "C" __global__ __launch_bounds__(512) void k0_ln(const u16* __restrict__ x,char* __restrict__ ws,const u16* __restrict__ wqkv,const u16* __restrict__ wg,const u16* __restrict__ wu
  ){
  const int tid=threadIdx.x, lane=tid&63;
  if(tid>=256){
    const int t=tid-256;
    if(t>=192){ if(t<224) *(u32x4*)(ws+blockIdx.x*512+(t-192)*16)=u32x4{0,0,0,0}; return; }
    unsigned long long pc; int rel;
    asm volatile("s_getpc_b64 %0\n\ts_mov_b32 %1, k1_main@rel32@lo+4":"=s"(pc),"=s"(rel));
    pc+=(long long)rel;
    const int xs=blockIdx.x&7, idx=blockIdx.x>>3, mt=idx&7, j=idx>>3;
    const int csz=rel<-16? -rel-16 : 0;
    const u32x4* cp=(const u32x4*)(pc+min(idx*4096+t*16,csz)); const u32x4* cp2=(const u32x4*)(pc+min(idx*4096+3072+(t&63)*16,csz));
    const char *p0,*p1,*p2;
    const int nl=j<3?K0LINES:2; const int tt=t<72*nl?t:0; const int row=mt*72+(nl==2?tt>>1:tt), ln=nl==2?tt&1:0;
    if(j<3){ const size_t o=(size_t)row*(F*2)+((xs*3+j)*3+ln)*128; p0=(const char*)wg+o; p1=(const char*)wu+o; }
    else { p0=(const char*)wqkv+(size_t)row*(3*D*2)+((1+ln)%3)*1152+xs*144; p1=p0+140; }
    p2=p0;
    unsigned t0_,t1,t2,t3,t4;
    asm volatile("global_load_dword %0, %5, off\n\tglobal_load_dword %1, %6, off" NTK "\n\tglobal_load_dword %2, %7, off" NTK "\n\tglobal_load_dword %3, %8, off\n\tglobal_load_dword %4, %9, off" NTK "\n\ts_waitcnt vmcnt(0)":"=&v"(t0_),"=&v"(t1),"=&v"(t2),"=&v"(t3),"=&v"(t4):"v"(cp),"v"(p0),"v"(p1),"v"(cp2),"v"(p2):"memory");
    return; }
  const int l32=lane&31, gw=blockIdx.x>>3, rb=(blockIdx.x>>1)&3, m=(blockIdx.x&1)*8+(tid>>5);
  const int row=gw*64+4*(m&~1)+2*rb+(m&1);
  const bool has3=l32<8;
  const u16* xp=x+row*D+l32*8;
  u32x4 v[3];
  v[0]=*(const u32x4*)xp; v[1]=*(const u32x4*)(xp+256); v[2]=*(const u32x4*)(xp+(has3?512:0));
  const float piv=bflo(*(const unsigned*)(x+row*D));
  float f[24]; float s=0,q=0;
  #pragma unroll
  for(int i=0;i<3;i++){ float s1=0,q1=0;
    #pragma unroll
    for(int j=0;j<4;j++){ float a=bflo(v[i][j])-piv,b=bfhi(v[i][j])-piv; f[i*8+2*j]=a; f[i*8+2*j+1]=b; s1+=a+b; q1=fmaf(a,a,fmaf(b,b,q1)); }
    if(i<2||has3){ s+=s1; q+=q1; } }
  s=red32(s); q=red32(q);
  const float mean=s*(1.0f/D), var=fmaxf(q*(1.0f/D)-mean*mean,0.f);
  const float sc=LOG2E*rsqrtf(var+1e-5f), of=-mean*sc;
  char* dst=ws+WS_XN+(size_t)gw*18*4096+rb*1024+(l32>>2)*4096+((l32&3)*16+m)*16;
  #pragma unroll
  for(int i=0;i<3;i++){
    u32x4 o;
    #pragma unroll
    for(int j=0;j<4;j++) o[j]=pack2(fmaf(f[i*8+2*j],sc,of),fmaf(f[i*8+2*j+1],sc,of));
    if(i<2||has3) *(u32x4*)(dst+i*32768)=o;
  }
}

extern "C" void kernel_launch(void* const* d_in,const int* in_sizes,int n_in,void* d_out,int out_size,void* d_ws,size_t ws_size,hipStream_t stream){
  const u16* x=(const u16*)d_in[0]; const u16* wqkv=(const u16*)d_in[1]; const u16* wo=(const u16*)d_in[2];
  const u16* wg=(const u16*)d_in[4]; const u16* wu=(const u16*)d_in[5]; const u16* wd=(const u16*)d_in[6];
  char* ws=(char*)d_ws;
  (void)hipFuncSetAttribute((const void*)k1_main,hipFuncAttributeMaxDynamicSharedMemorySize,6*SLOT+1024);
  k0_ln<<<256,512,0,stream>>>(x,ws,wqkv,wg,wu
  );
  k1_main<<<256,256,6*SLOT+1024,stream>>>(x,wqkv,wo,wg,wu,wd,(u16*)d_out,ws
  );
}
```

```cpp
#define NOCT
#include <hip/hip_runtime.h>
#include <utility>
typedef unsigned short u16; typedef unsigned long long u64;
typedef __attribute__((ext_vector_type(4))) unsigned u32x4;
typedef __attribute__((ext_vector_type(2))) unsigned u32x2;
typedef __attribute__((ext_vector_type(4))) int i32x4;
typedef __attribute__((ext_vector_type(4))) short s16x4;
typedef __attribute__((ext_vector_type(8))) __bf16 bf16x8;
typedef __attribute__((ext_vector_type(2))) __bf16 bf16x2;
typedef __attribute__((ext_vector_type(4))) float f32x4;
typedef __attribute__((address_space(3))) s16x4* lds_s16x4_p;
typedef __attribute__((address_space(3))) void* lds_vp;
#define DI __device__ __forceinline__
#define SB() __builtin_amdgcn_sched_barrier(0)
#define AI __attribute__((always_inline))
#define IC(x) std::integral_constant<int,(x)>{}
template<class Fn,int... I> DI void sfor_impl(Fn&& f, std::integer_sequence<int,I...>){ (f(std::integral_constant<int,I>{}),...); }
template<int N,class Fn> DI void sfor(Fn&& f){ sfor_impl(f,std::make_integer_sequence<int,N>{}); }

constexpr int S=2048,D=576,F=4608;
constexpr float LOG2E=1.4426950408889634f;
constexpr unsigned WS_EP=0;
constexpr unsigned WS_HF=4096;
constexpr unsigned WS_PF=WS_HF+32768;
constexpr unsigned WS_SF=WS_PF+16384;
constexpr unsigned WS_OF=WS_SF+4096;
constexpr unsigned WS_KF=WS_OF+4096;
constexpr unsigned WS_XF=WS_KF+32768;
constexpr unsigned WS_XN=1u<<20;
constexpr unsigned WS_O =4u<<20;
constexpr unsigned WS_OL=7u<<20;
constexpr unsigned WS_KV=11u<<20;
constexpr unsigned WS_SB=8u<<20;
constexpr unsigned WS_H =16u<<20;
constexpr unsigned WS_P =40u<<20;
constexpr unsigned WS_BYTES=64u<<20;
constexpr int SLOT=24576;

#define NTW ""
#define NTT ""
#define NTK ""
#define K0LINES 1

DI s16x4 trr(unsigned a){ return __builtin_amdgcn_ds_read_tr16_b64_v4i16((lds_s16x4_p)(size_t)a); }
DI bf16x8 cat(s16x4 a,s16x4 b){ return __builtin_bit_cast(bf16x8,__builtin_shufflevector(a,b,0,1,2,3,4,5,6,7)); }
DI unsigned pack2(float a,float b){ bf16x2 v={(__bf16)a,(__bf16)b}; return __builtin_bit_cast(unsigned,v); }
DI float bflo(unsigned v){ return __uint_as_float(v<<16); }
DI float bfhi(unsigned v){ return __uint_as_float(v&0xffff0000u); }
DI i32x4 mk_rsrc(const void* p, unsigned bytes){
  unsigned long long a=(unsigned long long)p; i32x4 r; r[0]=(int)a; r[1]=(int)(a>>32); r[2]=(int)bytes; r[3]=0x00020000; return r;
}
template<unsigned SO,unsigned LO> DI void glds16(i32x4 rsrc, unsigned voff, unsigned sbase, unsigned lbase){
  unsigned t;
  asm volatile("s_add_u32 m0, %4, %6\n\ts_add_u32 %0, %3, %5\n\tbuffer_load_dwordx4 %1, %2, %0 offen" NTW " lds":"=&s"(t):"v"(voff),"s"(rsrc),"s"(sbase),"s"(lbase),"i"(SO),"i"(LO):"memory","scc");
}
DI void touch(i32x4 rsrc, unsigned voff, unsigned soff, unsigned ldummy){
  asm volatile("s_mov_b32 m0, %3\n\ts_nop 0\n\tbuffer_load_dword %0, %1, %2 offen" NTT " lds"::"v"(voff),"s"(rsrc),"s"(soff),"s"(ldummy):"memory");
}
#define CPFA 8192
DI void code_touch(i32x4 rc, unsigned voff, unsigned ahead, unsigned ldummy){
  unsigned long long pc; asm volatile("s_getpc_b64 %0":"=s"(pc));
  touch(rc,voff,(unsigned)pc-(unsigned)rc[0]+ahead,ldummy);
}
template<bool AG,bool SC1,int OFF> DI void ldfrag(bf16x8& d,i32x4 rsrc,unsigned voff,unsigned soff){
  if constexpr(AG){ if constexpr(SC1) asm volatile("buffer_load_dwordx4 %0, %1, %2, %3 offen offset:%4 sc1":"=a"(d):"v"(voff),"s"(rsrc),"s"(soff),"i"(OFF):"memory");
                    else asm volatile("buffer_load_dwordx4 %0, %1, %2, %3 offen offset:%4":"=a"(d):"v"(voff),"s"(rsrc),"s"(soff),"i"(OFF):"memory"); }
  else { if constexpr(SC1) asm volatile("buffer_load_dwordx4 %0, %1, %2, %3 offen offset:%4 sc1":"=v"(d):"v"(voff),"s"(rsrc),"s"(soff),"i"(OFF):"memory");
         else asm volatile("buffer_load_dwordx4 %0, %1, %2, %3 offen offset:%4":"=v"(d):"v"(voff),"s"(rsrc),"s"(soff),"i"(OFF):"memory"); }
}
template<int OFF> DI void st16_sc1(u32x4 v,i32x4 rsrc,unsigned voff,unsigned soff){
  asm volatile("buffer_store_dwordx4 %0, %1, %2, %3 offen offset:%4 sc1\n\ts_nop 1"::"v"(v),"v"(voff),"s"(rsrc),"s"(soff),"i"(OFF):"memory");
}
DI void st8_sc1(u32x2 v,i32x4 rsrc,unsigned voff,unsigned soff){
  asm volatile("buffer_store_dwordx2 %0, %1, %2, %3 offen sc1"::"v"(v),"v"(voff),"s"(rsrc),"s"(soff):"memory");
}
DI void ld8_sc1(u64& d,i32x4 rsrc,unsigned voff,unsigned soff){
  asm volatile("buffer_load_dwordx2 %0, %1, %2, %3 offen sc1":"=v"(d):"v"(voff),"s"(rsrc),"s"(soff):"memory");
}
template<int OFF> DI void ld16_sc1(u32x4& d,i32x4 rsrc,unsigned voff,unsigned soff){
  asm volatile("buffer_load_dwordx4 %0, %1, %2, %3 offen offset:%4 sc1":"=v"(d):"v"(voff),"s"(rsrc),"s"(soff),"i"(OFF):"memory");
}
DI void ld4_plain(unsigned& d,i32x4 rsrc,unsigned voff,unsigned soff){
  asm volatile("buffer_load_dword %0, %1, %2, %3 offen":"=v"(d):"v"(voff),"s"(rsrc),"s"(soff):"memory");
}
DI void ld16_plain(u32x4& d,i32x4 rsrc,unsigned voff,unsigned soff){
  asm volatile("buffer_load_dwordx4 %0, %1, %2, %3 offen":"=v"(d):"v"(voff),"s"(rsrc),"s"(soff):"memory");
}
template<class T> DI void pin(T& v){ asm volatile("":"+v"(v)); }
template<int N> DI void waitvm(){ asm volatile("s_waitcnt vmcnt(%0)"::"i"(N>63?63:N):"memory"); }
template<int N> DI void poll_check(u64& v,u64 tag,i32x4 rsrc,unsigned voff,unsigned soff){
  unsigned cnt;
  asm volatile("s_waitcnt vmcnt(%5)\n\t"
               "s_mov_b32 %1, 0x4000\n"
               "1:\n\t"
               "v_cmp_ne_u64 vcc, %6, %0\n\t"
               "s_nop 0\n\t"
               "s_cbranch_vccz 2f\n\t"
               "s_sleep 1\n\t"
               "buffer_load_dwordx2 %0, %2, %3, %4 offen sc1\n\t"
               "s_sub_u32 %1, %1, 1\n\t"
               "s_waitcnt vmcnt(0)\n\t"
               "s_cbranch_scc0 1b\n"
               "2:"
               :"+v"(v),"=&s"(cnt):"v"(voff),"s"(rsrc),"s"(soff),"i"(N>63?63:N),"s"(tag):"memory","vcc","scc");
}
template<int CTRL> DI float dppadd(float v){ return v+__int_as_float(__builtin_amdgcn_update_dpp(0,__float_as_int(v),CTRL,0xf,0xf,true)); }
DI float red32(float v){
  v=dppadd<0xB1>(v); v=dppadd<0x4E>(v); v=dppadd<0x141>(v); v=dppadd<0x140>(v);
  auto r=__builtin_amdgcn_permlane16_swap(__float_as_uint(v),__float_as_uint(v),false,false);
  return __uint_as_float(r[0])+__uint_as_float(r[1]);
}
template<bool FIRST,bool AG> DI void mma(f32x4& acc,const bf16x8& a,const bf16x8& b){
  if constexpr(FIRST){ if constexpr(AG) asm volatile("v_mfma_f32_16x16x32_bf16 %0, %1, %2, 0":"=&v"(acc):"v"(a),"a"(b)); else asm volatile("v_mfma_f32_16x16x32_bf16 %0, %1, %2, 0":"=&v"(acc):"v"(a),"v"(b)); }
  else { if constexpr(AG) asm volatile("v_mfma_f32_16x16x32_bf16 %0, %1, %2, %0":"+v"(acc):"v"(a),"a"(b)); else asm volatile("v_mfma_f32_16x16x32_bf16 %0, %1, %2, %0":"+v"(acc):"v"(a),"v"(b)); }
}

constexpr int PDF=3, NBUF=4;
enum { K_A=0,K_Q=1,K_W=2,K_B=3,K_C=4 };
struct Stage{ int kind; int idx; int nsteps; };
constexpr int NST0=26, NST1=24;
template<int ROLE> constexpr int nstage(){ return ROLE==0?NST0:NST1; }
template<int ROLE> constexpr Stage stage(int s){
  if(ROLE==0){
    if(s<18) return Stage{K_A,s,24};
    constexpr int G[8][3]={{K_B,0,24},{K_B,1,24},{K_B,2,24},{K_B,3,24},{K_B,4,12},{K_C,0,24},{K_C,1,24},{K_C,2,24}};
    return Stage{G[s-18][0],G[s-18][1],G[s-18][2]};
  } else {
    if(s<14) return Stage{K_Q,s,s==13?10:20};
    if(s<17) return Stage{K_B,s-14,24};
    if(s<22) return Stage{K_W,s-17,s==21?10:20};
    return Stage{K_B,s-19,s==23?12:24};
  }
}
constexpr int a_mat(int s){ return s<6? (s&1) : (s%6)/3; }
constexpr int a_kp(int s){ return s<6? (s>>1) : s%3; }
constexpr int q_sec(int kk){ return kk<36? 1+(kk&1) : 0; }
constexpr int q_ks(int kk){ return kk<36? kk>>1 : kk-36; }
template<int ROLE> constexpr int stage_start(int s){ int n=0; for(int i=0;i<s;i++) n+=stage<ROLE>(i).nsteps; return n; }
template<int ROLE> constexpr int stage_n(int s){ int k=stage<ROLE>(s).kind; return (k==K_Q||k==K_W)?5:6; }
template<int ROLE> constexpr int nstep(){ return stage_start<ROLE>(nstage<ROLE>()); }
struct Frag{ int ph; int ks; int c; bool first; int win; int base; int off; int d1; int bd; };
enum { O_NONE=0,O_GL,O_XL,O_HL,O_OL,O_HST,O_FLAG,O_POLL,O_TOUCH,O_PST,O_SST,O_KVST,O_KVLD,O_POIS,O_PLD,O_CODE };
struct Op{ short t; short i; short w; };
constexpr int NMAX=640;
struct Tab{ Frag f[NMAX]; Op op[NMAX][4]; short bar[NMAX]; short wait[NMAX]; short chk[NMAX]; short chkw[NMAX]; int tail_after_pst; int blkwait; int voB; int baseB[3]; int wopat; int olt; int err; };
constexpr int XPRO=12;
constexpr int silu_start(int p){ return p==0?121:144*p+73; }
constexpr int fin_start(int p,int t){ return p==0? (t==0?156:188) : 144*p+138+6*t; }
#define HFD0 30
#define HFD1 40
#define HFD2 20
#define CPFS 90
#define P2P 24
#define P2C 10
#define PROWS 24
#define PSTS 2
#define TA0 50
#define TA1 104
#define G6AT 96
#define TA2 126
#define TA3 164
#define PLD0 22
#define PFD0 18
#define HLEAD 28
#define PFA 100
#define PFW 198
#define PFD 204
template<int ROLE> constexpr Tab make_tab(){
  Tab t{};
  constexpr int NS=nstage<ROLE>(), N=nstep<ROLE>();
  int ss[32]={}; { int n=0; for(int s=0;s<=NS;s++){ ss[s]=n; if(s<NS) n+=stage<ROLE>(s).nsteps; } }
  int firstB[3]={-1,-1,-1};
  for(int s=0;s<NS;s++){ const Stage st=stage<ROLE>(s); const int sl=s%6;
    for(int i=0;i<st.nsteps;i++){ Frag f{}; const int n=ss[s]+i;
      if(st.kind==K_A){ int kl=i/4,c=i%4;
        if(s==6){ if(i<18){ kl=i/3; c=i%3; } else { kl=i-18; c=3; } }
        else if(s==12&&i<12){ kl=(i%6)/2; c=(i/6)*2+(i&1); }
        else if((s==11||s==17)&&i>=12){ kl=3+((i-12)%6)/2; c=((i-12)/6)*2+(i&1); }
        f.ph=a_mat(s); f.ks=a_kp(s)*6+kl; f.c=c; f.first=(f.ks==0); f.win=sl/2; f.base=c; f.off=(sl%2)*SLOT+kl*4096; f.d1=2048;
      } else if(st.kind==K_Q){ int kk=4*st.idx+i/5; f.ph=q_sec(kk)==1?3:q_sec(kk)==2?4:5; f.ks=q_ks(kk); f.c=i%5; f.first=(f.ks==0); f.win=sl/2; f.base=0; f.off=(sl%2)*SLOT+(i/5)*5120+f.c*8; f.d1=2560;
      } else if(st.kind==K_W){ int kk=4*st.idx+i/5; f.ph=6; f.ks=(kk+12)%18; f.c=i%5; f.first=(kk==0); f.win=sl/2; f.base=1; f.off=(sl%2)*SLOT+(i/5)*5120+f.c*32; f.d1=2560;
      } else if(st.kind==K_B){ f.ph=2; f.ks=4*st.idx+i/6; f.c=i%6; f.base=f.c&1; f.off=(sl%2)*SLOT+(i/6)*6144+(f.c&~1)*32; f.d1=3072; f.first=(f.ks==0); f.win=sl/2;
        if(firstB[f.win]<0) firstB[f.win]=n;
      } else { const int kl=st.idx==2?i%6:i/4, c=st.idx==2?i/6:i%4; f.ph=7; f.ks=6*st.idx+kl; f.c=6+c; f.base=c; f.off=(sl%2)*SLOT+kl*4096; f.d1=2048; f.first=(f.ks==0); f.win=sl/2; }
      t.f[n]=f; } }
  { int cur[6]={0,0,0,0,0,0}; for(int n=0;n<N;n++){ Frag& f=t.f[n]; if(f.ph==2) f.base+=4; f.bd=(f.win-cur[f.base])*49152; cur[f.base]=f.win; } }
  for(int n=N;n<NMAX;n++) t.f[n]=t.f[N-1];
  for(int n=0;n<NMAX;n++){ t.bar[n]=-1; t.wait[n]=-1; t.chk[n]=-1; t.chkw[n]=0; }
  { int fb=N; for(int w=0;w<3;w++) if(firstB[w]>=0&&firstB[w]<fb) fb=firstB[w]; t.baseB[0]=fb-PDF-2; }
  auto put=[&](int ty,int idx,int n,int maxper)->int{
    constexpr int pref[4]={2,3,1,0};
    for(;;n++){ int used=0; for(int r=0;r<4;r++) used+=t.op[n][r].t!=O_NONE; if(used>=maxper) continue;
      for(int k=0;k<4;k++){ int r=pref[k]; if(t.op[n][r].t==O_NONE){ t.op[n][r]=Op{(short)ty,(short)idx,0}; return n; } } } };
  auto findB=[&](int ks,int c,int from)->int{ for(int n=from;n<N;n++) if(t.f[n].ph==2&&t.f[n].ks==ks&&t.f[n].c==c) return n; return -1; };
  if(ROLE==0) for(int p=0;p<3;p++) for(int tt=0;tt<2;tt++) for(int rb=0;rb<4;rb++){ int i=rb*6+5; int n=fin_start(p,tt)+i/4; t.op[n][i%4]=Op{O_HST,(short)(p*8+tt*4+rb),0}; }
  { int n=0; auto X=[&](int a,int b){ for(int i=a*4;i<b*4;i++) n=put(O_XL,i,n,1); }; auto G=[&](int s){ if(s>=6&&n<ss[s-4]-PDF) n=ss[s-4]-PDF; for(int i=0;i<stage_n<ROLE>(s);i++) n=put(O_GL,s*8+i,n,1); };
    if(ROLE==0){ X(3,8); G(2); X(8,12); G(3); G(4); X(12,15); G(5); X(15,18); G(6); G(7); G(8);
      put(O_TOUCH,0,TA0,2); put(O_TOUCH,1,TA1,2);
      put(O_TOUCH,8,TA2,2); put(O_TOUCH,9,TA3,2);
      }
    else { X(3,4); G(2); X(4,8); G(3); X(8,10); G(4); X(10,12); G(5); X(12,14); G(6); X(14,16); G(7); X(16,18); G(8); } }
  for(int s=9;s<NS;s++){ int b0=ss[s-4]-PDF, len=stage<ROLE>(s-4).nsteps, cnt=stage_n<ROLE>(s); for(int i=0;i<cnt;i++){ int n=put(O_GL,s*8+i,b0+i*len/cnt,1); if(stage<ROLE>(s).kind==K_B&&t.voB==0) t.voB=n; } }
  if(ROLE==1) put(O_TOUCH,1,PFA,2);
  const int b1done=findB(17,5,0)+1;
  const int u2=findB(12,0,0);
  if(ROLE==0){
    constexpr int HFD[3]={HFD0,HFD1,HFD2};
    int nf2=0;
    for(int p=0;p<3;p++){ int n=put(O_FLAG,p,fin_start(p,1)+6+HFD[p],2); if(p<2) put(O_TOUCH,4+p,n+1,2); nf2=n; }
    put(O_TOUCH,2,PFW,2); put(O_TOUCH,3,PFD,2);
    for(int i=0;i<8;i++) put(O_POIS,12+i,300+4*i,1);
    put(O_POLL,0,376,2); t.chk[392]=0;
    for(int i=0;i<48;i++){ int e=432+6*(i/4)-HLEAD+(i%4); put(O_HL,i,i<16?394+2*i:e,1); }
    for(int i=0;i<8;i++){ int e=u2-HLEAD+i; put(O_HL,48+i,e>nf2+1?e:nf2+1,1); }
    put(O_POLL,1,u2-P2P,2); t.chk[u2-P2C]=1;
    for(int i=8;i<24;i++) put(O_HL,48+i,u2-P2C+1+(i-8),1);
    for(int i=0;i<12;i++) put(O_PST,i,b1done+2+PSTS*i,1);
    put(O_FLAG,6,b1done+2+12*PSTS+PFD0,2);
    for(int i=0;i<6;i++) put(O_PST,12+i,N-18+6*(i/2)+2+(i&1),1);
    for(int i=0;i<3;i++) put(O_PLD,i,b1done+2+12*PSTS+3*i,1);
    put(O_POLL,7,N-8,3);
  } else {
    for(int i=0;i<18;i++) put(O_KVST,i,182+i,1);
    put(O_FLAG,3,224,2); put(O_POLL,2,236,2); t.chk[250]=2;
    for(int i=0;i<5;i++) put(O_KVLD,i,251+i,2);
    put(O_POLL,3,258,2);
    t.chk[271]=4;
    for(int i=24;i<48;i++) put(O_HL,i,272+(i-24),1);
    put(O_FLAG,5,288,2);
    put(O_POLL,5,308,2); t.chk[322]=5;
    const int w0=ss[17];
    for(int i=0;i<16;i++) put(O_OL,48+i,323+i,1);
    for(int i=0;i<8;i++) put(O_OL,64+i,339+i,1);
    for(int i=0;i<48;i++) put(O_OL,i,348+i,1);
    t.olt=w0+19;
    for(int i=0;i<10;i++) put(O_SST,i,u2+3+i,1);
    put(O_FLAG,4,N-6,2);
    for(int i=0;i<12;i++) put(O_POIS,i,120+3*i,1);
    put(O_FLAG,6,232,2);
    put(O_POLL,1,u2-44,2); t.chk[u2-30]=1;
    for(int i=0;i<24;i++) put(O_HL,48+i,u2-29+3*i/2,1);
    put(O_POLL,6,u2+2,2); t.chk[u2+20]=6;
    for(int i=0;i<2;i++) put(O_PLD,i,u2+21+3*i,1);
    put(O_POLL,7,N-2,2);
  }
  int sGL[32]={},cGL[32]={},sS[18]={},gen[18][4]={},sHST[3]={},sPoll[9]={},sKVST=0,sKVLD=0,sSST=0,sPST=0,sOST=0,sOLL=-1,cOLL=0;
  bool chkd[9]={};
  for(int i=0;i<9;i++) sPoll[i]=-1;
  int seq=1; sGL[0]=sGL[1]=0; cGL[0]=cGL[1]=stage_n<ROLE>(0);
  for(int i=0;i<XPRO;i++){ sS[i/4]=seq++; gen[i/4][i%4]=1; }
  int need_done=0, nb=1;
  for(int n=0;n<N;n++){
    if(ROLE==1&&n==270){ if(sPoll[3]<sKVLD) t.err=11000; t.blkwait=seq-sPoll[3]-1; sPoll[3]=-1; chkd[3]=true;
      for(int i=0;i<24;i++){ sS[i/4]=seq++; gen[i/4][i%4]=2; }
      sPoll[4]=seq++; seq+=9; sOST=seq-1; }
    int need=-1; const Frag f=t.f[n];
    if(nb<NS && n==ss[nb]-PDF-1){ t.bar[n]=nb; need=sGL[nb]; if(cGL[nb]!=stage_n<ROLE>(nb)) t.err=1000+n; nb++; }
    if(ROLE==1&&n==t.olt){ if(cOLL!=8) t.err=2000+n; if(sOLL>need) need=sOLL; for(int r=0;r<4;r++){ gen[16][r]=3; gen[17][r]=3; } sS[16]=sS[17]=-1; }
    { const int eg=(f.ph==2||f.ph==7)?2:f.ph==6?3:1; for(int r=0;r<4;r++) if(gen[f.ks][r]!=eg) t.err=3000+n; if(sS[f.ks]>need) need=sS[f.ks]; }
    if(need>need_done){ t.wait[n]=(short)(seq-need-1); need_done=need; }
    if(t.chk[n]>=0){ if(sPoll[t.chk[n]]<0) t.err=4000+n; t.chkw[n]=(short)(seq-sPoll[t.chk[n]]-1); chkd[t.chk[n]]=true; sPoll[t.chk[n]]=-1; }
    for(int r=0;r<4;r++){ Op& o=t.op[n][r]; if(o.t==O_NONE) continue;
      switch(o.t){
        case O_GL: sGL[o.i/8]=seq; cGL[o.i/8]++; if(o.i/8>=6 && nb<=o.i/8-4) t.err=5000+n; break;
        case O_XL: sS[o.i/4]=seq; gen[o.i/4][o.i%4]=1; break;
        case O_HL: { int k=(o.i%72)/4; if(k==f.ks) t.err=6000+n; sS[k]=seq; gen[k][o.i%4]=2;
                     int c=k>=12?((ROLE==0&&k<14)?8:1):(ROLE==0?0:(k<6?3:4)); if(!chkd[c]) t.err=7000+n; } break;
        case O_OL: if(!chkd[5]) t.err=8000+n; if(o.i<64){ int k=o.i/4; if(k==f.ks) t.err=6000+n; sS[k]=seq; gen[k][o.i%4]=3; } else { sOLL=seq; cOLL++; } break;
        case O_HST: sHST[o.i/8]=seq; break;
        case O_POLL: for(int i=0;i<8;i++) if(sPoll[i]>=0) t.err=9000+n; sPoll[o.i]=seq; break;
        case O_KVST: sKVST=seq; break; case O_KVLD: if(!chkd[2]) t.err=8500+n; sKVLD=seq; break;
        case O_SST: sSST=seq; break; case O_PST: case O_POIS: sPST=seq; break;
        case O_FLAG: { int l=o.i<3?sHST[o.i]:o.i==3?sKVST:o.i==4?sSST:o.i==5?sOST:sPST; o.w=(short)(seq-l-1); if(o.i==2) chkd[8]=true; } break;
        default: break; }
      seq++; }
  }
  t.tail_after_pst=seq-sPoll[7]-1;
  for(int n=N;n<NMAX;n++) for(int r=0;r<4;r++) if(t.op[n][r].t!=O_NONE) t.err=10000+n;
  return t;
}
template<int ROLE> inline constexpr Tab TAB=make_tab<ROLE>();
template<int R,int E> struct ErrIs{ static_assert(E==0,"schedule error"); };
template struct ErrIs<0,TAB<0>.err>; template struct ErrIs<1,TAB<1>.err>;

DI void dot2a(float& acc,unsigned a,unsigned b){ asm volatile("v_dot2_f32_bf16 %0, %1, %2, %0":"+v"(acc):"v"(a),"v"(b)); }
DI void dot2i(float& acc,unsigned a,unsigned b,float c){ asm volatile("v_dot2_f32_bf16 %0, %1, %2, %3":"=v"(acc):"v"(a),"v"(b),"v"(c)); }
DI void dot2z(float& acc,unsigned a,unsigned b){ asm volatile("v_dot2_f32_bf16 %0, %1, %2, 0":"=v"(acc):"v"(a),"v"(b)); }
template<int CTRL> DI unsigned dppmov(unsigned old,unsigned src){ return (unsigned)__builtin_amdgcn_update_dpp((int)old,(int)src,CTRL,0xf,0xf,false); }
template<int DL,int BK> DI unsigned shiftc(unsigned cur,unsigned h){
  constexpr int sh=4*BK+(DL==1?2:0);
  unsigned t; if constexpr(sh==0) t=h; else t=(unsigned)__builtin_amdgcn_mov_dpp((int)h,0x100+sh,0xf,0xf,true);
  return dppmov<0x110+2*DL>(t,cur);
}

template<int ROLE>
DI void body(const u16* __restrict__ x,const u16* __restrict__ w0,const u16* __restrict__ w1,const u16* __restrict__ wd,const u16* __restrict__ wo,u16* __restrict__ out,char* __restrict__ ws,
             unsigned lds0,int mt,int xs,int j,unsigned e0,i32x4 rc,u64 t_start,u64* dbg){
  const u64 tag=((u64)(e0*0x9E3779B1u+0x7F4A7C15u)<<32)|(u64)(e0+1u);
  const int tid=threadIdx.x, lane=tid&63, w=__builtin_amdgcn_readfirstlane(tid>>6);
  const i32x4 r0=mk_rsrc(w0,ROLE==0?D*F*2:D*3*D*2), r1=mk_rsrc(w1,ROLE==0?D*F*2:D*D*2), rd=mk_rsrc(wd,D*F*2), rw=mk_rsrc(ws,WS_BYTES);
  const unsigned l16=lane*16;
  const int g=lane>>4, q=(lane&15)>>2, p=lane&3;
  const int gw=mt*4+w;
  const int cg=xs*3+j;
  constexpr int NSTEP=nstep<ROLE>();
  #define STS(i)
  unsigned voA[5];
  unsigned soA[3];
  const int rot=ROLE==0?j:0;
  const int rotx=0;
  if constexpr(ROLE==0){
    voA[0]=(8*((lane>>5)&1)+((lane>>3)&3))*(F*2)+(((lane&7)^(((lane>>4)&3)<<1))*16);
    #pragma unroll
    for(int k=0;k<3;k++){ int kp=k+rotx; kp=kp>=3?kp-3:kp; soA[k]=(kp*192+16*(w&1)+4*(w>>1))*(F*2)+cg*3*128; }
  } else {
    #pragma unroll
    for(int i=0;i<5;i++){ int idx=64*i+lane,rho=idx/10,pc=idx%10; int k=8*((rho>>2)&3)+4*(rho>>4)+(rho&3); voA[i]=k*(3*D*2)+(pc>8?8:pc)*16; }
    soA[0]=xs*144;
  }
  auto wo_pat=[&](int i,int kind) AI -> unsigned { unsigned ln=lane; pin(ln);
    int idx=64*i+ln,rho=idx/10,pc=idx-rho*10; int gg=(rho>>2)&3,jj=4*(rho>>4)+(rho&3);
    int slot=kind? 16+(jj&1) : 8*(w&1)+jj; int od=gg&1; int feat=od?(slot<2?16+slot:slot-2):slot;
    int row=18*gg+feat+(kind?72*(jj>>1):0); return row*(D*2)+pc*16; };
  const int gW=xs*4+w, tW=9*(mt>>1)+4*(mt&1);
  const unsigned soW=tW*32;
  unsigned vo1[3],voC;
  auto set_voB=[&]() AI { unsigned ln=lane; pin(ln);
    #pragma unroll
    for(int t=0;t<3;t++){ int idx=64*t+ln; int row=idx/12,pc=idx-row*12; vo1[t]=row*(D*2)+((pc^(((row>>2)&1)<<1))*16); pin(vo1[t]); }
    if constexpr(ROLE==0){ voC=(ln>>3)*(D*2)+(((ln&7)^(((ln>>4)&3)<<1))*16); pin(voC); } };
  const int tB=(0x1e180a04u>>(8*j))&255, tC=(0x00141000u>>(8*j))&255;
  const unsigned soB=(xs*576)*(D*2)+tB*32;
  unsigned soC[3],soH[3];
  #pragma unroll
  for(int k=0;k<3;k++){ int jp=k+rot; jp=jp>=3?jp-3:jp; soC[k]=(xs*576+jp*192+8*w)*(D*2)+tC*32; soH[k]=WS_H+(gw*144+xs*18+jp*6)*4096; }
  const unsigned lA=lds0+w*(ROLE==0?1024:5120), lB=lds0+w*6144;
  const unsigned w8=8*w;
  auto do_glds=[&](auto stc,auto ic) AI {
    constexpr int s_=decltype(stc)::value,i=decltype(ic)::value; constexpr Stage st=stage<ROLE>(s_); constexpr int sl=s_%6;
    if constexpr(st.kind==K_A){
      constexpr int pr=s_/6,mat=a_mat(s_),kp=a_kp(s_);
      glds16<pr*128+i*32*(F*2),sl*SLOT+i*4096>(mat?r1:r0,voA[0],soA[kp],lA);
    } else if constexpr(st.kind==K_Q){
      constexpr auto qoff=[](int kk){ if(kk>53) kk=53; return q_ks(kk)*3+q_sec(kk); };
      constexpr int b=4*st.idx;
      constexpr unsigned KP=qoff(b)|(qoff(b+1)<<8)|(qoff(b+2)<<16)|(qoff(b+3)<<24);
      const unsigned u=(KP>>w8)&255u; const unsigned ks=(u*171u)>>9, sec=u-3*ks;
      glds16<0,sl*SLOT+i*1024>(r0,voA[i],soA[0]+(ks*96+sec)*1152,lA);
    } else if constexpr(st.kind==K_W){
      constexpr auto woff=[](int kk){ if(kk>17) kk=17; return kk<4? kk>>1 : kk<6? (kk-4)*4 : (kk-2)>>1; };
      constexpr int b=4*st.idx;
      constexpr unsigned KP=woff(b)|(woff(b+1)<<8)|(woff(b+2)<<16)|(woff(b+3)<<24);
      unsigned u=(KP>>w8)&255u;
      if constexpr(st.idx==0){ voA[i]=wo_pat(i,0); pin(voA[i]); }
      unsigned vo=voA[i];
      if constexpr(st.idx==1){ const unsigned msk=(w<2)?0u:~0u; u=(u+(mt&msk))&7u; unsigned pl=wo_pat(i,1); vo=(w<2)?pl:vo; } else u=(u+mt)&7u;
      glds16<0,sl*SLOT+i*1024>(r1,vo,soW+u*(72*D*2),lA);
    } else if constexpr(st.kind==K_B){
      constexpr auto rowb=[](int kk){ if(kk>17) kk=17; int l=kk/2; return (l%3)*6+(l/3)*2+(kk&1); };
      constexpr int b=4*st.idx;
      constexpr unsigned KP=rowb(b)|(rowb(b+1)<<8)|(rowb(b+2)<<16)|(rowb(b+3)<<24);
      unsigned rsel=((KP>>w8)&255u)+rot*6; rsel=rsel>=18?rsel-18:rsel;
      glds16<(i/3)*16*(D*2),sl*SLOT+i*1024>(rd,vo1[i%3],soB+rsel*(32*D*2),lB);
    } else {
      glds16<(st.idx*64+(i&1)*32)*(D*2),sl*SLOT+i*4096>(rd,voC,soC[i/2],lA);
    }
  };
  unsigned soX[3];
  #pragma unroll
  for(int k=0;k<3;k++){ int kp=k+rotx; kp=kp>=3?kp-3:kp; soX[k]=WS_XN+(gw*18+kp*6)*4096; }
  bf16x8 xr[18][4];
  auto ld_x=[&](auto ic) AI { constexpr int i=decltype(ic)::value,ks=i/4,r=i%4; ldfrag<(ks<16),true,r*1024>(xr[ks][r],rw,l16,soX[ks/6]+(ks%6)*4096); };
  auto ld_o=[&](auto ic) AI { constexpr int i=decltype(ic)::value,sl=i/4,r=i%4; constexpr int ks=sl>=12?sl-12:sl+4; ldfrag<true,true,r*1024>(xr[sl][r],rw,l16,WS_O+(gW*18+((ks+2*mt)&15))*4096); };
  auto ld_h=[&](auto ic) AI { constexpr int i=decltype(ic)::value%72,kk=i/4,r=i%4; constexpr int l=kk/2;
    ldfrag<(kk<16),true,r*1024>(xr[kk][r],rw,l16,soH[l%3]+((l/3)*2+(kk&1))*4096); };
  const unsigned hf_line=WS_HF+((mt*8+xs)*4+w)*128;
  auto put_flag=[&](unsigned so) AI { unsigned ln=lane; pin(ln); u32x2 tv={(unsigned)tag,(unsigned)(tag>>32)}; pin(tv); st8_sc1(tv,rw,ln==0?0u:0x40000000u,so); };
  u64 pollv=0;
  constexpr int G0=stage_n<ROLE>(0);
  sfor<G0>([&](auto i) AI { do_glds(IC(0),i); });
  waitvm<G0>();
  put_flag(w<3? WS_XF+(mt*4+j)*256+(xs*3+w)*8 : 0x80000000u);
  sfor<G0>([&](auto i) AI { do_glds(IC(1),i); });
  { const unsigned vp=(lane%24)*8;
    ld8_sc1(pollv,rw,vp,WS_XF+gw*256); poll_check<0>(pollv,tag,rw,vp,WS_XF+gw*256); }
  sfor<XPRO>([&](auto i) AI { ld_x(i); });
  unsigned bw[6];
  if constexpr(ROLE==0){ const int row=4*g+q; unsigned b0=lds0+row*128+32*((row>>1)&3)+8*p;
    #pragma unroll
    for(int c=0;c<4;c++){ bw[c]=b0^(c*32); pin(bw[c]); } }
  else { bw[0]=lds0+(4*g+q)*160+36*p-4*(p&1); bw[1]=lds0+(4*g+q)*160+8*p; pin(bw[0]); pin(bw[1]); }
  auto set_bases_B=[&]() AI { unsigned ln=lane; pin(ln); const int g2=ln>>4,row=4*g2+((ln&15)>>2),p2=ln&3;
    bw[4]=lds0+row*192+8*p2+(g2&1)*32; bw[5]=bw[4]+(((g2&1)^1)-(g2&1))*32; pin(bw[4]); pin(bw[5]); };
  float c2v=LOG2E*LOG2E; pin(c2v);
  __builtin_amdgcn_s_barrier();
  STS(1);
  f32x4 ga[2][5][4],ua[5][4],da[10][4];
  bf16x8 a[NBUF];
  auto faddr=[&](auto nc) AI -> unsigned { constexpr Frag f=TAB<ROLE>.f[decltype(nc)::value]; return bw[f.base]+f.off; };
  sfor<PDF>([&](auto ic) AI { constexpr int i=decltype(ic)::value; constexpr Frag f=TAB<ROLE>.f[i]; unsigned ad=faddr(ic); a[i]=cat(trr(ad),trr(ad+f.d1)); });
  float x0=0,e1=0,e2=0; u32x4 hv;
  SB();
  auto fin=[&](auto tc,auto icn,auto prc) AI {
    constexpr int t=decltype(tc)::value, i=decltype(icn)::value, pr=decltype(prc)::value; constexpr int rb=i/6, jj=i%6;
    if constexpr(jj<4){ constexpr int cc=2*t+(jj>>1), e=(jj&1)*2; unsigned v=pack2(ga[pr&1][cc][rb][e]*ua[cc][rb][e],ga[pr&1][cc][rb][e+1]*ua[cc][rb][e+1]); pin(v); hv[jj]=v; }
  };
  auto pk8=[&](const f32x4& u0,const f32x4& u1) AI -> u32x4 { u32x4 v; v[0]=pack2(u0[0],u0[1]); v[1]=pack2(u0[2],u0[3]); v[2]=pack2(u1[0],u1[1]); v[3]=pack2(u1[2],u1[3]); return v; };
  const unsigned pbase=WS_P+(xs*36*64+gw*2)*1024;
  auto pstore=[&](auto tc,auto hc) AI { constexpr int t=decltype(tc)::value,hh=decltype(hc)::value; st16_sc1<hh*1024>(pk8(da[t][2*hh],da[t][2*hh+1]),rw,l16,pbase+(t<6?tB+t:tC+t-6)*65536); };
  const unsigned pf_w=WS_PF+((mt*4+j)*4+w)*128+xs*8;
  u32x4 kq[9],vq[9],hk[5],OL[8];
  const bool odd=g&1;
  const unsigned kf_own=WS_KF+(xs*32+gw)*128, kf_prev=WS_KF+(xs*32+(gw>0?gw-1:0))*128;
  const unsigned kv_prev=gw>0?WS_KV+(xs*32+gw-1)*5120:0x80000000u;

  const int gc=mt*4+(xs>>1), rp=mt*8+xs;
  const unsigned pf_r=WS_PF+((mt*4+j)*4+(xs>>1))*128;
  const int tl0=tB+w, tl1=tB+(int)((0x03020504u>>w8)&255u);
  u32x4 xv[3];
  auto vrow=[&]() AI -> unsigned { unsigned ln=lane; pin(ln); return (4*(ln&14)+(ln&1)+2*((ln>>4)&1))*(D*2)+(ln>>5)*16; };
  const unsigned srow=(gc*64+4*(xs&1))*(D*2);
  auto pollvo=[&](int id) AI -> unsigned { unsigned ln=lane; pin(ln);
    return id==0? ((ln%6)/2*3+(ln%6)%2)*8 : id==1? ((ln%3)*3+2)*8 : id==2? 0u : id==3? (ln%3)*24 : id==4? ((ln%3)*3+1)*8 : (id==7&&ROLE==0)? ((ln&8)? (WS_SF+gc*128+(ln&7)*8) : (pf_r+(ln&7)*8)) : (ln&7)*8; };
  auto pollso=[&](int id) AI -> unsigned { return id==2? kf_prev : id==5? WS_OF+gW*128 : id==6? pf_r : id==7? (ROLE==0?0u:WS_SF+gc*128) : hf_line; };
  sfor<NSTEP>([&](auto nc) AI {
    constexpr int n=decltype(nc)::value;
    constexpr Frag f=TAB<ROLE>.f[n];
    if constexpr(ROLE==1 && n==180){
      asm volatile("s_nop 7"); SB();
      #pragma unroll
      for(int r=0;r<4;r++){
        #pragma unroll
        for(int pi=0;pi<9;pi++) kq[pi][r]=pack2(ga[0][pi/2][r][(pi&1)*2],ga[0][pi/2][r][(pi&1)*2+1]);
        unsigned t9=pack2(ga[0][4][r][2],ga[0][4][r][3]), t0=kq[0][r]; pin(t9); pin(t0); kq[0][r]=odd?t9:t0;
      }
      #pragma unroll
      for(int pi=0;pi<9;pi++) pin(kq[pi]);
      #pragma unroll
      for(int f2=0;f2<9;f2++)
        #pragma unroll
        for(int e2_=0;e2_<2;e2_++){ const int pos=2*f2+e2_;
          unsigned A=pack2(ua[pos/4][2][pos%4],ua[pos/4][3][pos%4]), B=pack2(ua[pos/4][0][pos%4],ua[pos/4][1][pos%4]);
          if(f2==0){ unsigned A9=pack2(ua[4][2][2+e2_],ua[4][3][2+e2_]), B9=pack2(ua[4][0][2+e2_],ua[4][1][2+e2_]); pin(A); pin(B); pin(A9); pin(B9); A=odd?A9:A; B=odd?B9:B; }
          vq[f2][2*e2_]=A; vq[f2][2*e2_+1]=B; }
      #pragma unroll
      for(int pi=0;pi<9;pi++) pin(vq[pi]);
      SB();
    }
    if constexpr(ROLE==1 && n==270){
      STS(2);
      poll_check<TAB<ROLE>.blkwait>(pollv,tag,rw,pollvo(3),hf_line);
      asm volatile("s_nop 7"); pin(hk[0]); pin(hk[1]); pin(hk[2]); pin(hk[3]); pin(hk[4]); SB();
      unsigned qp[4][9];
      #pragma unroll
      for(int r=0;r<4;r++){
        #pragma unroll
        for(int pi=0;pi<9;pi++){ qp[r][pi]=pack2(ga[0][pi/2][r][(pi&1)*2],ga[0][pi/2][r][(pi&1)*2+1]); pin(qp[r][pi]); }
        unsigned t9=pack2(ga[0][4][r][2],ga[0][4][r][3]); pin(t9); qp[r][0]=odd?t9:qp[r][0];
      }
      const int am=(lane&15)>>1;
      const float b1=(gw==0&&am<1)?-1e30f:0.f, b2=(gw==0&&am<2)?-1e30f:0.f;
      float lg[4][8];
      sfor<9>([&](auto pc) AI { constexpr int pi=decltype(pc)::value;
        if constexpr(pi<8){ ld_h(IC(3*pi)); ld_h(IC(3*pi+1)); ld_h(IC(3*pi+2)); }
        unsigned ke[12];
        #pragma unroll
        for(int r=0;r<4;r++){ unsigned c=kq[pi][r], h=hk[pi/4][r]; ke[8+r]=c; ke[4+r]=shiftc<1,pi%4>(c,h); if(r>0) ke[r]=shiftc<2,pi%4>(c,h); }
        #pragma unroll
        for(int ww=0;ww<8;ww++)
          #pragma unroll
          for(int r=3;r>=0;r--){
            if(pi>0) dot2a(lg[r][ww],ke[8+r-ww],qp[r][pi]);
            else if(ww<=r) dot2z(lg[r][ww],ke[8+r-ww],qp[r][pi]);
            else dot2i(lg[r][ww],ke[8+r-ww],qp[r][pi],ww-r<=4?b1:b2);
          }
      });
      asm volatile("s_nop 2"); SB();
      const float csc=0.23570226039551584f/LOG2E;
      unsigned pp[4][5];
      #pragma unroll
      for(int r=0;r<4;r++){
        float mx=__builtin_fmaxf(__builtin_fmaxf(__builtin_fmaxf(lg[r][0],lg[r][1]),__builtin_fmaxf(lg[r][2],lg[r][3])),__builtin_fmaxf(__builtin_fmaxf(lg[r][4],lg[r][5]),__builtin_fmaxf(lg[r][6],lg[r][7])));
        const float mo=-mx*csc; float sum=0;
        #pragma unroll
        for(int ww=0;ww<8;ww++){ lg[r][ww]=__builtin_amdgcn_exp2f(fmaf(lg[r][ww],csc,mo)); sum+=lg[r][ww]; }
        const float ri=__builtin_amdgcn_rcpf(sum*LOG2E);
        #pragma unroll
        for(int ww=0;ww<8;ww++) lg[r][ww]*=ri;
        if(r&1){
          #pragma unroll
          for(int t=0;t<4;t++) pp[r][t]=pack2(lg[r][2*t+1],lg[r][2*t]);
        } else {
          pp[r][0]=pack2(lg[r][0],0.f);
          #pragma unroll
          for(int t=1;t<4;t++) pp[r][t]=pack2(lg[r][2*t],lg[r][2*t-1]);
          pp[r][4]=pack2(0.f,lg[r][7]);
        }
      }
      SB();
      unsigned op[4][9];
      float o2[2][4][2];
      sfor<10>([&](auto fc) AI { constexpr int f2=decltype(fc)::value; constexpr int si=9+f2;
        if constexpr(f2==5) ld8_sc1(pollv,rw,pollvo(4),hf_line);
        if constexpr(f2<9){
          unsigned pr[2][6];
          #pragma unroll
          for(int e2_=0;e2_<2;e2_++){
            unsigned A=vq[f2][2*e2_],B=vq[f2][2*e2_+1],hA=hk[si/4][2*e2_],hB=hk[si/4][2*e2_+1];
            pr[e2_][0]=A; pr[e2_][1]=B; pr[e2_][2]=shiftc<1,si%4>(A,hA); pr[e2_][3]=shiftc<1,si%4>(B,hB); pr[e2_][4]=shiftc<2,si%4>(A,hA); pr[e2_][5]=shiftc<2,si%4>(B,hB); }
          #pragma unroll
          for(int t=0;t<5;t++)
            #pragma unroll
            for(int r=3;r>=0;r--)
              #pragma unroll
              for(int e2_=0;e2_<2;e2_++){ const int s0=(r>=2)?0:1;
                if(t==0) dot2z(o2[f2&1][r][e2_],pr[e2_][s0],pp[r][0]);
                else if(t<4||!(r&1)) dot2a(o2[f2&1][r][e2_],pr[e2_][s0+t],pp[r][t]); }
        } else asm volatile("s_nop 2");
        SB();
        if constexpr(f2>0){
          #pragma unroll
          for(int r=0;r<4;r++){ op[r][f2-1]=pack2(o2[(f2-1)&1][r][0],o2[(f2-1)&1][r][1]); pin(op[r][f2-1]); }
        }
      });
      sfor<8>([&](auto ic) AI { constexpr int r=decltype(ic)::value/2,s2=decltype(ic)::value%2;
        u32x4 v={op[r][4*s2],op[r][4*s2+1],op[r][4*s2+2],op[r][4*s2+3]}; st16_sc1<r*1024>(v,rw,l16,WS_O+(gw*18+2*xs+s2)*4096); });
      { u32x4 v={op[0][8],op[1][8],op[2][8],op[3][8]}; st16_sc1<0>(v,rw,l16,WS_OL+(gw*8+xs)*1024); }
      STS(3);
      SB();
    }
    if constexpr(TAB<ROLE>.wait[n]>=0){ waitvm<TAB<ROLE>.wait[n]>(); SB(); }
    if constexpr(TAB<ROLE>.bar[n]>=0){ __builtin_amdgcn_s_barrier(); SB(); }
    if constexpr(ROLE==1 && n==TAB<ROLE>.olt){
      #pragma unroll
      for(int hh=0;hh<8;hh++) pin(OL[hh]);
      #pragma unroll
      for(int aa=0;aa<2;aa++)
        #pragma unroll
        for(int r=0;r<4;r++){ u32x4 v={OL[4*aa][r],OL[4*aa+1][r],OL[4*aa+2][r],OL[4*aa+3][r]}; pin(v); xr[16+aa][r]=__builtin_bit_cast(bf16x8,v); }
      SB();
    }
    if constexpr(TAB<ROLE>.chk[n]>=0){ poll_check<TAB<ROLE>.chkw[n]>(pollv,tag,rw,pollvo(TAB<ROLE>.chk[n]),pollso(TAB<ROLE>.chk[n])); SB(); }
    if constexpr(n==TAB<ROLE>.voB){ set_voB(); SB(); }
    if constexpr(n==TAB<ROLE>.baseB[0]){ set_bases_B(); SB(); }
    constexpr int nn=n+PDF; constexpr Frag fn=TAB<ROLE>.f[nn];
    s16x4 lo;
    sfor<4>([&](auto rc) AI {
      constexpr int r=decltype(rc)::value;
      if constexpr(f.ph==1||f.ph==4) mma<f.first,(f.ks<16)>(ua[f.c][r],a[n%NBUF],xr[f.ks][r]);
      else if constexpr(f.ph==2||f.ph==7) mma<f.first,(f.ks<16)>(da[f.c][r],a[n%NBUF],xr[f.ks][r]);
      else mma<f.first,(f.ks<16)>(ga[f.ph==0?(n/144)&1:0][f.c][r],a[n%NBUF],xr[f.ks][r]);
      SB();
      if constexpr(nn<NSTEP){
        if constexpr(r==0){ if constexpr(fn.bd!=0){ bw[fn.base]+=fn.bd; pin(bw[fn.base]); } lo=trr(faddr(IC(nn))); SB(); }
        if constexpr(r==1){ a[nn%NBUF]=cat(lo,trr(faddr(IC(nn))+fn.d1)); SB(); }
      }
      constexpr Op o=TAB<ROLE>.op[n][r]; constexpr int oi=o.i;
      if constexpr(o.t==O_GL){ do_glds(IC(oi/8),IC(oi%8)); SB(); }
      if constexpr(o.t==O_XL){ ld_x(IC(oi)); SB(); }
      if constexpr(o.t==O_HL){ ld_h(IC(oi)); SB(); }
      if constexpr(o.t==O_OL){ if constexpr(oi<64) ld_o(IC(oi)); else ld16_sc1<(oi&3)*1024>(OL[oi&7],rw,l16,WS_OL+(gW*8+(oi&4))*1024); SB(); }
      if constexpr(o.t==O_HST){ st16_sc1<(oi%4)*1024>(hv,rw,l16,WS_H+(gw*144+(cg*3+oi/8)*2+(oi/4)%2)*4096); SB(); }
      if constexpr(o.t==O_PST){ pstore(IC(oi/2),IC(oi%2)); SB(); }
      if constexpr(o.t==O_PLD){ ld16_plain(xv[oi],mk_rsrc(x,S*D*2),vrow(),srow+(oi==0?tl0:oi==1?tl1:tC+w)*32); SB(); }
      if constexpr(o.t==O_CODE){ unsigned ln=lane; pin(ln); code_touch(rc,ln<16?ln*128:0x80000000u,w*2048+CPFA,lds0+6*SLOT); SB(); }
      if constexpr(o.t==O_POIS){ constexpr int t=oi/2,hh=oi%2; unsigned m1=~0u; pin(m1); u32x4 v={m1,m1,m1,m1}; st16_sc1<hh*1024>(v,rw,l16,pbase+(t<6?tB+t:tC+t-6)*65536); SB(); }
      if constexpr(o.t==O_SST){ constexpr int t=oi/2,hh=oi%2; unsigned so=WS_SB+((tW+t)*64+gW*2)*1024; if constexpr(t==0) so|=(unsigned)(mt&1)<<31;
          st16_sc1<hh*1024>(pk8(ga[0][t][2*hh],ga[0][t][2*hh+1]),rw,l16,so); SB(); }
      if constexpr(o.t==O_KVST){ const unsigned vk=((lane&15)>=12)?(g*4+(lane&3))*16u:0x80000000u;
          st16_sc1<(oi%16)*256>(oi<9?kq[oi%9]:vq[oi%9],rw,vk,WS_KV+(xs*32+gw)*5120+(oi/16)*4096); SB(); }
      if constexpr(o.t==O_KVLD){ const unsigned vk=q*256+(g*4+p)*16; ld16_sc1<0>(hk[oi],rw,vk,kv_prev+oi*1024); SB(); }
      if constexpr(o.t==O_TOUCH){
          unsigned ln=lane; pin(ln); const unsigned ldm=lds0+6*SLOT;
          if constexpr((oi<2||oi>=8) && ROLE==0) touch((oi&1)?r1:r0,ln<18?ln*(F*2):0x80000000u,gw*18*(F*2)+(cg*3+1+oi/8)*128,ldm);
          else if constexpr(oi<2) touch(r0,ln<36?(ln>>1)*(3*D*2)+(ln&1)*140:0x80000000u,gw*18*(3*D*2)+(oi?0:2)*1152+xs*144,ldm);
          else if constexpr(oi==2) touch(mk_rsrc(wo,D*D*2),ln<54?ln*128:0x80000000u,((j*8+mt)*4+w)*6912,ldm);
          else if constexpr(oi==3) touch(rd,ln<54?ln*128:0x80000000u,xs*576*(D*2)+((j*8+mt)*4+w)*6912,ldm);
          else touch(rw,ln*128,WS_H+(gw*144+(cg*3+(oi-4))*2)*4096,ldm);
          SB(); }
      if constexpr(o.t==O_POLL){ ld8_sc1(pollv,rw,pollvo(oi),pollso(oi)); SB(); }
      if constexpr(o.t==O_FLAG){ waitvm<o.w>();
          put_flag(oi<3? hf_line+(j*3+oi)*8 : oi==3? kf_own : oi==4? WS_SF+gW*128+mt*8 : oi==5? WS_OF+gw*128+xs*8 : pf_w); SB(); }
      if constexpr(ROLE==0){
        constexpr int p=n<200?0:n<300?1:2;
        constexpr int m=n-silu_start(p);
        if constexpr(r==2 && m>=0 && m<64){ constexpr int k=m; x0=__builtin_amdgcn_exp2f(-ga[p&1][k/16][(k/4)%4][k%4]); pin(x0); SB(); }
        if constexpr(r==0 && m>=1 && m<65){ e1=fmaf(x0,c2v,c2v); pin(e1); SB(); }
        if constexpr(r==1 && m>=2 && m<66){ constexpr int k=m-2; float gv=ga[p&1][k/16][(k/4)%4][k%4]*e2; pin(gv); ga[p&1][k/16][(k/4)%4][k%4]=gv; SB(); }
        if constexpr(r==3 && m>=1 && m<65){ e2=__builtin_amdgcn_rcpf(e1); pin(e2); SB(); }
        constexpr int i0=(n-fin_start(p,0))*4+r, i1=(n-fin_start(p,1))*4+r;
        if constexpr(i0>=1&&i0<24){ fin(IC(0),IC(i0-1),IC(p)); SB(); }
        if constexpr(i1>=1&&i1<24){ fin(IC(1),IC(i1-1),IC(p)); SB(); }
      }
    });
  });
  STS(4);
  const int tl[3]={tl0,tl1,tC+w};
  asm volatile("s_nop 7"); SB();
  constexpr int NLATE=ROLE==0?2:12;
  if constexpr(ROLE==0) sfor<2>([&](auto i) AI { pstore(IC(9),i); });
  else sfor<12>([&](auto i) AI { pstore(IC(decltype(i)::value/2),IC(decltype(i)::value%2)); });
  constexpr int NT=ROLE==0?3:2;
  poll_check<TAB<ROLE>.tail_after_pst+NLATE>(pollv,tag,rw,pollvo(7),pollso(7));
  #pragma unroll
  for(int i=0;i<NT;i++) pin(xv[i]);
  STS(5);
  auto rsw=__builtin_amdgcn_make_buffer_rsrc((void*)ws,0,WS_BYTES,0x00020000);
  auto rso=__builtin_amdgcn_make_buffer_rsrc((void*)out,0,S*D*2,0x00020000);
  const unsigned vr=vrow();
  const unsigned skip1=(w<2)?0u:0x80000000u;
  auto ld_tile=[&](int t,u32x4* pv,unsigned sk) AI {
    #pragma unroll
    for(int k=0;k<8;k++) pv[k]=__builtin_amdgcn_raw_buffer_load_b128(rsw,l16,(WS_P+((k*36+t)*64+rp)*1024)|sk,16); };
  u32x4 selA={0,0,0,0}, selB={0,0,0,0};
  { const unsigned one=((lane>>4)==((lane&15)>>2))? ((lane&1)?0x3f800000u:0x00003f80u) : 0u;
    if(lane&2){ selA[1]=one; selB[3]=one; } else { selA[0]=one; selB[2]=one; } }
  auto sum_tile=[&](u32x4* pv,f32x4& c0,f32x4& c1) AI {
    c0=f32x4{0,0,0,0}; c1=f32x4{0,0,0,0};
    #pragma unroll
    for(int k=0;k<8;k++){ c0=__builtin_amdgcn_mfma_f32_16x16x32_bf16(__builtin_bit_cast(bf16x8,selA),__builtin_bit_cast(bf16x8,pv[k]),c0,0,0,0);
                          c1=__builtin_amdgcn_mfma_f32_16x16x32_bf16(__builtin_bit_cast(bf16x8,selB),__builtin_bit_cast(bf16x8,pv[k]),c1,0,0,0); } };
  auto fin_tile=[&](int t,const f32x4& c0,const f32x4& c1,u32x4 sv,u32x4 xx,unsigned sk) AI {
    float acc[8];
    #pragma unroll
    for(int e=0;e<8;e++){ const unsigned s2=sv[e>>1]; const float d0=e<4?c0[e&3]:c1[e&3];
      acc[e]=((e&1)?bfhi(s2):bflo(s2))*d0*__builtin_amdgcn_rcpf(1.0f+__builtin_amdgcn_exp2f(-LOG2E*d0)); }
    #pragma unroll
    for(int e=0;e<4;e++){ auto r=__builtin_amdgcn_permlane16_swap(__float_as_uint(acc[e]),__float_as_uint(acc[4+e]),false,false); acc[e]=__uint_as_float(r[0]); acc[4+e]=__uint_as_float(r[1]); }
    u32x4 o;
    #pragma unroll
    for(int e=0;e<4;e++) o[e]=pack2(acc[2*e]+bflo(xx[e]),acc[2*e+1]+bfhi(xx[e]));
    __builtin_amdgcn_raw_buffer_store_b128(o,rso,vr,(srow+t*32)|sk,0);
  };
  auto sum_fix=[&](int t,u32x4* pv,f32x4& c0,f32x4& c1) AI {
    sum_tile(pv,c0,c1);
    for(int it=0;it<4096 && __any(c0[0]!=c0[0]);it++){ bool any=false;
      #pragma unroll
      for(int k=0;k<8;k++){ unsigned m=max(max(pv[k][0],pv[k][1]),max(pv[k][2],pv[k][3]));
        if(__any(m==~0u)){ any=true; pv[k]=__builtin_amdgcn_raw_buffer_load_b128(rsw,l16,WS_P+((k*36+t)*64+rp)*1024,16); } }
      if(!any) break;
      asm volatile("":::"memory");
      sum_tile(pv,c0,c1); } };
  u32x4 pv0[8],pv1[8],pv2[8],sv[3];
  #pragma unroll
  for(int i=0;i<NT;i++) sv[i]=__builtin_amdgcn_raw_buffer_load_b128(rsw,l16,(WS_SB+(tl[i]*64+rp)*1024)|(i==1?skip1:0u),16);
  ld_tile(tl[0],pv0,0u); ld_tile(tl[1],pv1,skip1);
  if constexpr(ROLE==0) ld_tile(tl[2],pv2,0u);
  SB();
  f32x4 c0,c1;
  if constexpr(ROLE==1) sum_fix(tl[0],pv0,c0,c1); else sum_tile(pv0,c0,c1);
  fin_tile(tl[0],c0,c1,sv[0],xv[0],0u);
  if constexpr(ROLE==1) sum_fix(tl[1],pv1,c0,c1); else sum_tile(pv1,c0,c1);
  fin_tile(tl[1],c0,c1,sv[1],xv[1],skip1);
  STS(6);
  if constexpr(ROLE==0){ sum_fix(tl[2],pv2,c0,c1); fin_tile(tl[2],c0,c1,sv[2],xv[2],0u); }
  if constexpr(ROLE==0){ if(blockIdx.x==0&&tid==0) *(unsigned*)(ws+WS_EP)=e0+1u; }
}

extern "C" __global__ __launch_bounds__(256,1) void k1_main(const u16* __restrict__ x,const u16* __restrict__ wqkv,const u16* __restrict__ wo,const u16* __restrict__ wg,const u16* __restrict__ wu,const u16* __restrict__ wd,u16* __restrict__ out,char* __restrict__ ws
  ){
  u64* dbg=nullptr;
  extern __shared__ __attribute__((aligned(16))) char smem[];
  const unsigned lds0=(unsigned)(size_t)(lds_vp)smem;
  const int xs=blockIdx.x&7, idx=blockIdx.x>>3, mt=idx&7, j=idx>>3;
  u64 t_start=0;
  const int lane=threadIdx.x&63, w=__builtin_amdgcn_readfirstlane(threadIdx.x>>6);
  const i32x4 rw=mk_rsrc(ws,WS_BYTES), rx=mk_rsrc(x,S*D*2);
  const int l32=lane&31, rr=2*w+(lane>>5), m=(xs&1)*8+rr;
  const unsigned xo=((mt*4+j)*64+4*(m&~1)+(xs&6)+(m&1))*(D*2);
  const bool has3=l32<8;
  u32x4 v[3]; unsigned pv_,ep;
  ld16_plain(v[0],rx,xo+l32*16,0u); ld16_plain(v[1],rx,xo+l32*16,512u); ld16_plain(v[2],rx,has3?xo+l32*16:0x80000000u,1024u);
  ld4_plain(pv_,rx,xo,0u); ld4_plain(ep,rw,0u,WS_EP);
  SB();
  i32x4 rc;
  { unsigned long long pc; int r0_,r1_;
    asm volatile("s_getpc_b64 %0\n\ts_mov_b32 %1, k1_main@rel32@lo+4\n\ts_mov_b32 %2, k_end@rel32@lo+12":"=s"(pc),"=s"(r0_),"=s"(r1_));
    rc=mk_rsrc((const void*)(pc+(long long)r0_), r1_>r0_? (unsigned)(r1_-r0_):0u); }
  touch(rc,(w>=2&&lane<32)?lane*128:0x80000000u,w==3?idx*4096:0u,lds0+6*SLOT);
  {
    const unsigned ldm=lds0+6*SLOT, big=0x80000000u, lw=(w==3)?lane:64;
    const unsigned va=(lw<PROWS&&j<3)? (mt*PROWS+lw)*(F*2) : big;
    touch(mk_rsrc(wg,D*F*2),va,(xs*3+j)*384,ldm); touch(mk_rsrc(wu,D*F*2),va,(xs*3+j)*384,ldm);
    const unsigned vq=lw<64? ((lw>>2)*32+idx)*(3*D*2)+((lw>>1)&1)*1152+(lw&1)*140 : big;
    touch(mk_rsrc(wqkv,3*D*D*2),vq,1152+xs*144,ldm);
  }
  waitvm<4>();
  pin(v[0]); pin(v[1]); pin(v[2]); pin(pv_); pin(ep);
  const unsigned e0=__builtin_amdgcn_readfirstlane(ep);
  {
    const float piv=bflo(pv_);
    float f[24]; float s_=0,q_=0;
    #pragma unroll
    for(int i=0;i<3;i++){ float s1=0,q1=0;
      #pragma unroll
      for(int jj=0;jj<4;jj++){ float a_=bflo(v[i][jj])-piv,b_=bfhi(v[i][jj])-piv; f[i*8+2*jj]=a_; f[i*8+2*jj+1]=b_; s1+=a_+b_; q1=fmaf(a_,a_,fmaf(b_,b_,q1)); }
      if(i<2||has3){ s_+=s1; q_+=q1; } }
    s_=red32(s_); q_=red32(q_);
    const float mean=s_*(1.0f/D), var=fmaxf(q_*(1.0f/D)-mean*mean,0.f);
    const float sc=LOG2E*rsqrtf(var+1e-5f), of=-mean*sc;
    typedef __attribute__((address_space(3))) u32x4* lds_u32x4_p;
    const unsigned stg=lds0+5*SLOT;
    #pragma unroll
    for(int i=0;i<3;i++){ u32x4 o;
      #pragma unroll
      for(int jj=0;jj<4;jj++) o[jj]=pack2(fmaf(f[i*8+2*jj],sc,of),fmaf(f[i*8+2*jj+1],sc,of));
      *(lds_u32x4_p)(size_t)(stg+(l32+32*i)*128+rr*16)=o; }
    asm volatile("s_waitcnt lgkmcnt(0)":::"memory");
    __builtin_amdgcn_s_barrier();
    const unsigned vo=(lane>>5)*4096+((lane>>3)&3)*256+(lane&7)*16;
    const unsigned so=w<3? WS_XN+(mt*4+j)*18*4096+(xs>>1)*1024+(xs&1)*128+w*3*8192 : 0x80000000u;
    u32x4 o[3];
    #pragma unroll
    for(int i=0;i<3;i++) o[i]=*(lds_u32x4_p)(size_t)(stg+(w*3+i)*1024+lane*16);
    #pragma unroll
    for(int i=0;i<3;i++) st16_sc1<0>(o[i],rw,vo,so+i*8192);
  }
  if(j<3) body<0>(x,wg,wu,wd,wo,out,ws,lds0,mt,xs,j,e0,rc,t_start,dbg);
  else body<1>(x,wqkv,wo,wd,wo,out,ws,lds0,mt,xs,j,e0,rc,t_start,dbg);
}
extern "C" __global__ void k_end(){}

extern "C" void kernel_launch(void* const* d_in,const int* in_sizes,int n_in,void* d_out,int out_size,void* d_ws,size_t ws_size,hipStream_t stream){
  const u16* x=(const u16*)d_in[0]; const u16* wqkv=(const u16*)d_in[1]; const u16* wo=(const u16*)d_in[2];
  const u16* wg=(const u16*)d_in[4]; const u16* wu=(const u16*)d_in[5]; const u16* wd=(const u16*)d_in[6];
  char* ws=(char*)d_ws;
  (void)hipFuncSetAttribute((const void*)k1_main,hipFuncAttributeMaxDynamicSharedMemorySize,6*SLOT+1024);
  k1_main<<<256,256,6*SLOT+1024,stream>>>(x,wqkv,wo,wg,wu,wd,(u16*)d_out,ws
  );
}
```

```cpp
#include <hip/hip_runtime.h>
#include <utility>
typedef unsigned short u16; typedef unsigned long long u64;
typedef __attribute__((ext_vector_type(4))) unsigned u32x4;
typedef __attribute__((ext_vector_type(2))) unsigned u32x2;
typedef __attribute__((ext_vector_type(4))) int i32x4;
typedef __attribute__((ext_vector_type(4))) short s16x4;
typedef __attribute__((ext_vector_type(8))) __bf16 bf16x8;
typedef __attribute__((ext_vector_type(2))) __bf16 bf16x2;
typedef __attribute__((ext_vector_type(4))) float f32x4;
typedef __attribute__((address_space(3))) s16x4* lds_s16x4_p;
typedef __attribute__((address_space(3))) void* lds_vp;
#define DI __device__ __forceinline__
#define SB() __builtin_amdgcn_sched_barrier(0)
#define AI __attribute__((always_inline))
#define IC(x) std::integral_constant<int,(x)>{}
template<class Fn,int... I> DI void sfor_impl(Fn&& f, std::integer_sequence<int,I...>){ (f(std::integral_constant<int,I>{}),...); }
template<int N,class Fn> DI void sfor(Fn&& f){ sfor_impl(f,std::make_integer_sequence<int,N>{}); }

constexpr int S=2048,D=576,F=4608;
constexpr float LOG2E=1.4426950408889634f;
constexpr unsigned WS_EP=0;
constexpr unsigned WS_HF=4096;
constexpr unsigned WS_PF=WS_HF+32768;
constexpr unsigned WS_SF=WS_PF+16384;
constexpr unsigned WS_OF=WS_SF+4096;
constexpr unsigned WS_KF=WS_OF+4096;
constexpr unsigned WS_XF=WS_KF+32768;
constexpr unsigned WS_XN=1u<<20;
constexpr unsigned WS_O =4u<<20;
constexpr unsigned WS_OL=7u<<20;
constexpr unsigned WS_KV=11u<<20;
constexpr unsigned WS_SB=8u<<20;
constexpr unsigned WS_H =16u<<20;
constexpr unsigned WS_P =40u<<20;
constexpr unsigned WS_BYTES=64u<<20;
constexpr int SLOT=24576;

#define NTW ""
#define NTT ""
#define NTK ""
#define K0LINES 1

DI s16x4 trr(unsigned a){ return __builtin_amdgcn_ds_read_tr16_b64_v4i16((lds_s16x4_p)(size_t)a); }
DI bf16x8 cat(s16x4 a,s16x4 b){ return __builtin_bit_cast(bf16x8,__builtin_shufflevector(a,b,0,1,2,3,4,5,6,7)); }
DI unsigned pack2(float a,float b){ bf16x2 v={(__bf16)a,(__bf16)b}; return __builtin_bit_cast(unsigned,v); }
DI float bflo(unsigned v){ return __uint_as_float(v<<16); }
DI float bfhi(unsigned v){ return __uint_as_float(v&0xffff0000u); }
DI i32x4 mk_rsrc(const void* p, unsigned bytes){
  unsigned long long a=(unsigned long long)p; i32x4 r; r[0]=(int)a; r[1]=(int)(a>>32); r[2]=(int)bytes; r[3]=0x00020000; return r;
}
template<unsigned SO,unsigned LO> DI void glds16(i32x4 rsrc, unsigned voff, unsigned sbase, unsigned lbase){
  unsigned t;
  asm volatile("s_add_u32 m0, %4, %6\n\ts_add_u32 %0, %3, %5\n\tbuffer_load_dwordx4 %1, %2, %0 offen" NTW " lds":"=&s"(t):"v"(voff),"s"(rsrc),"s"(sbase),"s"(lbase),"i"(SO),"i"(LO):"memory","scc");
}
DI void touch(i32x4 rsrc, unsigned voff, unsigned soff, unsigned ldummy){
  asm volatile("s_mov_b32 m0, %3\n\ts_nop 0\n\tbuffer_load_dword %0, %1, %2 offen" NTT " lds"::"v"(voff),"s"(rsrc),"s"(soff),"s"(ldummy):"memory");
}
#define CPFA 8192
DI void code_touch(i32x4 rc, unsigned voff, unsigned ahead, unsigned ldummy){
  unsigned long long pc; asm volatile("s_getpc_b64 %0":"=s"(pc));
  touch(rc,voff,(unsigned)pc-(unsigned)rc[0]+ahead,ldummy);
}
template<bool AG,bool SC1,int OFF> DI void ldfrag(bf16x8& d,i32x4 rsrc,unsigned voff,unsigned soff){
  if constexpr(AG){ if constexpr(SC1) asm volatile("buffer_load_dwordx4 %0, %1, %2, %3 offen offset:%4 sc1":"=a"(d):"v"(voff),"s"(rsrc),"s"(soff),"i"(OFF):"memory");
                    else asm volatile("buffer_load_dwordx4 %0, %1, %2, %3 offen offset:%4":"=a"(d):"v"(voff),"s"(rsrc),"s"(soff),"i"(OFF):"memory"); }
  else { if constexpr(SC1) asm volatile("buffer_load_dwordx4 %0, %1, %2, %3 offen offset:%4 sc1":"=v"(d):"v"(voff),"s"(rsrc),"s"(soff),"i"(OFF):"memory");
         else asm volatile("buffer_load_dwordx4 %0, %1, %2, %3 offen offset:%4":"=v"(d):"v"(voff),"s"(rsrc),"s"(soff),"i"(OFF):"memory"); }
}
template<int OFF> DI void st16_sc1(u32x4 v,i32x4 rsrc,unsigned voff,unsigned soff){
  asm volatile("buffer_store_dwordx4 %0, %1, %2, %3 offen offset:%4 sc1\n\ts_nop 1"::"v"(v),"v"(voff),"s"(rsrc),"s"(soff),"i"(OFF):"memory");
}
DI void st8_sc1(u32x2 v,i32x4 rsrc,unsigned voff,unsigned soff){
  asm volatile("buffer_store_dwordx2 %0, %1, %2, %3 offen sc1"::"v"(v),"v"(voff),"s"(rsrc),"s"(soff):"memory");
}
DI void ld8_sc1(u64& d,i32x4 rsrc,unsigned voff,unsigned soff){
  asm volatile("buffer_load_dwordx2 %0, %1, %2, %3 offen sc1":"=v"(d):"v"(voff),"s"(rsrc),"s"(soff):"memory");
}
template<int OFF> DI void ld16_sc1(u32x4& d,i32x4 rsrc,unsigned voff,unsigned soff){
  asm volatile("buffer_load_dwordx4 %0, %1, %2, %3 offen offset:%4 sc1":"=v"(d):"v"(voff),"s"(rsrc),"s"(soff),"i"(OFF):"memory");
}
DI void ld4_plain(unsigned& d,i32x4 rsrc,unsigned voff,unsigned soff){
  asm volatile("buffer_load_dword %0, %1, %2, %3 offen":"=v"(d):"v"(voff),"s"(rsrc),"s"(soff):"memory");
}
DI void ld16_plain(u32x4& d,i32x4 rsrc,unsigned voff,unsigned soff){
  asm volatile("buffer_load_dwordx4 %0, %1, %2, %3 offen":"=v"(d):"v"(voff),"s"(rsrc),"s"(soff):"memory");
}
template<class T> DI void pin(T& v){ asm volatile("":"+v"(v)); }
template<int N> DI void waitvm(){ asm volatile("s_waitcnt vmcnt(%0)"::"i"(N>63?63:N):"memory"); }
template<int N> DI void poll_check(u64& v,u64 tag,i32x4 rsrc,unsigned voff,unsigned soff){
  unsigned cnt;
  asm volatile("s_waitcnt vmcnt(%5)\n\t"
               "s_mov_b32 %1, 0x4000\n"
               "1:\n\t"
               "v_cmp_ne_u64 vcc, %6, %0\n\t"
               "s_nop 0\n\t"
               "s_cbranch_vccz 2f\n\t"
               "s_sleep 1\n\t"
               "buffer_load_dwordx2 %0, %2, %3, %4 offen sc1\n\t"
               "s_sub_u32 %1, %1, 1\n\t"
               "s_waitcnt vmcnt(0)\n\t"
               "s_cbranch_scc0 1b\n"
               "2:"
               :"+v"(v),"=&s"(cnt):"v"(voff),"s"(rsrc),"s"(soff),"i"(N>63?63:N),"s"(tag):"memory","vcc","scc");
}
template<int CTRL> DI float dppadd(float v){ return v+__int_as_float(__builtin_amdgcn_update_dpp(0,__float_as_int(v),CTRL,0xf,0xf,true)); }
DI float red32(float v){
  v=dppadd<0xB1>(v); v=dppadd<0x4E>(v); v=dppadd<0x141>(v); v=dppadd<0x140>(v);
  auto r=__builtin_amdgcn_permlane16_swap(__float_as_uint(v),__float_as_uint(v),false,false);
  return __uint_as_float(r[0])+__uint_as_float(r[1]);
}
template<bool FIRST,bool AG> DI void mma(f32x4& acc,const bf16x8& a,const bf16x8& b){
  if constexpr(FIRST){ if constexpr(AG) asm volatile("v_mfma_f32_16x16x32_bf16 %0, %1, %2, 0":"=&v"(acc):"v"(a),"a"(b)); else asm volatile("v_mfma_f32_16x16x32_bf16 %0, %1, %2, 0":"=&v"(acc):"v"(a),"v"(b)); }
  else { if constexpr(AG) asm volatile("v_mfma_f32_16x16x32_bf16 %0, %1, %2, %0":"+v"(acc):"v"(a),"a"(b)); else asm volatile("v_mfma_f32_16x16x32_bf16 %0, %1, %2, %0":"+v"(acc):"v"(a),"v"(b)); }
}

constexpr int PDF=3, NBUF=4;
enum { K_A=0,K_Q=1,K_W=2,K_B=3,K_C=4 };
struct Stage{ int kind; int idx; int nsteps; };
constexpr int NST0=26, NST1=24;
template<int ROLE> constexpr int nstage(){ return ROLE==0?NST0:NST1; }
template<int ROLE> constexpr Stage stage(int s){
  if(ROLE==0){
    if(s<18) return Stage{K_A,s,24};
    constexpr int G[8][3]={{K_B,0,24},{K_B,1,24},{K_B,2,24},{K_B,3,24},{K_B,4,12},{K_C,0,24},{K_C,1,24},{K_C,2,24}};
    return Stage{G[s-18][0],G[s-18][1],G[s-18][2]};
  } else {
    if(s<14) return Stage{K_Q,s,s==13?10:20};
    if(s<17) return Stage{K_B,s-14,24};
    if(s<22) return Stage{K_W,s-17,s==21?10:20};
    return Stage{K_B,s-19,s==23?12:24};
  }
}
constexpr int a_mat(int s){ return s<6? (s&1) : (s%6)/3; }
constexpr int a_kp(int s){ return s<6? (s>>1) : s%3; }
constexpr int q_sec(int kk){ return kk<36? 1+(kk&1) : 0; }
constexpr int q_ks(int kk){ return kk<36? kk>>1 : kk-36; }
template<int ROLE> constexpr int stage_start(int s){ int n=0; for(int i=0;i<s;i++) n+=stage<ROLE>(i).nsteps; return n; }
template<int ROLE> constexpr int stage_n(int s){ int k=stage<ROLE>(s).kind; return (k==K_Q||k==K_W)?5:6; }
template<int ROLE> constexpr int nstep(){ return stage_start<ROLE>(nstage<ROLE>()); }
struct Frag{ int ph; int ks; int c; bool first; int win; int base; int off; int d1; int bd; int st; };
enum { O_NONE=0,O_GL,O_XL,O_HL,O_OL,O_HST,O_FLAG,O_POLL,O_TOUCH,O_PST,O_SST,O_KVST,O_KVLD,O_POIS,O_PLD,O_CODE };
struct Op{ short t; short i; short w; };
constexpr int NMAX=640;
struct Tab{ Frag f[NMAX]; Op op[NMAX][4]; short bar[NMAX]; short wait[NMAX]; short chk[NMAX]; short chkw[NMAX]; int tail_after_pst; int blkwait; int voB; int baseB[3]; int wopat; int olt; int err; };
constexpr int XPRO=12;
constexpr int silu_start(int p){ return p==0?121:144*p+73; }
constexpr int fin_start(int p,int t){ return p==0? (t==0?156:188) : 144*p+138+6*t; }
#define HFD0 30
#define HFD1 40
#define HFD2 20
#define CPFS 90
#define P2P 24
#define P2C 10
#define PROWS 24
#define SFD 8
#define TMK 16
#define CTM 0
#define TWLIST 212,230,248,112,140,196
#define R2ROWS 48
#define PSTS 2
#define TA0 50
#define TA1 104
#define G6AT 96
#define TA2 126
#define TA3 164
#define PLD0 22
#define PFD0 18
#define HLEAD 28
#define PFA 100
#define PFW 198
#define PFD 204
template<int ROLE> constexpr Tab make_tab(){
  Tab t{};
  constexpr int NS=nstage<ROLE>(), N=nstep<ROLE>();
  int ss[32]={}; { int n=0; for(int s=0;s<=NS;s++){ ss[s]=n; if(s<NS) n+=stage<ROLE>(s).nsteps; } }
  int firstB[3]={-1,-1,-1};
  for(int s=0;s<NS;s++){ const Stage st=stage<ROLE>(s); const int sl=s%6;
    for(int i=0;i<st.nsteps;i++){ Frag f{}; const int n=ss[s]+i;
      if(st.kind==K_A){ int kl=i/4,c=i%4;
        if(s==6){ if(i<18){ kl=i/3; c=i%3; } else { kl=i-18; c=3; } }
        else if(s==12&&i<12){ kl=(i%6)/2; c=(i/6)*2+(i&1); }
        else if((s==11||s==17)&&i>=12){ kl=3+((i-12)%6)/2; c=((i-12)/6)*2+(i&1); }
        f.ph=a_mat(s); f.ks=a_kp(s)*6+kl; f.c=c; f.first=(f.ks==0); f.win=sl/2; f.base=c; f.off=(sl%2)*SLOT+kl*4096; f.d1=2048;
      } else if(st.kind==K_Q){ int kk=4*st.idx+i/5; f.ph=q_sec(kk)==1?3:q_sec(kk)==2?4:5; f.ks=q_ks(kk); f.c=i%5; f.first=(f.ks==0); f.win=sl/2; f.base=0; f.off=(sl%2)*SLOT+(i/5)*5120+f.c*8; f.d1=2560;
      } else if(st.kind==K_W){ int kk=4*st.idx+i/5; f.ph=6; f.ks=(kk+12)%18; f.c=i%5; f.first=(kk==0); f.win=sl/2; f.base=1; f.off=(sl%2)*SLOT+(i/5)*5120+f.c*32; f.d1=2560;
      } else if(st.kind==K_B){ f.ph=2; f.ks=4*st.idx+i/6; f.c=i%6; f.base=f.c&1; f.off=(sl%2)*SLOT+(i/6)*6144+(f.c&~1)*32; f.d1=3072; f.first=(f.ks==0); f.win=sl/2;
        if(firstB[f.win]<0) firstB[f.win]=n;
      } else { const int kl=i/4, c=i%4; f.ph=7; f.ks=6*st.idx+kl; f.c=6+c; f.base=c; f.off=(sl%2)*SLOT+kl*4096; f.d1=2048; f.first=(f.ks==0); f.win=sl/2; }
      f.st=s; t.f[n]=f; } }
  auto tile_major=[&](int a,int b){ for(int i=a+1;i<b;i++){ Frag x=t.f[i]; int k=i-1; while(k>=a && t.f[k].c>x.c){ t.f[k+1]=t.f[k]; k--; } t.f[k+1]=x; } };
  if(ROLE==0){ tile_major(ss[21]+6*(TMK-12),ss[23]); tile_major(ss[23]+CTM,N); } else tile_major(ss[22],N);
  int barstep[32]={}; for(int s=0;s<NS;s++) barstep[s]=N; for(int n=N-1;n>=0;n--) barstep[t.f[n].st]=n-PDF-1;
  int tdone[10]={}; for(int n=0;n<N;n++) if(t.f[n].ph==2||t.f[n].ph==7) tdone[t.f[n].c]=n;
  { int cur[6]={0,0,0,0,0,0}; for(int n=0;n<N;n++){ Frag& f=t.f[n]; if(f.ph==2) f.base+=4; f.bd=(f.win-cur[f.base])*49152; cur[f.base]=f.win; } }
  for(int n=N;n<NMAX;n++) t.f[n]=t.f[N-1];
  for(int n=0;n<NMAX;n++){ t.bar[n]=-1; t.wait[n]=-1; t.chk[n]=-1; t.chkw[n]=0; }
  { int fb=N; for(int w=0;w<3;w++) if(firstB[w]>=0&&firstB[w]<fb) fb=firstB[w]; t.baseB[0]=fb-PDF-2; }
  auto put=[&](int ty,int idx,int n,int maxper)->int{
    constexpr int pref[4]={2,3,1,0};
    for(;;n++){ int used=0; for(int r=0;r<4;r++) used+=t.op[n][r].t!=O_NONE; if(used>=maxper) continue;
      for(int k=0;k<4;k++){ int r=pref[k]; if(t.op[n][r].t==O_NONE){ t.op[n][r]=Op{(short)ty,(short)idx,0}; return n; } } } };
  auto findB=[&](int ks,int c,int from)->int{ for(int n=from;n<N;n++) if(t.f[n].ph==2&&t.f[n].ks==ks&&t.f[n].c==c) return n; return -1; };
  if(ROLE==0) for(int p=0;p<3;p++) for(int tt=0;tt<2;tt++) for(int rb=0;rb<4;rb++){ int i=rb*6+5; int n=fin_start(p,tt)+i/4; t.op[n][i%4]=Op{O_HST,(short)(p*8+tt*4+rb),0}; }
  { int n=0; auto X=[&](int a,int b){ for(int i=a*4;i<b*4;i++) n=put(O_XL,i,n,1); }; auto G=[&](int s){ if(s>=6&&n<ss[s-4]-PDF) n=ss[s-4]-PDF; for(int i=0;i<stage_n<ROLE>(s);i++) n=put(O_GL,s*8+i,n,1); };
    if(ROLE==0){ X(3,8); G(2); X(8,12); G(3); G(4); X(12,15); G(5); X(15,18); G(6); G(7); G(8);
      put(O_TOUCH,0,TA0,2); put(O_TOUCH,1,TA1,2);
      put(O_TOUCH,8,TA2,2); put(O_TOUCH,9,TA3,2);
      }
    else { X(3,4); G(2); X(4,8); G(3); X(8,10); G(4); X(10,12); G(5); X(12,14); G(6); X(14,16); G(7); X(16,18); G(8); } }
  for(int s=9;s<NS;s++){ int b0=ss[s-4]-PDF, len=stage<ROLE>(s-4).nsteps, cnt=stage_n<ROLE>(s); for(int i=0;i<cnt;i++){ int n=put(O_GL,s*8+i,b0+i*len/cnt,1); if(stage<ROLE>(s).kind==K_B&&t.voB==0) t.voB=n; } }
  if(ROLE==1) put(O_TOUCH,1,PFA,2);
  const int b1done=findB(17,5,0)+1;
  const int u2=findB(12,0,0);
  if(ROLE==0){
    constexpr int HFD[3]={HFD0,HFD1,HFD2};
    int nf2=0;
    for(int p=0;p<3;p++){ int n=put(O_FLAG,p,fin_start(p,1)+6+HFD[p],2); if(p<2) put(O_TOUCH,4+p,n+1,2); nf2=n; }
    { constexpr int TW[6]={TWLIST}; for(int i=0;i<6;i++) put(O_TOUCH,10+i,TW[i],2); }
    for(int i=0;i<8;i++) put(O_POIS,12+i,300+4*i,1);
    put(O_POLL,0,376,2); t.chk[392]=0;
    for(int i=0;i<48;i++){ int e=432+6*(i/4)-HLEAD+(i%4); put(O_HL,i,i<16?394+2*i:e,1); }
    for(int i=0;i<8;i++){ int e=u2-HLEAD+i; put(O_HL,48+i,e>nf2+1?e:nf2+1,1); }
    put(O_POLL,1,u2-P2P,2); t.chk[u2-P2C]=1;
    for(int i=8;i<24;i++) put(O_HL,48+i,u2-P2C+1+(i-8),1);
    int lp=0;
    for(int i=0;i<18;i++){ int e=tdone[i/2]+3; if(e<lp+PSTS) e=lp+PSTS; lp=put(O_PST,i,e,1); if(i==11) put(O_FLAG,6,lp+PFD0,2); }
    for(int i=0;i<3;i++) put(O_PLD,i,b1done+8+3*i,1);
    put(O_POLL,7,N-8,3);
  } else {
    for(int i=0;i<18;i++) put(O_KVST,i,182+i,1);
    put(O_FLAG,3,224,2); put(O_POLL,2,236,2); t.chk[250]=2;
    for(int i=0;i<5;i++) put(O_KVLD,i,251+i,2);
    put(O_POLL,3,258,2);
    t.chk[271]=4;
    for(int i=24;i<48;i++) put(O_HL,i,272+(i-24),1);
    put(O_FLAG,5,288,2);
    put(O_POLL,5,308,2); t.chk[322]=5;
    const int w0=ss[17];
    for(int i=0;i<16;i++) put(O_OL,48+i,323+i,1);
    for(int i=0;i<8;i++) put(O_OL,64+i,339+i,1);
    for(int i=0;i<48;i++) put(O_OL,i,348+i,1);
    t.olt=w0+19;
    for(int i=0;i<10;i++) put(O_SST,i,u2+3+i,1);
    put(O_FLAG,4,N-SFD,2);
    for(int i=0;i<12;i++) put(O_POIS,i,120+3*i,1);
    put(O_FLAG,6,232,2);
    put(O_POLL,1,u2-52,2); t.chk[u2-38]=1;
    for(int i=0;i<24;i++) put(O_HL,48+i,u2-37+i,2);
    put(O_POLL,6,u2+2,2); t.chk[u2+20]=6;
    for(int i=0;i<2;i++) put(O_PLD,i,u2-6+3*i,1);
    for(int i=0;i<10;i++) put(O_PST,i,tdone[i/2]+3,1);
  }
  int sGL[32]={},cGL[32]={},sS[18]={},gen[18][4]={},sHST[3]={},sPoll[9]={},sKVST=0,sKVLD=0,sSST=0,sPST=0,sOST=0,sOLL=-1,cOLL=0,sPLD=0;
  bool chkd[9]={};
  for(int i=0;i<9;i++) sPoll[i]=-1;
  int seq=1; sGL[0]=sGL[1]=0; cGL[0]=cGL[1]=stage_n<ROLE>(0);
  for(int i=0;i<XPRO;i++){ sS[i/4]=seq++; gen[i/4][i%4]=1; }
  int need_done=0, nb=1;
  for(int n=0;n<N;n++){
    if(ROLE==1&&n==270){ if(sPoll[3]<sKVLD) t.err=11000; t.blkwait=seq-sPoll[3]-1; sPoll[3]=-1; chkd[3]=true;
      for(int i=0;i<24;i++){ sS[i/4]=seq++; gen[i/4][i%4]=2; }
      sPoll[4]=seq++; seq+=9; sOST=seq-1; }
    int need=-1; const Frag f=t.f[n];
    if(nb<NS && n==barstep[nb]){ t.bar[n]=nb; need=sGL[nb]; if(cGL[nb]!=stage_n<ROLE>(nb)) t.err=1000+n; nb++; }
    if(ROLE==1&&n==t.olt){ if(cOLL!=8) t.err=2000+n; if(sOLL>need) need=sOLL; for(int r=0;r<4;r++){ gen[16][r]=3; gen[17][r]=3; } sS[16]=sS[17]=-1; }
    { const int eg=(f.ph==2||f.ph==7)?2:f.ph==6?3:1; for(int r=0;r<4;r++) if(gen[f.ks][r]!=eg) t.err=3000+n; if(sS[f.ks]>need) need=sS[f.ks]; }
    if(need>need_done){ t.wait[n]=(short)(seq-need-1); need_done=need; }
    if(t.chk[n]>=0){ if(sPoll[t.chk[n]]<0) t.err=4000+n; t.chkw[n]=(short)(seq-sPoll[t.chk[n]]-1); chkd[t.chk[n]]=true; sPoll[t.chk[n]]=-1; }
    for(int r=0;r<4;r++){ Op& o=t.op[n][r]; if(o.t==O_NONE) continue;
      switch(o.t){
        case O_GL: sGL[o.i/8]=seq; cGL[o.i/8]++; if(o.i/8>=6 && nb<=o.i/8-4) t.err=5000+n; break;
        case O_XL: sS[o.i/4]=seq; gen[o.i/4][o.i%4]=1; break;
        case O_HL: { int k=(o.i%72)/4; if(k==f.ks) t.err=6000+n; sS[k]=seq; gen[k][o.i%4]=2;
                     int c=k>=12?((ROLE==0&&k<14)?8:1):(ROLE==0?0:(k<6?3:4)); if(!chkd[c]) t.err=7000+n; } break;
        case O_OL: if(!chkd[5]) t.err=8000+n; if(o.i<64){ int k=o.i/4; if(k==f.ks) t.err=6000+n; sS[k]=seq; gen[k][o.i%4]=3; } else { sOLL=seq; cOLL++; } break;
        case O_HST: sHST[o.i/8]=seq; break; case O_PLD: sPLD=seq; break;
        case O_POLL: for(int i=0;i<8;i++) if(sPoll[i]>=0) t.err=9000+n; sPoll[o.i]=seq; break;
        case O_KVST: sKVST=seq; break; case O_KVLD: if(!chkd[2]) t.err=8500+n; sKVLD=seq; break;
        case O_SST: sSST=seq; break; case O_PST: if(o.i<12) sPST=seq; break; case O_POIS: sPST=seq; break;
        case O_FLAG: { int l=o.i<3?sHST[o.i]:o.i==3?sKVST:o.i==4?sSST:o.i==5?sOST:sPST; o.w=(short)(seq-l-1); if(o.i==2) chkd[8]=true; } break;
        default: break; }
      seq++; }
  }
  if(nb!=NS) t.err=12000+nb;
  t.tail_after_pst=ROLE==0? seq-sPoll[7]-1 : seq-sPLD-1;
  for(int n=N;n<NMAX;n++) for(int r=0;r<4;r++) if(t.op[n][r].t!=O_NONE) t.err=10000+n;
  return t;
}
template<int ROLE> inline constexpr Tab TAB=make_tab<ROLE>();
template<int R,int E> struct ErrIs{ static_assert(E==0,"schedule error"); };
template struct ErrIs<0,TAB<0>.err>; template struct ErrIs<1,TAB<1>.err>;

DI void dot2a(float& acc,unsigned a,unsigned b){ asm volatile("v_dot2_f32_bf16 %0, %1, %2, %0":"+v"(acc):"v"(a),"v"(b)); }
DI void dot2i(float& acc,unsigned a,unsigned b,float c){ asm volatile("v_dot2_f32_bf16 %0, %1, %2, %3":"=v"(acc):"v"(a),"v"(b),"v"(c)); }
DI void dot2z(float& acc,unsigned a,unsigned b){ asm volatile("v_dot2_f32_bf16 %0, %1, %2, 0":"=v"(acc):"v"(a),"v"(b)); }
template<int CTRL> DI unsigned dppmov(unsigned old,unsigned src){ return (unsigned)__builtin_amdgcn_update_dpp((int)old,(int)src,CTRL,0xf,0xf,false); }
template<int DL,int BK> DI unsigned shiftc(unsigned cur,unsigned h){
  constexpr int sh=4*BK+(DL==1?2:0);
  unsigned t; if constexpr(sh==0) t=h; else t=(unsigned)__builtin_amdgcn_mov_dpp((int)h,0x100+sh,0xf,0xf,true);
  return dppmov<0x110+2*DL>(t,cur);
}

template<int ROLE>
DI void body(const u16* __restrict__ x,const u16* __restrict__ w0,const u16* __restrict__ w1,const u16* __restrict__ wd,const u16* __restrict__ wo,u16* __restrict__ out,char* __restrict__ ws,
             unsigned lds0,int mt,int xs,int j,unsigned e0,i32x4 rc,u64 t_start,u64* dbg){
  const u64 tag=((u64)(e0*0x9E3779B1u+0x7F4A7C15u)<<32)|(u64)(e0+1u);
  const int tid=threadIdx.x, lane=tid&63, w=__builtin_amdgcn_readfirstlane(tid>>6);
  const i32x4 r0=mk_rsrc(w0,ROLE==0?D*F*2:D*3*D*2), r1=mk_rsrc(w1,ROLE==0?D*F*2:D*D*2), rd=mk_rsrc(wd,D*F*2), rw=mk_rsrc(ws,WS_BYTES);
  const unsigned l16=lane*16;
  const int g=lane>>4, q=(lane&15)>>2, p=lane&3;
  const int gw=mt*4+w;
  const int cg=xs*3+j;
  constexpr int NSTEP=nstep<ROLE>();
  #define STS(i)
  unsigned voA[5];
  unsigned soA[3];
  const int rot=ROLE==0?j:0;
  const int rotx=0;
  if constexpr(ROLE==0){
    voA[0]=(8*((lane>>5)&1)+((lane>>3)&3))*(F*2)+(((lane&7)^(((lane>>4)&3)<<1))*16);
    #pragma unroll
    for(int k=0;k<3;k++){ int kp=k+rotx; kp=kp>=3?kp-3:kp; soA[k]=(kp*192+16*(w&1)+4*(w>>1))*(F*2)+cg*3*128; }
  } else {
    #pragma unroll
    for(int i=0;i<5;i++){ int idx=64*i+lane,rho=idx/10,pc=idx%10; int k=8*((rho>>2)&3)+4*(rho>>4)+(rho&3); voA[i]=k*(3*D*2)+(pc>8?8:pc)*16; }
    soA[0]=xs*144;
  }
  auto wo_pat=[&](int i,int kind) AI -> unsigned { unsigned ln=lane; pin(ln);
    int idx=64*i+ln,rho=idx/10,pc=idx-rho*10; int gg=(rho>>2)&3,jj=4*(rho>>4)+(rho&3);
    int slot=kind? 16+(jj&1) : 8*(w&1)+jj; int od=gg&1; int feat=od?(slot<2?16+slot:slot-2):slot;
    int row=18*gg+feat+(kind?72*(jj>>1):0); return row*(D*2)+pc*16; };
  const int gW=xs*4+w, tW=9*(mt>>1)+4*(mt&1);
  const unsigned soW=tW*32;
  unsigned vo1[3],voC;
  auto set_voB=[&]() AI { unsigned ln=lane; pin(ln);
    #pragma unroll
    for(int t=0;t<3;t++){ int idx=64*t+ln; int row=idx/12,pc=idx-row*12; vo1[t]=row*(D*2)+((pc^(((row>>2)&1)<<1))*16); pin(vo1[t]); }
    if constexpr(ROLE==0){ voC=(ln>>3)*(D*2)+(((ln&7)^(((ln>>4)&3)<<1))*16); pin(voC); } };
  const int tB=(0x1e180a04u>>(8*j))&255, tC=(0x00141000u>>(8*j))&255;
  const unsigned soB=(xs*576)*(D*2)+tB*32;
  unsigned soC[3],soH[3];
  #pragma unroll
  for(int k=0;k<3;k++){ int jp=k+rot; jp=jp>=3?jp-3:jp; soC[k]=(xs*576+jp*192+8*w)*(D*2)+tC*32; soH[k]=WS_H+(gw*144+xs*18+jp*6)*4096; }
  const unsigned lA=lds0+w*(ROLE==0?1024:5120), lB=lds0+w*6144;
  const unsigned w8=8*w;
  auto do_glds=[&](auto stc,auto ic) AI {
    constexpr int s_=decltype(stc)::value,i=decltype(ic)::value; constexpr Stage st=stage<ROLE>(s_); constexpr int sl=s_%6;
    if constexpr(st.kind==K_A){
      constexpr int pr=s_/6,mat=a_mat(s_),kp=a_kp(s_);
      glds16<pr*128+i*32*(F*2),sl*SLOT+i*4096>(mat?r1:r0,voA[0],soA[kp],lA);
    } else if constexpr(st.kind==K_Q){
      constexpr auto qoff=[](int kk){ if(kk>53) kk=53; return q_ks(kk)*3+q_sec(kk); };
      constexpr int b=4*st.idx;
      constexpr unsigned KP=qoff(b)|(qoff(b+1)<<8)|(qoff(b+2)<<16)|(qoff(b+3)<<24);
      const unsigned u=(KP>>w8)&255u; const unsigned ks=(u*171u)>>9, sec=u-3*ks;
      glds16<0,sl*SLOT+i*1024>(r0,voA[i],soA[0]+(ks*96+sec)*1152,lA);
    } else if constexpr(st.kind==K_W){
      constexpr auto woff=[](int kk){ if(kk>17) kk=17; return kk<4? kk>>1 : kk<6? (kk-4)*4 : (kk-2)>>1; };
      constexpr int b=4*st.idx;
      constexpr unsigned KP=woff(b)|(woff(b+1)<<8)|(woff(b+2)<<16)|(woff(b+3)<<24);
      unsigned u=(KP>>w8)&255u;
      if constexpr(st.idx==0){ voA[i]=wo_pat(i,0); pin(voA[i]); }
      unsigned vo=voA[i];
      if constexpr(st.idx==1){ const unsigned msk=(w<2)?0u:~0u; u=(u+(mt&msk))&7u; unsigned pl=wo_pat(i,1); vo=(w<2)?pl:vo; } else u=(u+mt)&7u;
      glds16<0,sl*SLOT+i*1024>(r1,vo,soW+u*(72*D*2),lA);
    } else if constexpr(st.kind==K_B){
      constexpr auto rowb=[](int kk){ if(kk>17) kk=17; int l=kk/2; return (l%3)*6+(l/3)*2+(kk&1); };
      constexpr int b=4*st.idx;
      constexpr unsigned KP=rowb(b)|(rowb(b+1)<<8)|(rowb(b+2)<<16)|(rowb(b+3)<<24);
      unsigned rsel=((KP>>w8)&255u)+rot*6; rsel=rsel>=18?rsel-18:rsel;
      glds16<(i/3)*16*(D*2),sl*SLOT+i*1024>(rd,vo1[i%3],soB+rsel*(32*D*2),lB);
    } else {
      glds16<(st.idx*64+(i&1)*32)*(D*2),sl*SLOT+i*4096>(rd,voC,soC[i/2],lA);
    }
  };
  unsigned soX[3];
  #pragma unroll
  for(int k=0;k<3;k++){ int kp=k+rotx; kp=kp>=3?kp-3:kp; soX[k]=WS_XN+(gw*18+kp*6)*4096; }
  bf16x8 xr[18][4];
  auto ld_x=[&](auto ic) AI { constexpr int i=decltype(ic)::value,ks=i/4,r=i%4; ldfrag<(ks<16),true,r*1024>(xr[ks][r],rw,l16,soX[ks/6]+(ks%6)*4096); };
  auto ld_o=[&](auto ic) AI { constexpr int i=decltype(ic)::value,sl=i/4,r=i%4; constexpr int ks=sl>=12?sl-12:sl+4; ldfrag<true,true,r*1024>(xr[sl][r],rw,l16,WS_O+(gW*18+((ks+2*mt)&15))*4096); };
  auto ld_h=[&](auto ic) AI { constexpr int i=decltype(ic)::value%72,kk=i/4,r=i%4; constexpr int l=kk/2;
    ldfrag<(kk<16),true,r*1024>(xr[kk][r],rw,l16,soH[l%3]+((l/3)*2+(kk&1))*4096); };
  const unsigned hf_line=WS_HF+((mt*8+xs)*4+w)*128;
  auto put_flag=[&](unsigned so) AI { unsigned ln=lane; pin(ln); u32x2 tv={(unsigned)tag,(unsigned)(tag>>32)}; pin(tv); st8_sc1(tv,rw,ln==0?0u:0x40000000u,so); };
  u64 pollv=0;
  constexpr int G0=stage_n<ROLE>(0);
  sfor<G0>([&](auto i) AI { do_glds(IC(0),i); });
  waitvm<G0+2>();
  put_flag(w<3? WS_XF+(mt*4+j)*256+(xs*3+w)*8 : 0x80000000u);
  sfor<G0>([&](auto i) AI { do_glds(IC(1),i); });
  { const unsigned vp=(lane%24)*8;
    ld8_sc1(pollv,rw,vp,WS_XF+gw*256); poll_check<0>(pollv,tag,rw,vp,WS_XF+gw*256); }
  sfor<XPRO>([&](auto i) AI { ld_x(i); });
  unsigned bw[6];
  if constexpr(ROLE==0){ const int row=4*g+q; unsigned b0=lds0+row*128+32*((row>>1)&3)+8*p;
    #pragma unroll
    for(int c=0;c<4;c++){ bw[c]=b0^(c*32); pin(bw[c]); } }
  else { bw[0]=lds0+(4*g+q)*160+36*p-4*(p&1); bw[1]=lds0+(4*g+q)*160+8*p; pin(bw[0]); pin(bw[1]); }
  auto set_bases_B=[&]() AI { unsigned ln=lane; pin(ln); const int g2=ln>>4,row=4*g2+((ln&15)>>2),p2=ln&3;
    bw[4]=lds0+row*192+8*p2+(g2&1)*32; bw[5]=bw[4]+(((g2&1)^1)-(g2&1))*32; pin(bw[4]); pin(bw[5]); };
  float c2v=LOG2E*LOG2E; pin(c2v);
  __builtin_amdgcn_s_barrier();
  STS(1);
  f32x4 ga[2][5][4],ua[5][4],da[10][4];
  bf16x8 a[NBUF];
  auto faddr=[&](auto nc) AI -> unsigned { constexpr Frag f=TAB<ROLE>.f[decltype(nc)::value]; return bw[f.base]+f.off; };
  sfor<PDF>([&](auto ic) AI { constexpr int i=decltype(ic)::value; constexpr Frag f=TAB<ROLE>.f[i]; unsigned ad=faddr(ic); a[i]=cat(trr(ad),trr(ad+f.d1)); });
  float x0=0,e1=0,e2=0; u32x4 hv;
  SB();
  auto fin=[&](auto tc,auto icn,auto prc) AI {
    constexpr int t=decltype(tc)::value, i=decltype(icn)::value, pr=decltype(prc)::value; constexpr int rb=i/6, jj=i%6;
    if constexpr(jj<4){ constexpr int cc=2*t+(jj>>1), e=(jj&1)*2; unsigned v=pack2(ga[pr&1][cc][rb][e]*ua[cc][rb][e],ga[pr&1][cc][rb][e+1]*ua[cc][rb][e+1]); pin(v); hv[jj]=v; }
  };
  auto pk8=[&](const f32x4& u0,const f32x4& u1) AI -> u32x4 { u32x4 v; v[0]=pack2(u0[0],u0[1]); v[1]=pack2(u0[2],u0[3]); v[2]=pack2(u1[0],u1[1]); v[3]=pack2(u1[2],u1[3]); return v; };
  const unsigned pbase=WS_P+(xs*36*64+gw*2)*1024;
  auto pstore=[&](auto tc,auto hc) AI { constexpr int t=decltype(tc)::value,hh=decltype(hc)::value; st16_sc1<hh*1024>(pk8(da[t][2*hh],da[t][2*hh+1]),rw,l16,pbase+(t<6?tB+t:tC+t-6)*65536); };
  const unsigned pf_w=WS_PF+((mt*4+j)*4+w)*128+xs*8;
  u32x4 kq[9],vq[9],hk[5],OL[8];
  const bool odd=g&1;
  const unsigned kf_own=WS_KF+(xs*32+gw)*128, kf_prev=WS_KF+(xs*32+(gw>0?gw-1:0))*128;
  const unsigned kv_prev=gw>0?WS_KV+(xs*32+gw-1)*5120:0x80000000u;

  const int gc=mt*4+(xs>>1), rp=mt*8+xs;
  const unsigned pf_r=WS_PF+((mt*4+j)*4+(xs>>1))*128;
  const int tl0=tB+w, tl1=tB+(int)((0x03020504u>>w8)&255u);
  u32x4 xv[3],pv0[8],pv1[8]; bool early=false; unsigned pld_base=0;
  const unsigned skip1=(w<2)?0u:0x80000000u;
  auto vrow=[&]() AI -> unsigned { unsigned ln=lane; pin(ln); return (4*(ln&14)+(ln&1)+2*((ln>>4)&1))*(D*2)+(ln>>5)*16; };
  const unsigned srow=(gc*64+4*(xs&1))*(D*2);
  auto pollvo=[&](int id) AI -> unsigned { unsigned ln=lane; pin(ln);
    return id==0? ((ln%6)/2*3+(ln%6)%2)*8 : id==1? ((ln%3)*3+2)*8 : id==2? 0u : id==3? (ln%3)*24 : id==4? ((ln%3)*3+1)*8 : (ln&7)*8; };
  auto pollso=[&](int id) AI -> unsigned { return id==2? kf_prev : id==5? WS_OF+gW*128 : id==6? pf_r : id==7? pf_r : id==8? WS_SF+gc*128 : hf_line; };
  sfor<NSTEP>([&](auto nc) AI {
    constexpr int n=decltype(nc)::value;
    constexpr Frag f=TAB<ROLE>.f[n];
    if constexpr(ROLE==1 && n==180){
      asm volatile("s_nop 7"); SB();
      #pragma unroll
      for(int r=0;r<4;r++){
        #pragma unroll
        for(int pi=0;pi<9;pi++) kq[pi][r]=pack2(ga[0][pi/2][r][(pi&1)*2],ga[0][pi/2][r][(pi&1)*2+1]);
        unsigned t9=pack2(ga[0][4][r][2],ga[0][4][r][3]), t0=kq[0][r]; pin(t9); pin(t0); kq[0][r]=odd?t9:t0;
      }
      #pragma unroll
      for(int pi=0;pi<9;pi++) pin(kq[pi]);
      #pragma unroll
      for(int f2=0;f2<9;f2++)
        #pragma unroll
        for(int e2_=0;e2_<2;e2_++){ const int pos=2*f2+e2_;
          unsigned A=pack2(ua[pos/4][2][pos%4],ua[pos/4][3][pos%4]), B=pack2(ua[pos/4][0][pos%4],ua[pos/4][1][pos%4]);
          if(f2==0){ unsigned A9=pack2(ua[4][2][2+e2_],ua[4][3][2+e2_]), B9=pack2(ua[4][0][2+e2_],ua[4][1][2+e2_]); pin(A); pin(B); pin(A9); pin(B9); A=odd?A9:A; B=odd?B9:B; }
          vq[f2][2*e2_]=A; vq[f2][2*e2_+1]=B; }
      #pragma unroll
      for(int pi=0;pi<9;pi++) pin(vq[pi]);
      SB();
    }
    if constexpr(ROLE==1 && n==270){
      STS(2);
      poll_check<TAB<ROLE>.blkwait>(pollv,tag,rw,pollvo(3),hf_line);
      asm volatile("s_nop 7"); pin(hk[0]); pin(hk[1]); pin(hk[2]); pin(hk[3]); pin(hk[4]); SB();
      unsigned qp[4][9];
      #pragma unroll
      for(int r=0;r<4;r++){
        #pragma unroll
        for(int pi=0;pi<9;pi++){ qp[r][pi]=pack2(ga[0][pi/2][r][(pi&1)*2],ga[0][pi/2][r][(pi&1)*2+1]); pin(qp[r][pi]); }
        unsigned t9=pack2(ga[0][4][r][2],ga[0][4][r][3]); pin(t9); qp[r][0]=odd?t9:qp[r][0];
      }
      const int am=(lane&15)>>1;
      const float b1=(gw==0&&am<1)?-1e30f:0.f, b2=(gw==0&&am<2)?-1e30f:0.f;
      float lg[4][8];
      sfor<9>([&](auto pc) AI { constexpr int pi=decltype(pc)::value;
        if constexpr(pi<8){ ld_h(IC(3*pi)); ld_h(IC(3*pi+1)); ld_h(IC(3*pi+2)); }
        unsigned ke[12];
        #pragma unroll
        for(int r=0;r<4;r++){ unsigned c=kq[pi][r], h=hk[pi/4][r]; ke[8+r]=c; ke[4+r]=shiftc<1,pi%4>(c,h); if(r>0) ke[r]=shiftc<2,pi%4>(c,h); }
        #pragma unroll
        for(int ww=0;ww<8;ww++)
          #pragma unroll
          for(int r=3;r>=0;r--){
            if(pi>0) dot2a(lg[r][ww],ke[8+r-ww],qp[r][pi]);
            else if(ww<=r) dot2z(lg[r][ww],ke[8+r-ww],qp[r][pi]);
            else dot2i(lg[r][ww],ke[8+r-ww],qp[r][pi],ww-r<=4?b1:b2);
          }
      });
      asm volatile("s_nop 2"); SB();
      const float csc=0.23570226039551584f/LOG2E;
      unsigned pp[4][5];
      #pragma unroll
      for(int r=0;r<4;r++){
        float mx=__builtin_fmaxf(__builtin_fmaxf(__builtin_fmaxf(lg[r][0],lg[r][1]),__builtin_fmaxf(lg[r][2],lg[r][3])),__builtin_fmaxf(__builtin_fmaxf(lg[r][4],lg[r][5]),__builtin_fmaxf(lg[r][6],lg[r][7])));
        const float mo=-mx*csc; float sum=0;
        #pragma unroll
        for(int ww=0;ww<8;ww++){ lg[r][ww]=__builtin_amdgcn_exp2f(fmaf(lg[r][ww],csc,mo)); sum+=lg[r][ww]; }
        const float ri=__builtin_amdgcn_rcpf(sum*LOG2E);
        #pragma unroll
        for(int ww=0;ww<8;ww++) lg[r][ww]*=ri;
        if(r&1){
          #pragma unroll
          for(int t=0;t<4;t++) pp[r][t]=pack2(lg[r][2*t+1],lg[r][2*t]);
        } else {
          pp[r][0]=pack2(lg[r][0],0.f);
          #pragma unroll
          for(int t=1;t<4;t++) pp[r][t]=pack2(lg[r][2*t],lg[r][2*t-1]);
          pp[r][4]=pack2(0.f,lg[r][7]);
        }
      }
      SB();
      unsigned op[4][9];
      float o2[2][4][2];
      sfor<10>([&](auto fc) AI { constexpr int f2=decltype(fc)::value; constexpr int si=9+f2;
        if constexpr(f2==5) ld8_sc1(pollv,rw,pollvo(4),hf_line);
        if constexpr(f2<9){
          unsigned pr[2][6];
          #pragma unroll
          for(int e2_=0;e2_<2;e2_++){
            unsigned A=vq[f2][2*e2_],B=vq[f2][2*e2_+1],hA=hk[si/4][2*e2_],hB=hk[si/4][2*e2_+1];
            pr[e2_][0]=A; pr[e2_][1]=B; pr[e2_][2]=shiftc<1,si%4>(A,hA); pr[e2_][3]=shiftc<1,si%4>(B,hB); pr[e2_][4]=shiftc<2,si%4>(A,hA); pr[e2_][5]=shiftc<2,si%4>(B,hB); }
          #pragma unroll
          for(int t=0;t<5;t++)
            #pragma unroll
            for(int r=3;r>=0;r--)
              #pragma unroll
              for(int e2_=0;e2_<2;e2_++){ const int s0=(r>=2)?0:1;
                if(t==0) dot2z(o2[f2&1][r][e2_],pr[e2_][s0],pp[r][0]);
                else if(t<4||!(r&1)) dot2a(o2[f2&1][r][e2_],pr[e2_][s0+t],pp[r][t]); }
        } else asm volatile("s_nop 2");
        SB();
        if constexpr(f2>0){
          #pragma unroll
          for(int r=0;r<4;r++){ op[r][f2-1]=pack2(o2[(f2-1)&1][r][0],o2[(f2-1)&1][r][1]); pin(op[r][f2-1]); }
        }
      });
      sfor<8>([&](auto ic) AI { constexpr int r=decltype(ic)::value/2,s2=decltype(ic)::value%2;
        u32x4 v={op[r][4*s2],op[r][4*s2+1],op[r][4*s2+2],op[r][4*s2+3]}; st16_sc1<r*1024>(v,rw,l16,WS_O+(gw*18+2*xs+s2)*4096); });
      { u32x4 v={op[0][8],op[1][8],op[2][8],op[3][8]}; st16_sc1<0>(v,rw,l16,WS_OL+(gw*8+xs)*1024); }
      STS(3);
      SB();
    }
    if constexpr(TAB<ROLE>.wait[n]>=0){ waitvm<TAB<ROLE>.wait[n]>(); SB(); }
    if constexpr(TAB<ROLE>.bar[n]>=0){ __builtin_amdgcn_s_barrier(); SB(); }
    if constexpr(ROLE==1 && n==TAB<ROLE>.olt){
      #pragma unroll
      for(int hh=0;hh<8;hh++) pin(OL[hh]);
      #pragma unroll
      for(int aa=0;aa<2;aa++)
        #pragma unroll
        for(int r=0;r<4;r++){ u32x4 v={OL[4*aa][r],OL[4*aa+1][r],OL[4*aa+2][r],OL[4*aa+3][r]}; pin(v); xr[16+aa][r]=__builtin_bit_cast(bf16x8,v); }
      SB();
    }
    if constexpr(ROLE==0&&TAB<ROLE>.chk[n]==7){ waitvm<TAB<ROLE>.chkw[n]>(); pin(pollv); early=__builtin_amdgcn_ballot_w64(pollv!=tag)==0; pld_base=early?WS_P+rp*1024:0x80000000u; SB(); }
    else if constexpr(TAB<ROLE>.chk[n]>=0){ poll_check<TAB<ROLE>.chkw[n]>(pollv,tag,rw,pollvo(TAB<ROLE>.chk[n]),pollso(TAB<ROLE>.chk[n])); SB(); }
    if constexpr(n==TAB<ROLE>.voB){ set_voB(); SB(); }
    if constexpr(n==TAB<ROLE>.baseB[0]){ set_bases_B(); SB(); }
    constexpr int nn=n+PDF; constexpr Frag fn=TAB<ROLE>.f[nn];
    s16x4 lo;
    sfor<4>([&](auto rc) AI {
      constexpr int r=decltype(rc)::value;
      if constexpr(f.ph==1||f.ph==4) mma<f.first,(f.ks<16)>(ua[f.c][r],a[n%NBUF],xr[f.ks][r]);
      else if constexpr(f.ph==2||f.ph==7) mma<f.first,(f.ks<16)>(da[f.c][r],a[n%NBUF],xr[f.ks][r]);
      else mma<f.first,(f.ks<16)>(ga[f.ph==0?(n/144)&1:0][f.c][r],a[n%NBUF],xr[f.ks][r]);
      SB();
      if constexpr(nn<NSTEP){
        if constexpr(r==0){ if constexpr(fn.bd!=0){ bw[fn.base]+=fn.bd; pin(bw[fn.base]); } lo=trr(faddr(IC(nn))); SB(); }
        if constexpr(r==1){ a[nn%NBUF]=cat(lo,trr(faddr(IC(nn))+fn.d1)); SB(); }
      }
      constexpr Op o=TAB<ROLE>.op[n][r]; constexpr int oi=o.i;
      if constexpr(o.t==O_GL){ do_glds(IC(oi/8),IC(oi%8)); SB(); }
      if constexpr(o.t==O_XL){ ld_x(IC(oi)); SB(); }
      if constexpr(o.t==O_HL){ ld_h(IC(oi)); SB(); }
      if constexpr(o.t==O_OL){ if constexpr(oi<64) ld_o(IC(oi)); else ld16_sc1<(oi&3)*1024>(OL[oi&7],rw,l16,WS_OL+(gW*8+(oi&4))*1024); SB(); }
      if constexpr(o.t==O_HST){ st16_sc1<(oi%4)*1024>(hv,rw,l16,WS_H+(gw*144+(cg*3+oi/8)*2+(oi/4)%2)*4096); SB(); }
      if constexpr(o.t==O_PST){ pstore(IC(oi/2),IC(oi%2)); SB(); }
      if constexpr(o.t==O_PLD && oi>=8){ ld16_sc1<0>(oi<16?pv0[oi&7]:pv1[oi&7],rw,l16,(pld_base+((oi&7)*36+(oi<16?tl0:tl1))*65536)|(oi<16?0u:skip1)); SB(); }
      if constexpr(o.t==O_PLD && oi<8){ ld16_plain(xv[oi],mk_rsrc(x,S*D*2),vrow(),srow+(oi==0?tl0:oi==1?tl1:tC+w)*32); SB(); }
      if constexpr(o.t==O_CODE){ unsigned ln=lane; pin(ln); code_touch(rc,ln<16?ln*128:0x80000000u,w*2048+CPFA,lds0+6*SLOT); SB(); }
      if constexpr(o.t==O_POIS){ constexpr int t=oi/2,hh=oi%2; unsigned m1=~0u; pin(m1); u32x4 v={m1,m1,m1,m1}; st16_sc1<hh*1024>(v,rw,l16,pbase+(t<6?tB+t:tC+t-6)*65536); SB(); }
      if constexpr(o.t==O_SST){ constexpr int t=oi/2,hh=oi%2; unsigned so=WS_SB+((tW+t)*64+gW*2)*1024; if constexpr(t==0) so|=(unsigned)(mt&1)<<31;
          st16_sc1<hh*1024>(pk8(ga[0][t][2*hh],ga[0][t][2*hh+1]),rw,l16,so); SB(); }
      if constexpr(o.t==O_KVST){ const unsigned vk=((lane&15)>=12)?(g*4+(lane&3))*16u:0x80000000u;
          st16_sc1<(oi%16)*256>(oi<9?kq[oi%9]:vq[oi%9],rw,vk,WS_KV+(xs*32+gw)*5120+(oi/16)*4096); SB(); }
      if constexpr(o.t==O_KVLD){ const unsigned vk=q*256+(g*4+p)*16; ld16_sc1<0>(hk[oi],rw,vk,kv_prev+oi*1024); SB(); }
      if constexpr(o.t==O_TOUCH){
          unsigned ln=lane; pin(ln); const unsigned ldm=lds0+6*SLOT;
          if constexpr(oi>=10 && oi<13) touch(mk_rsrc(wo,D*D*2),ln<18?ln*128:0x80000000u,((j*8+mt)*4+w)*6912+(oi-10)*2304,ldm);
          else if constexpr(oi>=13) touch(rd,ln<18?ln*128:0x80000000u,xs*576*(D*2)+((j*8+mt)*4+w)*6912+(oi-13)*2304,ldm);
          else if constexpr((oi<2||oi>=8) && ROLE==0) touch((oi&1)?r1:r0,ln<18?ln*(F*2):0x80000000u,gw*18*(F*2)+(cg*3+1+oi/8)*128,ldm);
          else if constexpr(oi<2) touch(r0,ln<36?(ln>>1)*(3*D*2)+(ln&1)*140:0x80000000u,gw*18*(3*D*2)+(oi?0:2)*1152+xs*144,ldm);
          else if constexpr(oi==2) touch(mk_rsrc(wo,D*D*2),ln<54?ln*128:0x80000000u,((j*8+mt)*4+w)*6912,ldm);
          else if constexpr(oi==3) touch(rd,ln<54?ln*128:0x80000000u,xs*576*(D*2)+((j*8+mt)*4+w)*6912,ldm);
          else touch(rw,ln*128,WS_H+(gw*144+(cg*3+(oi-4))*2)*4096,ldm);
          SB(); }
      if constexpr(o.t==O_POLL){ ld8_sc1(pollv,rw,pollvo(oi),pollso(oi)); SB(); }
      if constexpr(o.t==O_FLAG){ waitvm<o.w>();
          put_flag(oi<3? hf_line+(j*3+oi)*8 : oi==3? kf_own : oi==4? WS_SF+gW*128+mt*8 : oi==5? WS_OF+gw*128+xs*8 : pf_w); SB(); }
      if constexpr(ROLE==0){
        constexpr int p=n<200?0:n<300?1:2;
        constexpr int m=n-silu_start(p);
        if constexpr(r==2 && m>=0 && m<64){ constexpr int k=m; x0=__builtin_amdgcn_exp2f(-ga[p&1][k/16][(k/4)%4][k%4]); pin(x0); SB(); }
        if constexpr(r==0 && m>=1 && m<65){ e1=fmaf(x0,c2v,c2v); pin(e1); SB(); }
        if constexpr(r==1 && m>=2 && m<66){ constexpr int k=m-2; float gv=ga[p&1][k/16][(k/4)%4][k%4]*e2; pin(gv); ga[p&1][k/16][(k/4)%4][k%4]=gv; SB(); }
        if constexpr(r==3 && m>=1 && m<65){ e2=__builtin_amdgcn_rcpf(e1); pin(e2); SB(); }
        constexpr int i0=(n-fin_start(p,0))*4+r, i1=(n-fin_start(p,1))*4+r;
        if constexpr(i0>=1&&i0<24){ fin(IC(0),IC(i0-1),IC(p)); SB(); }
        if constexpr(i1>=1&&i1<24){ fin(IC(1),IC(i1-1),IC(p)); SB(); }
      }
    });
  });
  STS(4);
  const int tl[3]={tl0,tl1,tC+w};
  asm volatile("s_nop 7"); SB();
  constexpr int NLATE=2;
  if constexpr(ROLE==0) sfor<2>([&](auto i) AI { pstore(IC(9),i); });
  else sfor<2>([&](auto i) AI { pstore(IC(5),i); });
  constexpr int NT=ROLE==0?3:2;
  if constexpr(ROLE==0) poll_check<TAB<ROLE>.tail_after_pst+NLATE>(pollv,tag,rw,pollvo(7),pollso(7)); else waitvm<TAB<ROLE>.tail_after_pst+NLATE>();
  #pragma unroll
  for(int i=0;i<NT;i++) pin(xv[i]);
  STS(5);
  auto rsw=__builtin_amdgcn_make_buffer_rsrc((void*)ws,0,WS_BYTES,0x00020000);
  auto rso=__builtin_amdgcn_make_buffer_rsrc((void*)out,0,S*D*2,0x00020000);
  const unsigned vr=vrow();
  auto ld_tile=[&](int t,u32x4* pv,unsigned sk) AI {
    #pragma unroll
    for(int k=0;k<8;k++) pv[k]=__builtin_amdgcn_raw_buffer_load_b128(rsw,l16,(WS_P+((k*36+t)*64+rp)*1024)|sk,16); };
  u32x4 selA={0,0,0,0}, selB={0,0,0,0};
  { const unsigned one=((lane>>4)==((lane&15)>>2))? ((lane&1)?0x3f800000u:0x00003f80u) : 0u;
    if(lane&2){ selA[1]=one; selB[3]=one; } else { selA[0]=one; selB[2]=one; } }
  auto sum_tile=[&](u32x4* pv,f32x4& c0,f32x4& c1) AI {
    c0=f32x4{0,0,0,0}; c1=f32x4{0,0,0,0};
    #pragma unroll
    for(int k=0;k<8;k++){ c0=__builtin_amdgcn_mfma_f32_16x16x32_bf16(__builtin_bit_cast(bf16x8,selA),__builtin_bit_cast(bf16x8,pv[k]),c0,0,0,0);
                          c1=__builtin_amdgcn_mfma_f32_16x16x32_bf16(__builtin_bit_cast(bf16x8,selB),__builtin_bit_cast(bf16x8,pv[k]),c1,0,0,0); } };
  auto fin_tile=[&](int t,const f32x4& c0,const f32x4& c1,u32x4 sv,u32x4 xx,unsigned sk) AI {
    float acc[8];
    #pragma unroll
    for(int e=0;e<8;e++){ const unsigned s2=sv[e>>1]; const float d0=e<4?c0[e&3]:c1[e&3];
      acc[e]=((e&1)?bfhi(s2):bflo(s2))*d0*__builtin_amdgcn_rcpf(1.0f+__builtin_amdgcn_exp2f(-LOG2E*d0)); }
    #pragma unroll
    for(int e=0;e<4;e++){ auto r=__builtin_amdgcn_permlane16_swap(__float_as_uint(acc[e]),__float_as_uint(acc[4+e]),false,false); acc[e]=__uint_as_float(r[0]); acc[4+e]=__uint_as_float(r[1]); }
    u32x4 o;
    #pragma unroll
    for(int e=0;e<4;e++) o[e]=pack2(acc[2*e]+bflo(xx[e]),acc[2*e+1]+bfhi(xx[e]));
    __builtin_amdgcn_raw_buffer_store_b128(o,rso,vr,(srow+t*32)|sk,0);
  };
  auto sum_fix=[&](int t,u32x4* pv,f32x4& c0,f32x4& c1) AI {
    sum_tile(pv,c0,c1);
    for(int it=0;it<4096 && __any(c0[0]!=c0[0]);it++){ bool any=false;
      #pragma unroll
      for(int k=0;k<8;k++){ unsigned m=max(max(pv[k][0],pv[k][1]),max(pv[k][2],pv[k][3]));
        if(__any(m==~0u)){ any=true; pv[k]=__builtin_amdgcn_raw_buffer_load_b128(rsw,l16,WS_P+((k*36+t)*64+rp)*1024,16); } }
      if(!any) break;
      asm volatile("":::"memory");
      sum_tile(pv,c0,c1); } };
  u32x4 sv[3];
  u32x4 pv2[8];
  ld_tile(tl[0],pv0,0u);
  ld8_sc1(pollv,rw,pollvo(8),pollso(8));
  ld_tile(tl[1],pv1,skip1);
  if constexpr(ROLE==0) ld_tile(tl[2],pv2,0u);
  poll_check<ROLE==0?16:8>(pollv,tag,rw,pollvo(8),pollso(8));
  #pragma unroll
  for(int i=0;i<NT;i++) sv[i]=__builtin_amdgcn_raw_buffer_load_b128(rsw,l16,(WS_SB+(tl[i]*64+rp)*1024)|(i==1?skip1:0u),16);
  SB();
  f32x4 c0,c1,c2,c3,c4,c5;
  if constexpr(ROLE==1){ sum_fix(tl[0],pv0,c0,c1); sum_fix(tl[1],pv1,c2,c3); }
  else { sum_tile(pv0,c0,c1); sum_tile(pv1,c2,c3); }
  fin_tile(tl[0],c0,c1,sv[0],xv[0],0u);
  fin_tile(tl[1],c2,c3,sv[1],xv[1],skip1);
  STS(6);
  if constexpr(ROLE==0){ sum_fix(tl[2],pv2,c4,c5); fin_tile(tl[2],c4,c5,sv[2],xv[2],0u); }
  if constexpr(ROLE==0){ if(blockIdx.x==0&&tid==0) *(unsigned*)(ws+WS_EP)=e0+1u; }
}

extern "C" __global__ __launch_bounds__(256,1) void k1_main(const u16* __restrict__ x,const u16* __restrict__ wqkv,const u16* __restrict__ wo,const u16* __restrict__ wg,const u16* __restrict__ wu,const u16* __restrict__ wd,u16* __restrict__ out,char* __restrict__ ws
  ){
  u64* dbg=nullptr;
  __shared__ __attribute__((aligned(16))) char smem[6*SLOT+1024];
  const unsigned lds0=(unsigned)(size_t)(lds_vp)smem;
  const int xs=blockIdx.x&7, idx=blockIdx.x>>3, mt=idx&7, j=idx>>3;
  u64 t_start=0;
  const int lane=threadIdx.x&63, w=__builtin_amdgcn_readfirstlane(threadIdx.x>>6);
  const i32x4 rw=mk_rsrc(ws,WS_BYTES), rx=mk_rsrc(x,S*D*2);
  const int l32=lane&31, rr=2*w+(lane>>5), m=(xs&1)*8+rr;
  const unsigned xo=((mt*4+j)*64+4*(m&~1)+(xs&6)+(m&1))*(D*2);
  const bool has3=l32<8;
  u32x4 v[3]; unsigned pv_,ep;
  ld16_plain(v[0],rx,xo+l32*16,0u); ld16_plain(v[1],rx,xo+l32*16,512u); ld16_plain(v[2],rx,has3?xo+l32*16:0x80000000u,1024u);
  ld4_plain(pv_,rx,xo,0u); ld4_plain(ep,rw,0u,WS_EP);
  SB();
  i32x4 rc;
  { unsigned long long pc; int r0_,r1_;
    asm volatile("s_getpc_b64 %0\n\ts_mov_b32 %1, k1_main@rel32@lo+4\n\ts_mov_b32 %2, k_end@rel32@lo+12":"=s"(pc),"=s"(r0_),"=s"(r1_));
    rc=mk_rsrc((const void*)(pc+(long long)r0_), r1_>r0_? (unsigned)(r1_-r0_):0u); }
  touch(rc,(w>=2&&lane<32)?lane*128:0x80000000u,w==3?idx*4096:0u,lds0+6*SLOT);
  {
    const unsigned ldm=lds0+6*SLOT, big=0x80000000u, lw=(w==3)?lane:64;
    const unsigned va=(lw<PROWS&&j<3)? (mt*PROWS+lw)*(F*2) : big;
    touch(mk_rsrc(wg,D*F*2),va,(xs*3+j)*384,ldm); touch(mk_rsrc(wu,D*F*2),va,(xs*3+j)*384,ldm);
    const unsigned vq=lw<64? ((lw>>2)*32+idx)*(3*D*2)+((lw>>1)&1)*1152+(lw&1)*140 : big;
    touch(mk_rsrc(wqkv,3*D*D*2),vq,1152+xs*144,ldm);
  }
  waitvm<4>();
  pin(v[0]); pin(v[1]); pin(v[2]); pin(pv_); pin(ep);
  const unsigned e0=__builtin_amdgcn_readfirstlane(ep);
  {
    const float piv=bflo(pv_);
    float f[24]; float s_=0,q_=0;
    #pragma unroll
    for(int i=0;i<3;i++){ float s1=0,q1=0;
      #pragma unroll
      for(int jj=0;jj<4;jj++){ float a_=bflo(v[i][jj])-piv,b_=bfhi(v[i][jj])-piv; f[i*8+2*jj]=a_; f[i*8+2*jj+1]=b_; s1+=a_+b_; q1=fmaf(a_,a_,fmaf(b_,b_,q1)); }
      if(i<2||has3){ s_+=s1; q_+=q1; } }
    s_=red32(s_); q_=red32(q_);
    const float mean=s_*(1.0f/D), var=fmaxf(q_*(1.0f/D)-mean*mean,0.f);
    auto rb16=[](float t){ return bflo(pack2(t,0.f)); };
    const float mb=rb16(mean+piv), sd=rb16(sqrtf(rb16(rb16(var)+1e-5f)));
    const float sc=LOG2E/sd, of=(piv-mb)*sc;
    typedef __attribute__((address_space(3))) u32x4* lds_u32x4_p;
    const unsigned stg=lds0+5*SLOT;
    #pragma unroll
    for(int i=0;i<3;i++){ u32x4 o;
      #pragma unroll
      for(int jj=0;jj<4;jj++) o[jj]=pack2(fmaf(f[i*8+2*jj],sc,of),fmaf(f[i*8+2*jj+1],sc,of));
      *(lds_u32x4_p)(size_t)(stg+(l32+32*i)*128+rr*16)=o; }
    asm volatile("s_waitcnt lgkmcnt(0)":::"memory");
    __builtin_amdgcn_s_barrier();
    const unsigned vo=(lane>>5)*4096+((lane>>3)&3)*256+(lane&7)*16;
    const unsigned so=w<3? WS_XN+(mt*4+j)*18*4096+(xs>>1)*1024+(xs&1)*128+w*3*8192 : 0x80000000u;
    u32x4 o[3];
    #pragma unroll
    for(int i=0;i<3;i++) o[i]=*(lds_u32x4_p)(size_t)(stg+(w*3+i)*1024+lane*16);
    #pragma unroll
    for(int i=0;i<3;i++) st16_sc1<0>(o[i],rw,vo,so+i*8192);
  }
  {
    const unsigned ldm=lds0+6*SLOT, big=0x80000000u, lw=(w==3)?lane:64;
    const unsigned va=(lw<R2ROWS&&j<3)? (8*PROWS+mt*R2ROWS+lw)*(F*2) : big;
    touch(mk_rsrc(wg,D*F*2),va,(xs*3+j)*384,ldm); touch(mk_rsrc(wu,D*F*2),va,(xs*3+j)*384,ldm);
  }
  if(j<3) body<0>(x,wg,wu,wd,wo,out,ws,lds0,mt,xs,j,e0,rc,t_start,dbg);
  else body<1>(x,wqkv,wo,wd,wo,out,ws,lds0,mt,xs,j,e0,rc,t_start,dbg);
}
extern "C" __global__ void k_end(){}

extern "C" void kernel_launch(void* const* d_in,const int* in_sizes,int n_in,void* d_out,int out_size,void* d_ws,size_t ws_size,hipStream_t stream){
  const u16* x=(const u16*)d_in[0]; const u16* wqkv=(const u16*)d_in[1]; const u16* wo=(const u16*)d_in[2];
  const u16* wg=(const u16*)d_in[4]; const u16* wu=(const u16*)d_in[5]; const u16* wd=(const u16*)d_in[6];
  char* ws=(char*)d_ws;
  k1_main<<<256,256,0,stream>>>(x,wqkv,wo,wg,wu,wd,(u16*)d_out,ws
  );
}
```

```cpp
#define EARLYFLAG
#include <hip/hip_runtime.h>
#include <utility>
typedef unsigned short u16; typedef unsigned long long u64;
typedef __attribute__((ext_vector_type(4))) unsigned u32x4;
typedef __attribute__((ext_vector_type(2))) unsigned u32x2;
typedef __attribute__((ext_vector_type(4))) int i32x4;
typedef __attribute__((ext_vector_type(4))) short s16x4;
typedef __attribute__((ext_vector_type(8))) __bf16 bf16x8;
typedef __attribute__((ext_vector_type(2))) __bf16 bf16x2;
typedef __attribute__((ext_vector_type(4))) float f32x4;
typedef __attribute__((address_space(3))) s16x4* lds_s16x4_p;
typedef __attribute__((address_space(3))) void* lds_vp;
#define DI __device__ __forceinline__
#define SB() __builtin_amdgcn_sched_barrier(0)
#define AI __attribute__((always_inline))
#define IC(x) std::integral_constant<int,(x)>{}
template<class Fn,int... I> DI void sfor_impl(Fn&& f, std::integer_sequence<int,I...>){ (f(std::integral_constant<int,I>{}),...); }
template<int N,class Fn> DI void sfor(Fn&& f){ sfor_impl(f,std::make_integer_sequence<int,N>{}); }

constexpr int S=2048,D=576,F=4608;
constexpr float LOG2E=1.4426950408889634f;
constexpr unsigned WS_EP=0;
constexpr unsigned WS_HF=4096;
constexpr unsigned WS_PF=WS_HF+32768;
constexpr unsigned WS_SF=WS_PF+16384;
constexpr unsigned WS_OF=WS_SF+4096;
constexpr unsigned WS_KF=WS_OF+4096;
constexpr unsigned WS_XF=WS_KF+32768;
constexpr unsigned WS_XN=1u<<20;
constexpr unsigned WS_O =4u<<20;
constexpr unsigned WS_OL=7u<<20;
constexpr unsigned WS_KV=11u<<20;
constexpr unsigned WS_SB=8u<<20;
constexpr unsigned WS_H =16u<<20;
constexpr unsigned WS_P =40u<<20;
constexpr unsigned WS_BYTES=64u<<20;
constexpr int SLOT=24576;

#define NTW ""
#define NTT ""
#define NTK ""
#define K0LINES 1

DI s16x4 trr(unsigned a){ return __builtin_amdgcn_ds_read_tr16_b64_v4i16((lds_s16x4_p)(size_t)a); }
DI bf16x8 cat(s16x4 a,s16x4 b){ return __builtin_bit_cast(bf16x8,__builtin_shufflevector(a,b,0,1,2,3,4,5,6,7)); }
DI unsigned pack2(float a,float b){ bf16x2 v={(__bf16)a,(__bf16)b}; return __builtin_bit_cast(unsigned,v); }
DI float bflo(unsigned v){ return __uint_as_float(v<<16); }
DI float bfhi(unsigned v){ return __uint_as_float(v&0xffff0000u); }
DI i32x4 mk_rsrc(const void* p, unsigned bytes){
  unsigned long long a=(unsigned long long)p; i32x4 r; r[0]=(int)a; r[1]=(int)(a>>32); r[2]=(int)bytes; r[3]=0x00020000; return r;
}
template<unsigned SO,unsigned LO> DI void glds16(i32x4 rsrc, unsigned voff, unsigned sbase, unsigned lbase){
  unsigned t;
  asm volatile("s_add_u32 m0, %4, %6\n\ts_add_u32 %0, %3, %5\n\tbuffer_load_dwordx4 %1, %2, %0 offen" NTW " lds":"=&s"(t):"v"(voff),"s"(rsrc),"s"(sbase),"s"(lbase),"i"(SO),"i"(LO):"memory","scc");
}
DI void touch(i32x4 rsrc, unsigned voff, unsigned soff, unsigned ldummy){
  asm volatile("s_mov_b32 m0, %3\n\ts_nop 0\n\tbuffer_load_dword %0, %1, %2 offen" NTT " lds"::"v"(voff),"s"(rsrc),"s"(soff),"s"(ldummy):"memory");
}
#define CPFA 8192
DI void code_touch(i32x4 rc, unsigned voff, unsigned ahead, unsigned ldummy){
  unsigned long long pc; asm volatile("s_getpc_b64 %0":"=s"(pc));
  touch(rc,voff,(unsigned)pc-(unsigned)rc[0]+ahead,ldummy);
}
template<bool AG,bool SC1,int OFF> DI void ldfrag(bf16x8& d,i32x4 rsrc,unsigned voff,unsigned soff){
  if constexpr(AG){ if constexpr(SC1) asm volatile("buffer_load_dwordx4 %0, %1, %2, %3 offen offset:%4 sc1":"=a"(d):"v"(voff),"s"(rsrc),"s"(soff),"i"(OFF):"memory");
                    else asm volatile("buffer_load_dwordx4 %0, %1, %2, %3 offen offset:%4":"=a"(d):"v"(voff),"s"(rsrc),"s"(soff),"i"(OFF):"memory"); }
  else { if constexpr(SC1) asm volatile("buffer_load_dwordx4 %0, %1, %2, %3 offen offset:%4 sc1":"=v"(d):"v"(voff),"s"(rsrc),"s"(soff),"i"(OFF):"memory");
         else asm volatile("buffer_load_dwordx4 %0, %1, %2, %3 offen offset:%4":"=v"(d):"v"(voff),"s"(rsrc),"s"(soff),"i"(OFF):"memory"); }
}
template<int OFF> DI void st16_sc1(u32x4 v,i32x4 rsrc,unsigned voff,unsigned soff){
  asm volatile("buffer_store_dwordx4 %0, %1, %2, %3 offen offset:%4 sc1\n\ts_nop 1"::"v"(v),"v"(voff),"s"(rsrc),"s"(soff),"i"(OFF):"memory");
}
DI void st8_sc1(u32x2 v,i32x4 rsrc,unsigned voff,unsigned soff){
  asm volatile("buffer_store_dwordx2 %0, %1, %2, %3 offen sc1"::"v"(v),"v"(voff),"s"(rsrc),"s"(soff):"memory");
}
DI void ld8_sc1(u64& d,i32x4 rsrc,unsigned voff,unsigned soff){
  asm volatile("buffer_load_dwordx2 %0, %1, %2, %3 offen sc1":"=v"(d):"v"(voff),"s"(rsrc),"s"(soff):"memory");
}
template<int OFF> DI void ld16_sc1(u32x4& d,i32x4 rsrc,unsigned voff,unsigned soff){
  asm volatile("buffer_load_dwordx4 %0, %1, %2, %3 offen offset:%4 sc1":"=v"(d):"v"(voff),"s"(rsrc),"s"(soff),"i"(OFF):"memory");
}
DI void ld4_plain(unsigned& d,i32x4 rsrc,unsigned voff,unsigned soff){
  asm volatile("buffer_load_dword %0, %1, %2, %3 offen":"=v"(d):"v"(voff),"s"(rsrc),"s"(soff):"memory");
}
DI void ld16_plain(u32x4& d,i32x4 rsrc,unsigned voff,unsigned soff){
  asm volatile("buffer_load_dwordx4 %0, %1, %2, %3 offen":"=v"(d):"v"(voff),"s"(rsrc),"s"(soff):"memory");
}
template<class T> DI void pin(T& v){ asm volatile("":"+v"(v)); }
template<int N> DI void waitvm(){ asm volatile("s_waitcnt vmcnt(%0)"::"i"(N>63?63:N):"memory"); }
template<int N> DI void poll_check(u64& v,u64 tag,i32x4 rsrc,unsigned voff,unsigned soff){
  unsigned cnt;
  asm volatile("s_waitcnt vmcnt(%5)\n\t"
               "s_mov_b32 %1, 0x4000\n"
               "1:\n\t"
               "v_cmp_ne_u64 vcc, %6, %0\n\t"
               "s_nop 0\n\t"
               "s_cbranch_vccz 2f\n\t"
               "s_sleep 1\n\t"
               "buffer_load_dwordx2 %0, %2, %3, %4 offen sc1\n\t"
               "s_sub_u32 %1, %1, 1\n\t"
               "s_waitcnt vmcnt(0)\n\t"
               "s_cbranch_scc0 1b\n"
               "2:"
               :"+v"(v),"=&s"(cnt):"v"(voff),"s"(rsrc),"s"(soff),"i"(N>63?63:N),"s"(tag):"memory","vcc","scc");
}
template<int ROLE,int K,int NK> DI void page_pad(){
  if constexpr(K+4<NK) asm volatile("s_branch 1f\n\t.p2align 12\n.Lpad_%0_%1:\n\ts_branch .Lpad_%0_%2\n1:"::"i"(ROLE),"i"(K),"i"(K+4));
  else asm volatile("s_branch 1f\n\t.p2align 12\n.Lpad_%0_%1:\n\ts_setpc_b64 s[98:99]\n1:"::"i"(ROLE),"i"(K));
}
template<int ROLE> DI void walk_pads(int w){
  asm volatile("s_getpc_b64 s[96:97]\n"
               "2:\n\t"
               "s_mov_b32 s98, .Lpad_%1_0-2b\n\t"
               "s_cmp_eq_u32 %0, 1\n\t"
               "s_cselect_b32 s98, .Lpad_%1_1-2b, s98\n\t"
               "s_cmp_eq_u32 %0, 2\n\t"
               "s_cselect_b32 s98, .Lpad_%1_2-2b, s98\n\t"
               "s_cmp_eq_u32 %0, 3\n\t"
               "s_cselect_b32 s98, .Lpad_%1_3-2b, s98\n\t"
               "s_add_u32 s96, s96, s98\n\t"
               "s_addc_u32 s97, s97, 0\n\t"
               "s_swappc_b64 s[98:99], s[96:97]"
               ::"s"(w),"i"(ROLE):"s96","s97","s98","s99","scc");
}
template<int CTRL> DI float dppadd(float v){ return v+__int_as_float(__builtin_amdgcn_update_dpp(0,__float_as_int(v),CTRL,0xf,0xf,true)); }
DI float red32(float v){
  v=dppadd<0xB1>(v); v=dppadd<0x4E>(v); v=dppadd<0x141>(v); v=dppadd<0x140>(v);
  auto r=__builtin_amdgcn_permlane16_swap(__float_as_uint(v),__float_as_uint(v),false,false);
  return __uint_as_float(r[0])+__uint_as_float(r[1]);
}
template<bool FIRST,bool AG> DI void mma(f32x4& acc,const bf16x8& a,const bf16x8& b){
  if constexpr(FIRST){ if constexpr(AG) asm volatile("v_mfma_f32_16x16x32_bf16 %0, %1, %2, 0":"=&v"(acc):"v"(a),"a"(b)); else asm volatile("v_mfma_f32_16x16x32_bf16 %0, %1, %2, 0":"=&v"(acc):"v"(a),"v"(b)); }
  else { if constexpr(AG) asm volatile("v_mfma_f32_16x16x32_bf16 %0, %1, %2, %0":"+v"(acc):"v"(a),"a"(b)); else asm volatile("v_mfma_f32_16x16x32_bf16 %0, %1, %2, %0":"+v"(acc):"v"(a),"v"(b)); }
}

constexpr int PDF=3, NBUF=4;
enum { K_A=0,K_Q=1,K_W=2,K_B=3,K_C=4 };
struct Stage{ int kind; int idx; int nsteps; };
constexpr int NST0=26, NST1=24;
template<int ROLE> constexpr int nstage(){ return ROLE==0?NST0:NST1; }
template<int ROLE> constexpr Stage stage(int s){
  if(ROLE==0){
    if(s<18) return Stage{K_A,s,24};
    constexpr int G[8][3]={{K_B,0,24},{K_B,1,24},{K_B,2,24},{K_B,3,24},{K_B,4,12},{K_C,0,24},{K_C,1,24},{K_C,2,24}};
    return Stage{G[s-18][0],G[s-18][1],G[s-18][2]};
  } else {
    if(s<14) return Stage{K_Q,s,s==13?10:20};
    if(s<17) return Stage{K_B,s-14,24};
    if(s<22) return Stage{K_W,s-17,s==21?10:20};
    return Stage{K_B,s-19,s==23?12:24};
  }
}
constexpr int a_mat(int s){ return s<6? (s&1) : (s%6)/3; }
constexpr int a_kp(int s){ return s<6? (s>>1) : s%3; }
constexpr int q_sec(int kk){ return kk<36? 1+(kk&1) : 0; }
constexpr int q_ks(int kk){ return kk<36? kk>>1 : kk-36; }
template<int ROLE> constexpr int stage_start(int s){ int n=0; for(int i=0;i<s;i++) n+=stage<ROLE>(i).nsteps; return n; }
template<int ROLE> constexpr int stage_n(int s){ int k=stage<ROLE>(s).kind; return (k==K_Q||k==K_W)?5:6; }
template<int ROLE> constexpr int nstep(){ return stage_start<ROLE>(nstage<ROLE>()); }
struct Frag{ int ph; int ks; int c; bool first; int win; int base; int off; int d1; int bd; int st; };
enum { O_NONE=0,O_GL,O_XL,O_HL,O_OL,O_HST,O_FLAG,O_POLL,O_TOUCH,O_PST,O_SST,O_KVST,O_KVLD,O_POIS,O_PLD,O_CODE };
struct Op{ short t; short i; short w; };
constexpr int NMAX=640;
struct Tab{ Frag f[NMAX]; Op op[NMAX][4]; short bar[NMAX]; short wait[NMAX]; short chk[NMAX]; short chkw[NMAX]; int tail_after_pst; int blkwait; int voB; int baseB[3]; int wopat; int olt; int err; };
constexpr int XPRO=12;
constexpr int silu_start(int p){ return p==0?121:144*p+73; }
constexpr int fin_start(int p,int t){ return p==0? (t==0?156:188) : 144*p+138+6*t; }
#define HFD0 30
#define HFD1 40
#define HFD2 20
#define CPFS 90
#define P2P 24
#define P2C 10
#define PROWS 24
#define SFD 8
#define TMK 16
#define CTM 0
#define TWLIST 212,230,248,112,140,196
#define R2ROWS 48
#define PSTS 2
#define TA0 50
#define TA1 104
#define G6AT 96
#define TA2 126
#define TA3 164
#define PLD0 22
#define PFD0 18
#define HLEAD 28
#define PFA 100
#define PFW 198
#define PFD 204
template<int ROLE> constexpr Tab make_tab(){
  Tab t{};
  constexpr int NS=nstage<ROLE>(), N=nstep<ROLE>();
  int ss[32]={}; { int n=0; for(int s=0;s<=NS;s++){ ss[s]=n; if(s<NS) n+=stage<ROLE>(s).nsteps; } }
  int firstB[3]={-1,-1,-1};
  for(int s=0;s<NS;s++){ const Stage st=stage<ROLE>(s); const int sl=s%6;
    for(int i=0;i<st.nsteps;i++){ Frag f{}; const int n=ss[s]+i;
      if(st.kind==K_A){ int kl=i/4,c=i%4;
        if(s==6){ if(i<18){ kl=i/3; c=i%3; } else { kl=i-18; c=3; } }
        else if(s==12&&i<12){ kl=(i%6)/2; c=(i/6)*2+(i&1); }
        else if((s==11||s==17)&&i>=12){ kl=3+((i-12)%6)/2; c=((i-12)/6)*2+(i&1); }
        f.ph=a_mat(s); f.ks=a_kp(s)*6+kl; f.c=c; f.first=(f.ks==0); f.win=sl/2; f.base=c; f.off=(sl%2)*SLOT+kl*4096; f.d1=2048;
      } else if(st.kind==K_Q){ int kk=4*st.idx+i/5; f.ph=q_sec(kk)==1?3:q_sec(kk)==2?4:5; f.ks=q_ks(kk); f.c=i%5; f.first=(f.ks==0); f.win=sl/2; f.base=0; f.off=(sl%2)*SLOT+(i/5)*5120+f.c*8; f.d1=2560;
      } else if(st.kind==K_W){ int kk=4*st.idx+i/5; f.ph=6; f.ks=(kk+12)%18; f.c=i%5; f.first=(kk==0); f.win=sl/2; f.base=1; f.off=(sl%2)*SLOT+(i/5)*5120+f.c*32; f.d1=2560;
      } else if(st.kind==K_B){ f.ph=2; f.ks=4*st.idx+i/6; f.c=i%6; f.base=f.c&1; f.off=(sl%2)*SLOT+(i/6)*6144+(f.c&~1)*32; f.d1=3072; f.first=(f.ks==0); f.win=sl/2;
        if(firstB[f.win]<0) firstB[f.win]=n;
      } else { const int kl=i/4, c=i%4; f.ph=7; f.ks=6*st.idx+kl; f.c=6+c; f.base=c; f.off=(sl%2)*SLOT+kl*4096; f.d1=2048; f.first=(f.ks==0); f.win=sl/2; }
      f.st=s; t.f[n]=f; } }
  auto tile_major=[&](int a,int b){ for(int i=a+1;i<b;i++){ Frag x=t.f[i]; int k=i-1; while(k>=a && t.f[k].c>x.c){ t.f[k+1]=t.f[k]; k--; } t.f[k+1]=x; } };
  if(ROLE==0){ tile_major(ss[21]+6*(TMK-12),ss[23]); tile_major(ss[23]+CTM,N); } else tile_major(ss[22],N);
  int barstep[32]={}; for(int s=0;s<NS;s++) barstep[s]=N; for(int n=N-1;n>=0;n--) barstep[t.f[n].st]=n-PDF-1;
  int tdone[10]={}; for(int n=0;n<N;n++) if(t.f[n].ph==2||t.f[n].ph==7) tdone[t.f[n].c]=n;
  { int cur[6]={0,0,0,0,0,0}; for(int n=0;n<N;n++){ Frag& f=t.f[n]; if(f.ph==2) f.base+=4; f.bd=(f.win-cur[f.base])*49152; cur[f.base]=f.win; } }
  for(int n=N;n<NMAX;n++) t.f[n]=t.f[N-1];
  for(int n=0;n<NMAX;n++){ t.bar[n]=-1; t.wait[n]=-1; t.chk[n]=-1; t.chkw[n]=0; }
  { int fb=N; for(int w=0;w<3;w++) if(firstB[w]>=0&&firstB[w]<fb) fb=firstB[w]; t.baseB[0]=fb-PDF-2; }
  auto put=[&](int ty,int idx,int n,int maxper)->int{
    constexpr int pref[4]={2,3,1,0};
    for(;;n++){ int used=0; for(int r=0;r<4;r++) used+=t.op[n][r].t!=O_NONE; if(used>=maxper) continue;
      for(int k=0;k<4;k++){ int r=pref[k]; if(t.op[n][r].t==O_NONE){ t.op[n][r]=Op{(short)ty,(short)idx,0}; return n; } } } };
  auto findB=[&](int ks,int c,int from)->int{ for(int n=from;n<N;n++) if(t.f[n].ph==2&&t.f[n].ks==ks&&t.f[n].c==c) return n; return -1; };
  if(ROLE==0) for(int p=0;p<3;p++) for(int tt=0;tt<2;tt++) for(int rb=0;rb<4;rb++){ int i=rb*6+5; int n=fin_start(p,tt)+i/4; t.op[n][i%4]=Op{O_HST,(short)(p*8+tt*4+rb),0}; }
  { int n=0; auto X=[&](int a,int b){ for(int i=a*4;i<b*4;i++) n=put(O_XL,i,n,1); }; auto G=[&](int s){ if(s>=6&&n<ss[s-4]-PDF) n=ss[s-4]-PDF; for(int i=0;i<stage_n<ROLE>(s);i++) n=put(O_GL,s*8+i,n,1); };
    if(ROLE==0){ X(3,8); G(2); X(8,12); G(3); G(4); X(12,15); G(5); X(15,18); G(6); G(7); G(8);
      put(O_TOUCH,0,TA0,2); put(O_TOUCH,1,TA1,2);
      put(O_TOUCH,8,TA2,2); put(O_TOUCH,9,TA3,2);
      }
    else { X(3,4); G(2); X(4,8); G(3); X(8,10); G(4); X(10,12); G(5); X(12,14); G(6); X(14,16); G(7); X(16,18); G(8); } }
  for(int s=9;s<NS;s++){ int b0=ss[s-4]-PDF, len=stage<ROLE>(s-4).nsteps, cnt=stage_n<ROLE>(s); for(int i=0;i<cnt;i++){ int n=put(O_GL,s*8+i,b0+i*len/cnt,1); if(stage<ROLE>(s).kind==K_B&&t.voB==0) t.voB=n; } }
  if(ROLE==1) put(O_TOUCH,1,PFA,2);
  const int b1done=findB(17,5,0)+1;
  const int u2=findB(12,0,0);
  if(ROLE==0){
    constexpr int HFD[3]={HFD0,HFD1,HFD2};
    int nf2=0;
    for(int p=0;p<3;p++){ int n=put(O_FLAG,p,fin_start(p,1)+6+HFD[p],2); if(p<2) put(O_TOUCH,4+p,n+1,2); nf2=n; }
    { constexpr int TW[6]={TWLIST}; for(int i=0;i<6;i++) put(O_TOUCH,10+i,TW[i],2); }
    for(int i=0;i<8;i++) put(O_POIS,12+i,300+4*i,1);
    put(O_POLL,0,376,2); t.chk[392]=0;
    for(int i=0;i<48;i++){ int e=432+6*(i/4)-HLEAD+(i%4); put(O_HL,i,i<16?394+2*i:e,1); }
    for(int i=0;i<8;i++){ int e=u2-HLEAD+i; put(O_HL,48+i,e>nf2+1?e:nf2+1,1); }
    put(O_POLL,1,u2-P2P,2); t.chk[u2-P2C]=1;
    for(int i=8;i<24;i++) put(O_HL,48+i,u2-P2C+1+(i-8),1);
    int lp=0;
    for(int i=0;i<18;i++){ int e=tdone[i/2]+3; if(e<lp+PSTS) e=lp+PSTS; lp=put(O_PST,i,e,1); if(i==11) put(O_FLAG,6,lp+PFD0,2); }
    for(int i=0;i<3;i++) put(O_PLD,i,b1done+8+3*i,1);
    put(O_POLL,7,N-8,3);
  } else {
    for(int i=0;i<18;i++) put(O_KVST,i,182+i,1);
    put(O_FLAG,3,224,2); put(O_POLL,2,236,2); t.chk[250]=2;
    for(int i=0;i<5;i++) put(O_KVLD,i,251+i,2);
    put(O_POLL,3,258,2);
    t.chk[271]=4;
    for(int i=24;i<48;i++) put(O_HL,i,272+(i-24),1);
    put(O_FLAG,5,288,2);
    put(O_POLL,5,308,2); t.chk[322]=5;
    const int w0=ss[17];
    for(int i=0;i<16;i++) put(O_OL,48+i,323+i,1);
    for(int i=0;i<8;i++) put(O_OL,64+i,339+i,1);
    for(int i=0;i<48;i++) put(O_OL,i,348+i,1);
    t.olt=w0+19;
    for(int i=0;i<10;i++) put(O_SST,i,u2+3+i,1);
    put(O_FLAG,4,N-SFD,2);
    for(int i=0;i<12;i++) put(O_POIS,i,120+3*i,1);
    put(O_FLAG,6,232,2);
    put(O_POLL,1,u2-52,2); t.chk[u2-38]=1;
    for(int i=0;i<24;i++) put(O_HL,48+i,u2-37+i,2);
    put(O_POLL,6,u2+2,2); t.chk[u2+20]=6;
    for(int i=0;i<2;i++) put(O_PLD,i,u2-6+3*i,1);
    for(int i=0;i<10;i++) put(O_PST,i,tdone[i/2]+3,1);
  }
  int sGL[32]={},cGL[32]={},sS[18]={},gen[18][4]={},sHST[3]={},sPoll[9]={},sKVST=0,sKVLD=0,sSST=0,sPST=0,sOST=0,sOLL=-1,cOLL=0,sPLD=0;
  bool chkd[9]={};
  for(int i=0;i<9;i++) sPoll[i]=-1;
  int seq=1; sGL[0]=sGL[1]=0; cGL[0]=cGL[1]=stage_n<ROLE>(0);
  for(int i=0;i<XPRO;i++){ sS[i/4]=seq++; gen[i/4][i%4]=1; }
  int need_done=0, nb=1;
  for(int n=0;n<N;n++){
    if(ROLE==1&&n==270){ if(sPoll[3]<sKVLD) t.err=11000; t.blkwait=seq-sPoll[3]-1; sPoll[3]=-1; chkd[3]=true;
      for(int i=0;i<24;i++){ sS[i/4]=seq++; gen[i/4][i%4]=2; }
      sPoll[4]=seq++; seq+=9; sOST=seq-1; }
    int need=-1; const Frag f=t.f[n];
    if(nb<NS && n==barstep[nb]){ t.bar[n]=nb; need=sGL[nb]; if(cGL[nb]!=stage_n<ROLE>(nb)) t.err=1000+n; nb++; }
    if(ROLE==1&&n==t.olt){ if(cOLL!=8) t.err=2000+n; if(sOLL>need) need=sOLL; for(int r=0;r<4;r++){ gen[16][r]=3; gen[17][r]=3; } sS[16]=sS[17]=-1; }
    { const int eg=(f.ph==2||f.ph==7)?2:f.ph==6?3:1; for(int r=0;r<4;r++) if(gen[f.ks][r]!=eg) t.err=3000+n; if(sS[f.ks]>need) need=sS[f.ks]; }
    if(need>need_done){ t.wait[n]=(short)(seq-need-1); need_done=need; }
    if(t.chk[n]>=0){ if(sPoll[t.chk[n]]<0) t.err=4000+n; t.chkw[n]=(short)(seq-sPoll[t.chk[n]]-1); chkd[t.chk[n]]=true; sPoll[t.chk[n]]=-1; }
    for(int r=0;r<4;r++){ Op& o=t.op[n][r]; if(o.t==O_NONE) continue;
      switch(o.t){
        case O_GL: sGL[o.i/8]=seq; cGL[o.i/8]++; if(o.i/8>=6 && nb<=o.i/8-4) t.err=5000+n; break;
        case O_XL: sS[o.i/4]=seq; gen[o.i/4][o.i%4]=1; break;
        case O_HL: { int k=(o.i%72)/4; if(k==f.ks) t.err=6000+n; sS[k]=seq; gen[k][o.i%4]=2;
                     int c=k>=12?((ROLE==0&&k<14)?8:1):(ROLE==0?0:(k<6?3:4)); if(!chkd[c]) t.err=7000+n; } break;
        case O_OL: if(!chkd[5]) t.err=8000+n; if(o.i<64){ int k=o.i/4; if(k==f.ks) t.err=6000+n; sS[k]=seq; gen[k][o.i%4]=3; } else { sOLL=seq; cOLL++; } break;
        case O_HST: sHST[o.i/8]=seq; break; case O_PLD: sPLD=seq; break;
        case O_POLL: for(int i=0;i<8;i++) if(sPoll[i]>=0) t.err=9000+n; sPoll[o.i]=seq; break;
        case O_KVST: sKVST=seq; break; case O_KVLD: if(!chkd[2]) t.err=8500+n; sKVLD=seq; break;
        case O_SST: sSST=seq; break; case O_PST: if(o.i<12) sPST=seq; break; case O_POIS: sPST=seq; break;
        case O_FLAG: { int l=o.i<3?sHST[o.i]:o.i==3?sKVST:o.i==4?sSST:o.i==5?sOST:sPST; o.w=(short)(seq-l-1); if(o.i==2) chkd[8]=true; } break;
        default: break; }
      seq++; }
  }
  if(nb!=NS) t.err=12000+nb;
  t.tail_after_pst=ROLE==0? seq-sPoll[7]-1 : seq-sPLD-1;
  for(int n=N;n<NMAX;n++) for(int r=0;r<4;r++) if(t.op[n][r].t!=O_NONE) t.err=10000+n;
  return t;
}
template<int ROLE> inline constexpr Tab TAB=make_tab<ROLE>();
#define PADS0 2,59,116,161,209,256,300,356,402,444,501,555,608
#define PADS1 3,60,112,171,217,265,1001,1002,280,336,390,440,1000
constexpr int PAD0[]={PADS0}, PAD1[]={PADS1};
template<int ROLE> constexpr int npad(){ return ROLE==0?sizeof(PAD0)/4:sizeof(PAD1)/4; }
template<int ROLE> constexpr int pad_at(int n){ for(int k=0;k<npad<ROLE>();k++) if((ROLE==0?PAD0[k]:PAD1[k])==n) return k; return -1; }
#define PAD(n) do{ if constexpr(pad_at<ROLE>(n)>=0){ page_pad<ROLE,pad_at<ROLE>(n),npad<ROLE>()>(); SB(); } }while(0)
template<int R,int E> struct ErrIs{ static_assert(E==0,"schedule error"); };
template struct ErrIs<0,TAB<0>.err>; template struct ErrIs<1,TAB<1>.err>;

DI void dot2a(float& acc,unsigned a,unsigned b){ asm volatile("v_dot2_f32_bf16 %0, %1, %2, %0":"+v"(acc):"v"(a),"v"(b)); }
DI void dot2i(float& acc,unsigned a,unsigned b,float c){ asm volatile("v_dot2_f32_bf16 %0, %1, %2, %3":"=v"(acc):"v"(a),"v"(b),"v"(c)); }
DI void dot2z(float& acc,unsigned a,unsigned b){ asm volatile("v_dot2_f32_bf16 %0, %1, %2, 0":"=v"(acc):"v"(a),"v"(b)); }
template<int CTRL> DI unsigned dppmov(unsigned old,unsigned src){ return (unsigned)__builtin_amdgcn_update_dpp((int)old,(int)src,CTRL,0xf,0xf,false); }
template<int DL,int BK> DI unsigned shiftc(unsigned cur,unsigned h){
  constexpr int sh=4*BK+(DL==1?2:0);
  unsigned t; if constexpr(sh==0) t=h; else t=(unsigned)__builtin_amdgcn_mov_dpp((int)h,0x100+sh,0xf,0xf,true);
  return dppmov<0x110+2*DL>(t,cur);
}

template<int ROLE>
DI void body(const u16* __restrict__ x,const u16* __restrict__ w0,const u16* __restrict__ w1,const u16* __restrict__ wd,const u16* __restrict__ wo,u16* __restrict__ out,char* __restrict__ ws,
             unsigned lds0,int mt,int xs,int j,unsigned e0,i32x4 rc,u64 t_start,u64* dbg){
  const u64 tag=((u64)(e0*0x9E3779B1u+0x7F4A7C15u)<<32)|(u64)(e0+1u);
  const int tid=threadIdx.x, lane=tid&63, w=__builtin_amdgcn_readfirstlane(tid>>6);
  const i32x4 r0=mk_rsrc(w0,ROLE==0?D*F*2:D*3*D*2), r1=mk_rsrc(w1,ROLE==0?D*F*2:D*D*2), rd=mk_rsrc(wd,D*F*2), rw=mk_rsrc(ws,WS_BYTES);
  const unsigned l16=lane*16;
  const int g=lane>>4, q=(lane&15)>>2, p=lane&3;
  const int gw=mt*4+w;
  const int cg=xs*3+j;
  constexpr int NSTEP=nstep<ROLE>();
  #define STS(i)
  unsigned voA[5];
  unsigned soA[3];
  const int rot=ROLE==0?j:0;
  const int rotx=0;
  if constexpr(ROLE==0){
    voA[0]=(8*((lane>>5)&1)+((lane>>3)&3))*(F*2)+(((lane&7)^(((lane>>4)&3)<<1))*16);
    #pragma unroll
    for(int k=0;k<3;k++){ int kp=k+rotx; kp=kp>=3?kp-3:kp; soA[k]=(kp*192+16*(w&1)+4*(w>>1))*(F*2)+cg*3*128; }
  } else {
    #pragma unroll
    for(int i=0;i<5;i++){ int idx=64*i+lane,rho=idx/10,pc=idx%10; int k=8*((rho>>2)&3)+4*(rho>>4)+(rho&3); voA[i]=k*(3*D*2)+(pc>8?8:pc)*16; }
    soA[0]=xs*144;
  }
  auto wo_pat=[&](int i,int kind) AI -> unsigned { unsigned ln=lane; pin(ln);
    int idx=64*i+ln,rho=idx/10,pc=idx-rho*10; int gg=(rho>>2)&3,jj=4*(rho>>4)+(rho&3);
    int slot=kind? 16+(jj&1) : 8*(w&1)+jj; int od=gg&1; int feat=od?(slot<2?16+slot:slot-2):slot;
    int row=18*gg+feat+(kind?72*(jj>>1):0); return row*(D*2)+pc*16; };
  const int gW=xs*4+w, tW=9*(mt>>1)+4*(mt&1);
  const unsigned soW=tW*32;
  unsigned vo1[3],voC;
  auto set_voB=[&]() AI { unsigned ln=lane; pin(ln);
    #pragma unroll
    for(int t=0;t<3;t++){ int idx=64*t+ln; int row=idx/12,pc=idx-row*12; vo1[t]=row*(D*2)+((pc^(((row>>2)&1)<<1))*16); pin(vo1[t]); }
    if constexpr(ROLE==0){ voC=(ln>>3)*(D*2)+(((ln&7)^(((ln>>4)&3)<<1))*16); pin(voC); } };
  const int tB=(0x1e180a04u>>(8*j))&255, tC=(0x00141000u>>(8*j))&255;
  const unsigned soB=(xs*576)*(D*2)+tB*32;
  unsigned soC[3],soH[3];
  #pragma unroll
  for(int k=0;k<3;k++){ int jp=k+rot; jp=jp>=3?jp-3:jp; soC[k]=(xs*576+jp*192+8*w)*(D*2)+tC*32; soH[k]=WS_H+(gw*144+xs*18+jp*6)*4096; }
  const unsigned lA=lds0+w*(ROLE==0?1024:5120), lB=lds0+w*6144;
  const unsigned w8=8*w;
  auto do_glds=[&](auto stc,auto ic) AI {
    constexpr int s_=decltype(stc)::value,i=decltype(ic)::value; constexpr Stage st=stage<ROLE>(s_); constexpr int sl=s_%6;
    if constexpr(st.kind==K_A){
      constexpr int pr=s_/6,mat=a_mat(s_),kp=a_kp(s_);
      glds16<pr*128+i*32*(F*2),sl*SLOT+i*4096>(mat?r1:r0,voA[0],soA[kp],lA);
    } else if constexpr(st.kind==K_Q){
      constexpr auto qoff=[](int kk){ if(kk>53) kk=53; return q_ks(kk)*3+q_sec(kk); };
      constexpr int b=4*st.idx;
      constexpr unsigned KP=qoff(b)|(qoff(b+1)<<8)|(qoff(b+2)<<16)|(qoff(b+3)<<24);
      const unsigned u=(KP>>w8)&255u; const unsigned ks=(u*171u)>>9, sec=u-3*ks;
      glds16<0,sl*SLOT+i*1024>(r0,voA[i],soA[0]+(ks*96+sec)*1152,lA);
    } else if constexpr(st.kind==K_W){
      constexpr auto woff=[](int kk){ if(kk>17) kk=17; return kk<4? kk>>1 : kk<6? (kk-4)*4 : (kk-2)>>1; };
      constexpr int b=4*st.idx;
      constexpr unsigned KP=woff(b)|(woff(b+1)<<8)|(woff(b+2)<<16)|(woff(b+3)<<24);
      unsigned u=(KP>>w8)&255u;
      if constexpr(st.idx==0){ voA[i]=wo_pat(i,0); pin(voA[i]); }
      unsigned vo=voA[i];
      if constexpr(st.idx==1){ const unsigned msk=(w<2)?0u:~0u; u=(u+(mt&msk))&7u; unsigned pl=wo_pat(i,1); vo=(w<2)?pl:vo; } else u=(u+mt)&7u;
      glds16<0,sl*SLOT+i*1024>(r1,vo,soW+u*(72*D*2),lA);
    } else if constexpr(st.kind==K_B){
      constexpr auto rowb=[](int kk){ if(kk>17) kk=17; int l=kk/2; return (l%3)*6+(l/3)*2+(kk&1); };
      constexpr int b=4*st.idx;
      constexpr unsigned KP=rowb(b)|(rowb(b+1)<<8)|(rowb(b+2)<<16)|(rowb(b+3)<<24);
      unsigned rsel=((KP>>w8)&255u)+rot*6; rsel=rsel>=18?rsel-18:rsel;
      glds16<(i/3)*16*(D*2),sl*SLOT+i*1024>(rd,vo1[i%3],soB+rsel*(32*D*2),lB);
    } else {
      glds16<(st.idx*64+(i&1)*32)*(D*2),sl*SLOT+i*4096>(rd,voC,soC[i/2],lA);
    }
  };
  unsigned soX[3];
  #pragma unroll
  for(int k=0;k<3;k++){ int kp=k+rotx; kp=kp>=3?kp-3:kp; soX[k]=WS_XN+(gw*18+kp*6)*4096; }
  bf16x8 xr[18][4];
  auto ld_x=[&](auto ic) AI { constexpr int i=decltype(ic)::value,ks=i/4,r=i%4; ldfrag<(ks<16),true,r*1024>(xr[ks][r],rw,l16,soX[ks/6]+(ks%6)*4096); };
  auto ld_o=[&](auto ic) AI { constexpr int i=decltype(ic)::value,sl=i/4,r=i%4; constexpr int ks=sl>=12?sl-12:sl+4; ldfrag<true,true,r*1024>(xr[sl][r],rw,l16,WS_O+(gW*18+((ks+2*mt)&15))*4096); };
  auto ld_h=[&](auto ic) AI { constexpr int i=decltype(ic)::value%72,kk=i/4,r=i%4; constexpr int l=kk/2;
    ldfrag<(kk<16),true,r*1024>(xr[kk][r],rw,l16,soH[l%3]+((l/3)*2+(kk&1))*4096); };
  const unsigned hf_line=WS_HF+((mt*8+xs)*4+w)*128;
  auto put_flag=[&](unsigned so) AI { unsigned ln=lane; pin(ln); u32x2 tv={(unsigned)tag,(unsigned)(tag>>32)}; pin(tv); st8_sc1(tv,rw,ln==0?0u:0x40000000u,so); };
  u64 pollv=0;
  constexpr int G0=stage_n<ROLE>(0);
  sfor<G0>([&](auto i) AI { do_glds(IC(0),i); });
  sfor<G0>([&](auto i) AI { do_glds(IC(1),i); });
  walk_pads<ROLE>(w);
  { const unsigned vp=(lane%24)*8;
    ld8_sc1(pollv,rw,vp,WS_XF+gw*256); poll_check<0>(pollv,tag,rw,vp,WS_XF+gw*256); }
  sfor<XPRO>([&](auto i) AI { ld_x(i); });
  unsigned bw[6];
  if constexpr(ROLE==0){ const int row=4*g+q; unsigned b0=lds0+row*128+32*((row>>1)&3)+8*p;
    #pragma unroll
    for(int c=0;c<4;c++){ bw[c]=b0^(c*32); pin(bw[c]); } }
  else { bw[0]=lds0+(4*g+q)*160+36*p-4*(p&1); bw[1]=lds0+(4*g+q)*160+8*p; pin(bw[0]); pin(bw[1]); }
  auto set_bases_B=[&]() AI { unsigned ln=lane; pin(ln); const int g2=ln>>4,row=4*g2+((ln&15)>>2),p2=ln&3;
    bw[4]=lds0+row*192+8*p2+(g2&1)*32; bw[5]=bw[4]+(((g2&1)^1)-(g2&1))*32; pin(bw[4]); pin(bw[5]); };
  float c2v=LOG2E*LOG2E; pin(c2v);
  __builtin_amdgcn_s_barrier();
  STS(1);
  f32x4 ga[2][5][4],ua[5][4],da[10][4];
  bf16x8 a[NBUF];
  auto faddr=[&](auto nc) AI -> unsigned { constexpr Frag f=TAB<ROLE>.f[decltype(nc)::value]; return bw[f.base]+f.off; };
  sfor<PDF>([&](auto ic) AI { constexpr int i=decltype(ic)::value; constexpr Frag f=TAB<ROLE>.f[i]; unsigned ad=faddr(ic); a[i]=cat(trr(ad),trr(ad+f.d1)); });
  float x0=0,e1=0,e2=0; u32x4 hv;
  SB();
  auto fin=[&](auto tc,auto icn,auto prc) AI {
    constexpr int t=decltype(tc)::value, i=decltype(icn)::value, pr=decltype(prc)::value; constexpr int rb=i/6, jj=i%6;
    if constexpr(jj<4){ constexpr int cc=2*t+(jj>>1), e=(jj&1)*2; unsigned v=pack2(ga[pr&1][cc][rb][e]*ua[cc][rb][e],ga[pr&1][cc][rb][e+1]*ua[cc][rb][e+1]); pin(v); hv[jj]=v; }
  };
  auto pk8=[&](const f32x4& u0,const f32x4& u1) AI -> u32x4 { u32x4 v; v[0]=pack2(u0[0],u0[1]); v[1]=pack2(u0[2],u0[3]); v[2]=pack2(u1[0],u1[1]); v[3]=pack2(u1[2],u1[3]); return v; };
  const unsigned pbase=WS_P+(xs*36*64+gw*2)*1024;
  auto pstore=[&](auto tc,auto hc) AI { constexpr int t=decltype(tc)::value,hh=decltype(hc)::value; st16_sc1<hh*1024>(pk8(da[t][2*hh],da[t][2*hh+1]),rw,l16,pbase+(t<6?tB+t:tC+t-6)*65536); };
  const unsigned pf_w=WS_PF+((mt*4+j)*4+w)*128+xs*8;
  u32x4 kq[9],vq[9],hk[5],OL[8];
  const bool odd=g&1;
  const unsigned kf_own=WS_KF+(xs*32+gw)*128, kf_prev=WS_KF+(xs*32+(gw>0?gw-1:0))*128;
  const unsigned kv_prev=gw>0?WS_KV+(xs*32+gw-1)*5120:0x80000000u;

  const int gc=mt*4+(xs>>1), rp=mt*8+xs;
  const unsigned pf_r=WS_PF+((mt*4+j)*4+(xs>>1))*128;
  const int tl0=tB+w, tl1=tB+(int)((0x03020504u>>w8)&255u);
  u32x4 xv[3],pv0[8],pv1[8]; bool early=false; unsigned pld_base=0;
  const unsigned skip1=(w<2)?0u:0x80000000u;
  auto vrow=[&]() AI -> unsigned { unsigned ln=lane; pin(ln); return (4*(ln&14)+(ln&1)+2*((ln>>4)&1))*(D*2)+(ln>>5)*16; };
  const unsigned srow=(gc*64+4*(xs&1))*(D*2);
  auto pollvo=[&](int id) AI -> unsigned { unsigned ln=lane; pin(ln);
    return id==0? ((ln%6)/2*3+(ln%6)%2)*8 : id==1? ((ln%3)*3+2)*8 : id==2? 0u : id==3? (ln%3)*24 : id==4? ((ln%3)*3+1)*8 : (ln&7)*8; };
  auto pollso=[&](int id) AI -> unsigned { return id==2? kf_prev : id==5? WS_OF+gW*128 : id==6? pf_r : id==7? pf_r : id==8? WS_SF+gc*128 : hf_line; };
  sfor<NSTEP>([&](auto nc) AI {
    constexpr int n=decltype(nc)::value;
    constexpr Frag f=TAB<ROLE>.f[n];
    if constexpr(ROLE==1 && n==180){
      asm volatile("s_nop 7"); SB();
      #pragma unroll
      for(int r=0;r<4;r++){
        #pragma unroll
        for(int pi=0;pi<9;pi++) kq[pi][r]=pack2(ga[0][pi/2][r][(pi&1)*2],ga[0][pi/2][r][(pi&1)*2+1]);
        unsigned t9=pack2(ga[0][4][r][2],ga[0][4][r][3]), t0=kq[0][r]; pin(t9); pin(t0); kq[0][r]=odd?t9:t0;
      }
      #pragma unroll
      for(int pi=0;pi<9;pi++) pin(kq[pi]);
      #pragma unroll
      for(int f2=0;f2<9;f2++)
        #pragma unroll
        for(int e2_=0;e2_<2;e2_++){ const int pos=2*f2+e2_;
          unsigned A=pack2(ua[pos/4][2][pos%4],ua[pos/4][3][pos%4]), B=pack2(ua[pos/4][0][pos%4],ua[pos/4][1][pos%4]);
          if(f2==0){ unsigned A9=pack2(ua[4][2][2+e2_],ua[4][3][2+e2_]), B9=pack2(ua[4][0][2+e2_],ua[4][1][2+e2_]); pin(A); pin(B); pin(A9); pin(B9); A=odd?A9:A; B=odd?B9:B; }
          vq[f2][2*e2_]=A; vq[f2][2*e2_+1]=B; }
      #pragma unroll
      for(int pi=0;pi<9;pi++) pin(vq[pi]);
      SB();
    }
    if constexpr(ROLE==1 && n==270){
      STS(2);
      poll_check<TAB<ROLE>.blkwait>(pollv,tag,rw,pollvo(3),hf_line);
      asm volatile("s_nop 7"); pin(hk[0]); pin(hk[1]); pin(hk[2]); pin(hk[3]); pin(hk[4]); SB();
      unsigned qp[4][9];
      #pragma unroll
      for(int r=0;r<4;r++){
        #pragma unroll
        for(int pi=0;pi<9;pi++){ qp[r][pi]=pack2(ga[0][pi/2][r][(pi&1)*2],ga[0][pi/2][r][(pi&1)*2+1]); pin(qp[r][pi]); }
        unsigned t9=pack2(ga[0][4][r][2],ga[0][4][r][3]); pin(t9); qp[r][0]=odd?t9:qp[r][0];
      }
      const int am=(lane&15)>>1;
      const float b1=(gw==0&&am<1)?-1e30f:0.f, b2=(gw==0&&am<2)?-1e30f:0.f;
      float lg[4][8];
      sfor<9>([&](auto pc) AI { constexpr int pi=decltype(pc)::value;
        if constexpr(pi==7) PAD(1001);
        if constexpr(pi<8){ ld_h(IC(3*pi)); ld_h(IC(3*pi+1)); ld_h(IC(3*pi+2)); }
        unsigned ke[12];
        #pragma unroll
        for(int r=0;r<4;r++){ unsigned c=kq[pi][r], h=hk[pi/4][r]; ke[8+r]=c; ke[4+r]=shiftc<1,pi%4>(c,h); if(r>0) ke[r]=shiftc<2,pi%4>(c,h); }
        #pragma unroll
        for(int ww=0;ww<8;ww++)
          #pragma unroll
          for(int r=3;r>=0;r--){
            if(pi>0) dot2a(lg[r][ww],ke[8+r-ww],qp[r][pi]);
            else if(ww<=r) dot2z(lg[r][ww],ke[8+r-ww],qp[r][pi]);
            else dot2i(lg[r][ww],ke[8+r-ww],qp[r][pi],ww-r<=4?b1:b2);
          }
      });
      asm volatile("s_nop 2"); SB();
      const float csc=0.23570226039551584f/LOG2E;
      unsigned pp[4][5];
      #pragma unroll
      for(int r=0;r<4;r++){
        float mx=__builtin_fmaxf(__builtin_fmaxf(__builtin_fmaxf(lg[r][0],lg[r][1]),__builtin_fmaxf(lg[r][2],lg[r][3])),__builtin_fmaxf(__builtin_fmaxf(lg[r][4],lg[r][5]),__builtin_fmaxf(lg[r][6],lg[r][7])));
        const float mo=-mx*csc; float sum=0;
        #pragma unroll
        for(int ww=0;ww<8;ww++){ lg[r][ww]=__builtin_amdgcn_exp2f(fmaf(lg[r][ww],csc,mo)); sum+=lg[r][ww]; }
        const float ri=__builtin_amdgcn_rcpf(sum*LOG2E);
        #pragma unroll
        for(int ww=0;ww<8;ww++) lg[r][ww]*=ri;
        if(r&1){
          #pragma unroll
          for(int t=0;t<4;t++) pp[r][t]=pack2(lg[r][2*t+1],lg[r][2*t]);
        } else {
          pp[r][0]=pack2(lg[r][0],0.f);
          #pragma unroll
          for(int t=1;t<4;t++) pp[r][t]=pack2(lg[r][2*t],lg[r][2*t-1]);
          pp[r][4]=pack2(0.f,lg[r][7]);
        }
      }
      SB();
      unsigned op[4][9];
      float o2[2][4][2];
      sfor<10>([&](auto fc) AI { constexpr int f2=decltype(fc)::value; constexpr int si=9+f2;
        if constexpr(f2==4) PAD(1002);
        if constexpr(f2==8) PAD(1003);
        if constexpr(f2==5) ld8_sc1(pollv,rw,pollvo(4),hf_line);
        if constexpr(f2<9){
          unsigned pr[2][6];
          #pragma unroll
          for(int e2_=0;e2_<2;e2_++){
            unsigned A=vq[f2][2*e2_],B=vq[f2][2*e2_+1],hA=hk[si/4][2*e2_],hB=hk[si/4][2*e2_+1];
            pr[e2_][0]=A; pr[e2_][1]=B; pr[e2_][2]=shiftc<1,si%4>(A,hA); pr[e2_][3]=shiftc<1,si%4>(B,hB); pr[e2_][4]=shiftc<2,si%4>(A,hA); pr[e2_][5]=shiftc<2,si%4>(B,hB); }
          #pragma unroll
          for(int t=0;t<5;t++)
            #pragma unroll
            for(int r=3;r>=0;r--)
              #pragma unroll
              for(int e2_=0;e2_<2;e2_++){ const int s0=(r>=2)?0:1;
                if(t==0) dot2z(o2[f2&1][r][e2_],pr[e2_][s0],pp[r][0]);
                else if(t<4||!(r&1)) dot2a(o2[f2&1][r][e2_],pr[e2_][s0+t],pp[r][t]); }
        } else asm volatile("s_nop 2");
        SB();
        if constexpr(f2>0){
          #pragma unroll
          for(int r=0;r<4;r++){ op[r][f2-1]=pack2(o2[(f2-1)&1][r][0],o2[(f2-1)&1][r][1]); pin(op[r][f2-1]); }
        }
      });
      sfor<8>([&](auto ic) AI { constexpr int r=decltype(ic)::value/2,s2=decltype(ic)::value%2;
        u32x4 v={op[r][4*s2],op[r][4*s2+1],op[r][4*s2+2],op[r][4*s2+3]}; st16_sc1<r*1024>(v,rw,l16,WS_O+(gw*18+2*xs+s2)*4096); });
      { u32x4 v={op[0][8],op[1][8],op[2][8],op[3][8]}; st16_sc1<0>(v,rw,l16,WS_OL+(gw*8+xs)*1024); }
      STS(3);
      SB();
    }
    PAD(n);
    if constexpr(TAB<ROLE>.wait[n]>=0){ waitvm<TAB<ROLE>.wait[n]>(); SB(); }
    if constexpr(TAB<ROLE>.bar[n]>=0){ __builtin_amdgcn_s_barrier(); SB(); }
    if constexpr(ROLE==1 && n==TAB<ROLE>.olt){
      #pragma unroll
      for(int hh=0;hh<8;hh++) pin(OL[hh]);
      #pragma unroll
      for(int aa=0;aa<2;aa++)
        #pragma unroll
        for(int r=0;r<4;r++){ u32x4 v={OL[4*aa][r],OL[4*aa+1][r],OL[4*aa+2][r],OL[4*aa+3][r]}; pin(v); xr[16+aa][r]=__builtin_bit_cast(bf16x8,v); }
      SB();
    }
    if constexpr(ROLE==0&&TAB<ROLE>.chk[n]==7){ waitvm<TAB<ROLE>.chkw[n]>(); pin(pollv); early=__builtin_amdgcn_ballot_w64(pollv!=tag)==0; pld_base=early?WS_P+rp*1024:0x80000000u; SB(); }
    else if constexpr(TAB<ROLE>.chk[n]>=0){ poll_check<TAB<ROLE>.chkw[n]>(pollv,tag,rw,pollvo(TAB<ROLE>.chk[n]),pollso(TAB<ROLE>.chk[n])); SB(); }
    if constexpr(n==TAB<ROLE>.voB){ set_voB(); SB(); }
    if constexpr(n==TAB<ROLE>.baseB[0]){ set_bases_B(); SB(); }
    constexpr int nn=n+PDF; constexpr Frag fn=TAB<ROLE>.f[nn];
    s16x4 lo;
    sfor<4>([&](auto rc) AI {
      constexpr int r=decltype(rc)::value;
      if constexpr(f.ph==1||f.ph==4) mma<f.first,(f.ks<16)>(ua[f.c][r],a[n%NBUF],xr[f.ks][r]);
      else if constexpr(f.ph==2||f.ph==7) mma<f.first,(f.ks<16)>(da[f.c][r],a[n%NBUF],xr[f.ks][r]);
      else mma<f.first,(f.ks<16)>(ga[f.ph==0?(n/144)&1:0][f.c][r],a[n%NBUF],xr[f.ks][r]);
      SB();
      if constexpr(nn<NSTEP){
        if constexpr(r==0){ if constexpr(fn.bd!=0){ bw[fn.base]+=fn.bd; pin(bw[fn.base]); } lo=trr(faddr(IC(nn))); SB(); }
        if constexpr(r==1){ a[nn%NBUF]=cat(lo,trr(faddr(IC(nn))+fn.d1)); SB(); }
      }
      constexpr Op o=TAB<ROLE>.op[n][r]; constexpr int oi=o.i;
      if constexpr(o.t==O_GL){ do_glds(IC(oi/8),IC(oi%8)); SB(); }
      if constexpr(o.t==O_XL){ ld_x(IC(oi)); SB(); }
      if constexpr(o.t==O_HL){ ld_h(IC(oi)); SB(); }
      if constexpr(o.t==O_OL){ if constexpr(oi<64) ld_o(IC(oi)); else ld16_sc1<(oi&3)*1024>(OL[oi&7],rw,l16,WS_OL+(gW*8+(oi&4))*1024); SB(); }
      if constexpr(o.t==O_HST){ st16_sc1<(oi%4)*1024>(hv,rw,l16,WS_H+(gw*144+(cg*3+oi/8)*2+(oi/4)%2)*4096); SB(); }
      if constexpr(o.t==O_PST){ pstore(IC(oi/2),IC(oi%2)); SB(); }
      if constexpr(o.t==O_PLD && oi>=8){ ld16_sc1<0>(oi<16?pv0[oi&7]:pv1[oi&7],rw,l16,(pld_base+((oi&7)*36+(oi<16?tl0:tl1))*65536)|(oi<16?0u:skip1)); SB(); }
      if constexpr(o.t==O_PLD && oi<8){ ld16_plain(xv[oi],mk_rsrc(x,S*D*2),vrow(),srow+(oi==0?tl0:oi==1?tl1:tC+w)*32); SB(); }
      if constexpr(o.t==O_CODE){ unsigned ln=lane; pin(ln); code_touch(rc,ln<16?ln*128:0x80000000u,w*2048+CPFA,lds0+6*SLOT); SB(); }
      if constexpr(o.t==O_POIS){ constexpr int t=oi/2,hh=oi%2; unsigned m1=~0u; pin(m1); u32x4 v={m1,m1,m1,m1}; st16_sc1<hh*1024>(v,rw,l16,pbase+(t<6?tB+t:tC+t-6)*65536); SB(); }
      if constexpr(o.t==O_SST){ constexpr int t=oi/2,hh=oi%2; unsigned so=WS_SB+((tW+t)*64+gW*2)*1024; if constexpr(t==0) so|=(unsigned)(mt&1)<<31;
          st16_sc1<hh*1024>(pk8(ga[0][t][2*hh],ga[0][t][2*hh+1]),rw,l16,so); SB(); }
      if constexpr(o.t==O_KVST){ const unsigned vk=((lane&15)>=12)?(g*4+(lane&3))*16u:0x80000000u;
          st16_sc1<(oi%16)*256>(oi<9?kq[oi%9]:vq[oi%9],rw,vk,WS_KV+(xs*32+gw)*5120+(oi/16)*4096); SB(); }
      if constexpr(o.t==O_KVLD){ const unsigned vk=q*256+(g*4+p)*16; ld16_sc1<0>(hk[oi],rw,vk,kv_prev+oi*1024); SB(); }
      if constexpr(o.t==O_TOUCH){
          unsigned ln=lane; pin(ln); const unsigned ldm=lds0+6*SLOT;
          if constexpr(oi>=10 && oi<13) touch(mk_rsrc(wo,D*D*2),ln<18?ln*128:0x80000000u,((j*8+mt)*4+w)*6912+(oi-10)*2304,ldm);
          else if constexpr(oi>=13) touch(rd,ln<18?ln*128:0x80000000u,xs*576*(D*2)+((j*8+mt)*4+w)*6912+(oi-13)*2304,ldm);
          else if constexpr((oi<2||oi>=8) && ROLE==0) touch((oi&1)?r1:r0,ln<18?ln*(F*2):0x80000000u,gw*18*(F*2)+(cg*3+1+oi/8)*128,ldm);
          else if constexpr(oi<2) touch(r0,ln<36?(ln>>1)*(3*D*2)+(ln&1)*140:0x80000000u,gw*18*(3*D*2)+(oi?0:2)*1152+xs*144,ldm);
          else if constexpr(oi==2) touch(mk_rsrc(wo,D*D*2),ln<54?ln*128:0x80000000u,((j*8+mt)*4+w)*6912,ldm);
          else if constexpr(oi==3) touch(rd,ln<54?ln*128:0x80000000u,xs*576*(D*2)+((j*8+mt)*4+w)*6912,ldm);
          else touch(rw,ln*128,WS_H+(gw*144+(cg*3+(oi-4))*2)*4096,ldm);
          SB(); }
      if constexpr(o.t==O_POLL){ ld8_sc1(pollv,rw,pollvo(oi),pollso(oi)); SB(); }
      if constexpr(o.t==O_FLAG){ waitvm<o.w>();
          put_flag(oi<3? hf_line+(j*3+oi)*8 : oi==3? kf_own : oi==4? WS_SF+gW*128+mt*8 : oi==5? WS_OF+gw*128+xs*8 : pf_w); SB(); }
      if constexpr(ROLE==0){
        constexpr int p=n<200?0:n<300?1:2;
        constexpr int m=n-silu_start(p);
        if constexpr(r==2 && m>=0 && m<64){ constexpr int k=m; x0=__builtin_amdgcn_exp2f(-ga[p&1][k/16][(k/4)%4][k%4]); pin(x0); SB(); }
        if constexpr(r==0 && m>=1 && m<65){ e1=fmaf(x0,c2v,c2v); pin(e1); SB(); }
        if constexpr(r==1 && m>=2 && m<66){ constexpr int k=m-2; float gv=ga[p&1][k/16][(k/4)%4][k%4]*e2; pin(gv); ga[p&1][k/16][(k/4)%4][k%4]=gv; SB(); }
        if constexpr(r==3 && m>=1 && m<65){ e2=__builtin_amdgcn_rcpf(e1); pin(e2); SB(); }
        constexpr int i0=(n-fin_start(p,0))*4+r, i1=(n-fin_start(p,1))*4+r;
        if constexpr(i0>=1&&i0<24){ fin(IC(0),IC(i0-1),IC(p)); SB(); }
        if constexpr(i1>=1&&i1<24){ fin(IC(1),IC(i1-1),IC(p)); SB(); }
      }
    });
  });
  STS(4);
  const int tl[3]={tl0,tl1,tC+w};
  asm volatile("s_nop 7"); SB();
  constexpr int NLATE=2;
  if constexpr(ROLE==0) sfor<2>([&](auto i) AI { pstore(IC(9),i); });
  else sfor<2>([&](auto i) AI { pstore(IC(5),i); });
  constexpr int NT=ROLE==0?3:2;
  if constexpr(ROLE==0) poll_check<TAB<ROLE>.tail_after_pst+NLATE>(pollv,tag,rw,pollvo(7),pollso(7)); else waitvm<TAB<ROLE>.tail_after_pst+NLATE>();
  #pragma unroll
  for(int i=0;i<NT;i++) pin(xv[i]);
  STS(5);
  auto rsw=__builtin_amdgcn_make_buffer_rsrc((void*)ws,0,WS_BYTES,0x00020000);
  auto rso=__builtin_amdgcn_make_buffer_rsrc((void*)out,0,S*D*2,0x00020000);
  const unsigned vr=vrow();
  auto ld_tile=[&](int t,u32x4* pv,unsigned sk) AI {
    #pragma unroll
    for(int k=0;k<8;k++) pv[k]=__builtin_amdgcn_raw_buffer_load_b128(rsw,l16,(WS_P+((k*36+t)*64+rp)*1024)|sk,16); };
  u32x4 selA={0,0,0,0}, selB={0,0,0,0};
  { const unsigned one=((lane>>4)==((lane&15)>>2))? ((lane&1)?0x3f800000u:0x00003f80u) : 0u;
    if(lane&2){ selA[1]=one; selB[3]=one; } else { selA[0]=one; selB[2]=one; } }
  auto sum_tile=[&](u32x4* pv,f32x4& c0,f32x4& c1) AI {
    c0=f32x4{0,0,0,0}; c1=f32x4{0,0,0,0};
    #pragma unroll
    for(int k=0;k<8;k++){ c0=__builtin_amdgcn_mfma_f32_16x16x32_bf16(__builtin_bit_cast(bf16x8,selA),__builtin_bit_cast(bf16x8,pv[k]),c0,0,0,0);
                          c1=__builtin_amdgcn_mfma_f32_16x16x32_bf16(__builtin_bit_cast(bf16x8,selB),__builtin_bit_cast(bf16x8,pv[k]),c1,0,0,0); } };
  auto fin_tile=[&](int t,const f32x4& c0,const f32x4& c1,u32x4 sv,u32x4 xx,unsigned sk) AI {
    float acc[8];
    #pragma unroll
    for(int e=0;e<8;e++){ const unsigned s2=sv[e>>1]; const float d0=e<4?c0[e&3]:c1[e&3];
      acc[e]=((e&1)?bfhi(s2):bflo(s2))*d0*__builtin_amdgcn_rcpf(1.0f+__builtin_amdgcn_exp2f(-LOG2E*d0)); }
    #pragma unroll
    for(int e=0;e<4;e++){ auto r=__builtin_amdgcn_permlane16_swap(__float_as_uint(acc[e]),__float_as_uint(acc[4+e]),false,false); acc[e]=__uint_as_float(r[0]); acc[4+e]=__uint_as_float(r[1]); }
    u32x4 o;
    #pragma unroll
    for(int e=0;e<4;e++) o[e]=pack2(acc[2*e]+bflo(xx[e]),acc[2*e+1]+bfhi(xx[e]));
    __builtin_amdgcn_raw_buffer_store_b128(o,rso,vr,(srow+t*32)|sk,0);
  };
  auto sum_fix=[&](int t,u32x4* pv,f32x4& c0,f32x4& c1) AI {
    sum_tile(pv,c0,c1);
    for(int it=0;it<4096 && __any(c0[0]!=c0[0]);it++){ bool any=false;
      #pragma unroll
      for(int k=0;k<8;k++){ unsigned m=max(max(pv[k][0],pv[k][1]),max(pv[k][2],pv[k][3]));
        if(__any(m==~0u)){ any=true; pv[k]=__builtin_amdgcn_raw_buffer_load_b128(rsw,l16,WS_P+((k*36+t)*64+rp)*1024,16); } }
      if(!any) break;
      asm volatile("":::"memory");
      sum_tile(pv,c0,c1); } };
  u32x4 sv[3];
  u32x4 pv2[8];
  ld_tile(tl[0],pv0,0u);
  ld8_sc1(pollv,rw,pollvo(8),pollso(8));
  ld_tile(tl[1],pv1,skip1);
  if constexpr(ROLE==0) ld_tile(tl[2],pv2,0u);
  poll_check<ROLE==0?16:8>(pollv,tag,rw,pollvo(8),pollso(8));
  #pragma unroll
  for(int i=0;i<NT;i++) sv[i]=__builtin_amdgcn_raw_buffer_load_b128(rsw,l16,(WS_SB+(tl[i]*64+rp)*1024)|(i==1?skip1:0u),16);
  SB();
  PAD(1000);
  f32x4 c0,c1,c2,c3,c4,c5;
  if constexpr(ROLE==1){ sum_fix(tl[0],pv0,c0,c1); sum_fix(tl[1],pv1,c2,c3); }
  else { sum_tile(pv0,c0,c1); sum_tile(pv1,c2,c3); }
  fin_tile(tl[0],c0,c1,sv[0],xv[0],0u);
  fin_tile(tl[1],c2,c3,sv[1],xv[1],skip1);
  STS(6);
  if constexpr(ROLE==0){ sum_fix(tl[2],pv2,c4,c5); fin_tile(tl[2],c4,c5,sv[2],xv[2],0u); }
  if constexpr(ROLE==0){ if(blockIdx.x==0&&tid==0) *(unsigned*)(ws+WS_EP)=e0+1u; }
}

extern "C" __global__ __launch_bounds__(256,1) void k1_main(const u16* __restrict__ x,const u16* __restrict__ wqkv,const u16* __restrict__ wo,const u16* __restrict__ wg,const u16* __restrict__ wu,const u16* __restrict__ wd,u16* __restrict__ out,char* __restrict__ ws
  ){
  u64* dbg=nullptr;
  __shared__ __attribute__((aligned(16))) char smem[6*SLOT+2048];
  const unsigned lds0=(unsigned)(size_t)(lds_vp)smem;
  const int xs=blockIdx.x&7, idx=blockIdx.x>>3, mt=idx&7, j=idx>>3;
  u64 t_start=0;
  const int lane=threadIdx.x&63, w=__builtin_amdgcn_readfirstlane(threadIdx.x>>6);
  const i32x4 rw=mk_rsrc(ws,WS_BYTES), rx=mk_rsrc(x,S*D*2);
  const int l32=lane&31, rr=2*w+(lane>>5), m=(xs&1)*8+rr;
  const unsigned xo=((mt*4+j)*64+4*(m&~1)+(xs&6)+(m&1))*(D*2);
  const bool has3=l32<8;
  u32x4 v[3]; unsigned pv_,ep;
  ld16_plain(v[0],rx,xo+l32*16,0u); ld16_plain(v[1],rx,xo+l32*16,512u); ld16_plain(v[2],rx,has3?xo+l32*16:0x80000000u,1024u);
  ld4_plain(pv_,rx,xo,0u); ld4_plain(ep,rw,0u,WS_EP);
  SB();
  i32x4 rc;
  { unsigned long long pc; int r0_,r1_;
    asm volatile("s_getpc_b64 %0\n\ts_mov_b32 %1, k1_main@rel32@lo+4\n\ts_mov_b32 %2, k_end@rel32@lo+12":"=s"(pc),"=s"(r0_),"=s"(r1_));
    rc=mk_rsrc((const void*)(pc+(long long)r0_), r1_>r0_? (unsigned)(r1_-r0_):0u); }
  touch(rc,(w>=2&&lane<32)?lane*128:0x80000000u,w==3?idx*4096:0u,lds0+6*SLOT);
  {
    const unsigned ldm=lds0+6*SLOT, big=0x80000000u, lw=(w==3)?lane:64;
    const unsigned va=(lw<PROWS&&j<3)? (mt*PROWS+lw)*(F*2) : big;
    touch(mk_rsrc(wg,D*F*2),va,(xs*3+j)*384,ldm); touch(mk_rsrc(wu,D*F*2),va,(xs*3+j)*384,ldm);
    const unsigned vq=lw<64? ((lw>>2)*32+idx)*(3*D*2)+((lw>>1)&1)*1152+(lw&1)*140 : big;
    touch(mk_rsrc(wqkv,3*D*D*2),vq,1152+xs*144,ldm);
  }
  waitvm<4>();
  pin(v[0]); pin(v[1]); pin(v[2]); pin(pv_); pin(ep);
  const unsigned e0=__builtin_amdgcn_readfirstlane(ep);
  {
    const float piv=bflo(pv_);
    float f[24]; float s_=0,q_=0;
    #pragma unroll
    for(int i=0;i<3;i++){ float s1=0,q1=0;
      #pragma unroll
      for(int jj=0;jj<4;jj++){ float a_=bflo(v[i][jj])-piv,b_=bfhi(v[i][jj])-piv; f[i*8+2*jj]=a_; f[i*8+2*jj+1]=b_; s1+=a_+b_; q1=fmaf(a_,a_,fmaf(b_,b_,q1)); }
      if(i<2||has3){ s_+=s1; q_+=q1; } }
    s_=red32(s_); q_=red32(q_);
    const float mean=s_*(1.0f/D), var=fmaxf(q_*(1.0f/D)-mean*mean,0.f);
    auto rb16=[](float t){ return bflo(pack2(t,0.f)); };
    const float mb=rb16(mean+piv), sd=rb16(sqrtf(rb16(rb16(var)+1e-5f)));
    const float sc=LOG2E/sd, of=(piv-mb)*sc;
    typedef __attribute__((address_space(3))) u32x4* lds_u32x4_p;
    const unsigned stg=lds0+5*SLOT;
    #pragma unroll
    for(int i=0;i<3;i++){ u32x4 o;
      #pragma unroll
      for(int jj=0;jj<4;jj++) o[jj]=pack2(fmaf(f[i*8+2*jj],sc,of),fmaf(f[i*8+2*jj+1],sc,of));
      *(lds_u32x4_p)(size_t)(stg+(l32+32*i)*128+rr*16)=o; }
    asm volatile("s_waitcnt lgkmcnt(0)":::"memory");
    __builtin_amdgcn_s_barrier();
    const unsigned vo=(lane>>5)*4096+((lane>>3)&3)*256+(lane&7)*16;
    const unsigned so=w<3? WS_XN+(mt*4+j)*18*4096+(xs>>1)*1024+(xs&1)*128+w*3*8192 : 0x80000000u;
    u32x4 o[3];
    #pragma unroll
    for(int i=0;i<3;i++) o[i]=*(lds_u32x4_p)(size_t)(stg+(w*3+i)*1024+lane*16);
    #pragma unroll
    for(int i=0;i<3;i++) st16_sc1<0>(o[i],rw,vo,so+i*8192);
  }
  {
    const unsigned ldm=lds0+6*SLOT, big=0x80000000u, lw=(w==3)?lane:64;
    const unsigned va=(lw<R2ROWS&&j<3)? (8*PROWS+mt*R2ROWS+lw)*(F*2) : big;
    touch(mk_rsrc(wg,D*F*2),va,(xs*3+j)*384,ldm); touch(mk_rsrc(wu,D*F*2),va,(xs*3+j)*384,ldm);
  }
  { waitvm<2>(); u32x2 tv={e0+1u,e0*0x9E3779B1u+0x7F4A7C15u};
    st8_sc1(tv,rw,lane==0?0u:0x40000000u,w<3? WS_XF+(mt*4+j)*256+(xs*3+w)*8 : 0x80000000u); }
  if(j<3) body<0>(x,wg,wu,wd,wo,out,ws,lds0,mt,xs,j,e0,rc,t_start,dbg);
  else body<1>(x,wqkv,wo,wd,wo,out,ws,lds0,mt,xs,j,e0,rc,t_start,dbg);
}
extern "C" __global__ void k_end(){}

extern "C" void kernel_launch(void* const* d_in,const int* in_sizes,int n_in,void* d_out,int out_size,void* d_ws,size_t ws_size,hipStream_t stream){
  const u16* x=(const u16*)d_in[0]; const u16* wqkv=(const u16*)d_in[1]; const u16* wo=(const u16*)d_in[2];
  const u16* wg=(const u16*)d_in[4]; const u16* wu=(const u16*)d_in[5]; const u16* wd=(const u16*)d_in[6];
  char* ws=(char*)d_ws;
  k1_main<<<256,256,0,stream>>>(x,wqkv,wo,wg,wu,wd,(u16*)d_out,ws
  );
}
```
